```python
import torch

def _softmax(x, axis=-1):
    return torch.softmax(x, dim=axis)

def _gelu(x):
    return torch.nn.functional.gelu(x, approximate='tanh')

def _silu(x):
    return torch.nn.functional.silu(x)

def _sigmoid(x):
    return torch.sigmoid(x)

def _layernorm(x, axis=-1, eps=1e-05):
    m = x.mean(dim=axis, keepdim=True)
    v = x.var(dim=axis, keepdim=True, unbiased=False)
    return (x - m) / torch.sqrt(v + eps)

def _scatter_add(tgt, idx, src, axis=0):
    return tgt.clone().index_add_(axis, idx, src)

def _segment_sum(src, seg, n):
    out = torch.zeros((n,) + src.shape[1:], dtype=src.dtype)
    return out.index_add_(0, seg, src)

def _pad(x, pw):
    flat = tuple((v for pair in reversed(pw) for v in pair))
    return torch.nn.functional.pad(x, flat)
kernel_name = 'adaLN_cond__grouped_kv_mqa__geglu_ffn__prompt'

def setup_inputs():

    _w = lambda a: (lambda t: t.to(torch.bfloat16) if t.is_floating_point() else t)(torch.as_tensor(a))

    def _A_setup():
        B, S, D, H = (1, 2048, 1024, 32)
        Dh = D // H
        return {'x': _w((lambda *s: torch.randn(*s, dtype=torch.bfloat16) * 0.05)(B, S, D)), 'wq': _w((lambda *s: torch.randn(*s, dtype=torch.bfloat16) * 0.05)(D, H * Dh)), 'wk': _w((lambda *s: torch.randn(*s, dtype=torch.bfloat16) * 0.05)(D, Dh)), 'wv': _w((lambda *s: torch.randn(*s, dtype=torch.bfloat16) * 0.05)(D, Dh)), 'wo': _w((lambda *s: torch.randn(*s, dtype=torch.bfloat16) * 0.05)(H * Dh, D))}
    _A_in = _A_setup()

    def _B_setup():
        B, S, D, F = (1, 2048, 1024, 3584)
        return {'x': _w((lambda *s: torch.randn(*s, dtype=torch.bfloat16) * 0.05)(B, S, D)), 'w_gate': _w((lambda *s: torch.randn(*s, dtype=torch.bfloat16) * 0.05)(D, F)), 'w_up': _w((lambda *s: torch.randn(*s, dtype=torch.bfloat16) * 0.05)(D, F)), 'w_down': _w((lambda *s: torch.randn(*s, dtype=torch.bfloat16) * 0.05)(F, D))}
    _B_in = _B_setup()
    return {'A_x': _A_in['x'], 'A_wq': _A_in['wq'], 'A_wk': _A_in['wk'], 'A_wv': _A_in['wv'], 'A_wo': _A_in['wo'], 'B_x': _B_in['x'], 'B_w_gate': _B_in['w_gate'], 'B_w_up': _B_in['w_up'], 'B_w_down': _B_in['w_down'], 'c': _w((lambda *s: torch.randn(*s, dtype=torch.bfloat16) * 0.1)(1, 1024) * 8.0), 'w_mod': _w((lambda *s: torch.randn(*s, dtype=torch.bfloat16) * 0.1)(1024, 6 * 1024) * 1024 ** (-0.5) * 8.0)}

def reference(A_x, A_wq, A_wk, A_wv, A_wo, B_x, B_w_gate, B_w_up, B_w_down, c, w_mod):

    def _A_ref(x, wq, wk, wv, wo):
        B, S, D = x.shape
        H = 32
        Dh = D // H
        q = (lambda x, *d: x.permute(*d))((x @ wq).reshape(B, S, H, Dh), 0, 2, 1, 3)
        k = x @ wk
        v = x @ wv
        scores = torch.einsum('bhqd,bkd->bhqk', q, k) * Dh ** (-0.5)
        p = _softmax(scores, axis=-1)
        o = torch.einsum('bhqk,bkd->bhqd', p, v)
        return (lambda x, *d: x.permute(*d))(o, 0, 2, 1, 3).reshape(B, S, H * Dh) @ wo

    def _B_ref(x, w_gate, w_up, w_down):
        return _gelu(x @ w_gate) * (x @ w_up) @ w_down
    x = A_x
    mod = (_silu(c) @ w_mod).reshape(c.shape[0], 6, 1, -1)
    sa, ca, ga, sb, cb, gb = (mod[:, 0].clone(), mod[:, 1].clone(), mod[:, 2].clone(), mod[:, 3].clone(), mod[:, 4].clone(), mod[:, 5].clone())
    h = _layernorm(x, axis=-1) * (1 + ca) + sa
    x = x + ga * _A_ref(h, A_wq, A_wk, A_wv, A_wo)
    h = _layernorm(x, axis=-1) * (1 + cb) + sb
    x = x + gb * _B_ref(h, B_w_gate, B_w_up, B_w_down)
    return x
```

```cpp
#include <hip/hip_runtime.h>
#include <hip/hip_bf16.h>

typedef short bf16x8 __attribute__((ext_vector_type(8)));
typedef short s16x4 __attribute__((ext_vector_type(4)));
typedef float f32x4 __attribute__((ext_vector_type(4)));
typedef float f32x16 __attribute__((ext_vector_type(16)));
typedef unsigned u32x4 __attribute__((ext_vector_type(4)));
typedef unsigned u32x2 __attribute__((ext_vector_type(2)));
typedef unsigned short u16;
#define LAS __attribute__((address_space(3)))
#define DI __device__ __forceinline__

constexpr int S_ = 2048, D_ = 1024, F_ = 3584;

constexpr size_t WS_MOD = 0;
constexpr size_t WS_KV  = 64 * 1024;
constexpr size_t WS_H   = 1u << 20;
constexpr size_t WS_O   = WS_H + (4u << 20);
constexpr size_t WS_X1  = WS_O + (4u << 20);
constexpr size_t WS_H2  = WS_X1 + (4u << 20);
constexpr size_t WS_ACT = WS_H2 + (4u << 20);

typedef float f32x2 __attribute__((ext_vector_type(2)));
typedef __bf16 bf16x2_t __attribute__((ext_vector_type(2)));
DI unsigned cvtpk(float lo, float hi) { const f32x2 v = {lo, hi}; return __builtin_bit_cast(unsigned, __builtin_convertvector(v, bf16x2_t)); }
DI float bflo(unsigned u) { return __uint_as_float(u << 16); }
DI float bfhi(unsigned u) { return __uint_as_float(u & 0xffff0000u); }
DI float bfr(float x) { return bflo(cvtpk(x, x)); }
#define MFMA32(a, b, c) __builtin_amdgcn_mfma_f32_32x32x16_bf16((a), (b), (c), 0, 0, 0)
#define MFMA16(a, b, c) __builtin_amdgcn_mfma_f32_16x16x32_bf16((a), (b), (c), 0, 0, 0)
#define WAITV(n) asm volatile("s_waitcnt vmcnt(%0)" ::"i"(n) : "memory")
#define WAITL0() asm volatile("s_waitcnt lgkmcnt(0)" ::: "memory")
#define BAR() do { asm volatile("" ::: "memory"); __builtin_amdgcn_s_barrier(); asm volatile("" ::: "memory"); } while (0)

DI void glds16(const void* sbase, unsigned voff, unsigned lds) {
    asm volatile("s_mov_b32 m0, %2\n\ts_nop 0\n\tglobal_load_lds_dwordx4 %0, %1" ::"v"(voff), "s"(sbase), "s"(lds) : "memory");
}
DI bf16x8 lds128(unsigned a) { return *(const LAS bf16x8*)(size_t)a; }
DI s16x4 ldstr(unsigned a) { return __builtin_amdgcn_ds_read_tr16_b64_v4i16((LAS s16x4*)(size_t)a); }
DI bf16x8 ldstr2(unsigned a0, unsigned a1) { s16x4 lo = ldstr(a0), hi = ldstr(a1); return __builtin_shufflevector(lo, hi, 0, 1, 2, 3, 4, 5, 6, 7); }
DI bf16x8 asb(u32x4 v) { return __builtin_bit_cast(bf16x8, v); }
DI float swapadd(float v) { auto r = __builtin_amdgcn_permlane32_swap(__float_as_uint(v), __float_as_uint(v), false, false); return __uint_as_float(r[0]) + __uint_as_float(r[1]); }
DI float swapmax(float v) { auto r = __builtin_amdgcn_permlane32_swap(__float_as_uint(v), __float_as_uint(v), false, false); return fmaxf(__uint_as_float(r[0]), __uint_as_float(r[1])); }

__global__ __launch_bounds__(256) void k_mod(const u16* __restrict__ c, const u16* __restrict__ wmod, u16* __restrict__ mod) {
    __shared__ float red[4][32];
    const int tid = threadIdx.x, lane = tid & 63, wid = tid >> 6;
    const int b = blockIdx.x;
    const int cg = (b & 7) * 24 + (b >> 3);
    const int rg = tid >> 2, cq = tid & 3;
    const u16* wp = wmod + (size_t)rg * 6144 + cg * 32 + cq * 8;
    u32x4 w[16];
#pragma unroll
    for (int i = 0; i < 16; ++i) w[i] = *(const u32x4*)(wp + (size_t)i * 64 * 6144);
    float acc[8];
#pragma unroll
    for (int j = 0; j < 8; ++j) acc[j] = 0.f;
#pragma unroll
    for (int i = 0; i < 16; ++i) {
        const float cv = bflo(c[rg + 64 * i]);
        const float s = bfr(cv / (1.f + __expf(-cv)));
#pragma unroll
        for (int j = 0; j < 4; ++j) { acc[2 * j] += s * bflo(w[i][j]); acc[2 * j + 1] += s * bfhi(w[i][j]); }
    }
#pragma unroll
    for (int j = 0; j < 8; ++j) {
        float v = acc[j];
        v += __shfl_xor(v, 4); v += __shfl_xor(v, 8); v += __shfl_xor(v, 16); v += __shfl_xor(v, 32);
        acc[j] = v;
    }
    if (lane < 4) {
#pragma unroll
        for (int j = 0; j < 8; ++j) red[wid][lane * 8 + j] = acc[j];
    }
    __syncthreads();
    if (tid < 32) {
        float v = bfr(red[0][tid] + red[1][tid] + red[2][tid] + red[3][tid]);
        const int col = cg * 32 + tid, seg = col >> 10;
        if (seg == 1 || seg == 4) v = 1.f + v;
        mod[col] = (u16)(cvtpk(v, v) & 0xffffu);
    }
}

__global__ __launch_bounds__(512) void k_ln1_kv(const u16* __restrict__ x, const u16* __restrict__ wk, const u16* __restrict__ wv,
                                                const u16* __restrict__ mod, char* __restrict__ himg, char* __restrict__ kvimg) {
    extern __shared__ __attribute__((aligned(16))) char smem[];
    const unsigned lds0 = (unsigned)(size_t)(LAS char*)smem;
    const int tid = threadIdx.x, lane = tid & 63, wid = __builtin_amdgcn_readfirstlane(tid >> 6);
    const int isv = (blockIdx.x >> 3) & 1, tb = (blockIdx.x & 7) + 8 * (blockIdx.x >> 4);
    const int m = lane & 31, hi = lane >> 5;
    const u16* wsrc = isv ? wv : wk;
#pragma unroll
    for (int i = 0; i < 8; ++i) glds16((const char*)wsrc + wid * 8192 + i * 1024, lane * 16, lds0 + wid * 8192 + i * 1024);
    u32x4 xv[8], cv[8], sv[8];
    const u16* xp = x + (size_t)(tb * 32 + m) * D_ + wid * 128 + hi * 8;
#pragma unroll
    for (int i = 0; i < 8; ++i) xv[i] = *(const u32x4*)(xp + i * 16);
#pragma unroll
    for (int i = 0; i < 8; ++i) { sv[i] = *(const u32x4*)(mod + wid * 128 + hi * 8 + i * 16); cv[i] = *(const u32x4*)(mod + 1024 + wid * 128 + hi * 8 + i * 16); }
    float s = 0.f;
#pragma unroll
    for (int i = 0; i < 8; ++i)
#pragma unroll
        for (int j = 0; j < 4; ++j) s += bflo(xv[i][j]) + bfhi(xv[i][j]);
    s = swapadd(s);
    const float mw = s * (1.f / 128.f);
    float q = 0.f;
#pragma unroll
    for (int i = 0; i < 8; ++i)
#pragma unroll
        for (int j = 0; j < 4; ++j) { const float a = bflo(xv[i][j]) - mw, b = bfhi(xv[i][j]) - mw; q += a * a + b * b; }
    q = swapadd(q);
    LAS float* st = (LAS float*)(size_t)(lds0 + 65536);
    if (hi == 0) { st[(wid * 32 + m) * 2] = mw; st[(wid * 32 + m) * 2 + 1] = q; }
    WAITL0(); BAR();
    float mean = 0.f, mws[8], m2 = 0.f;
#pragma unroll
    for (int w = 0; w < 8; ++w) { mws[w] = st[(w * 32 + m) * 2]; m2 += st[(w * 32 + m) * 2 + 1]; mean += mws[w]; }
    mean *= 0.125f;
#pragma unroll
    for (int w = 0; w < 8; ++w) { const float d = mws[w] - mean; m2 += 128.f * d * d; }
    const float rstd = rsqrtf(m2 * (1.f / 1024.f) + 1e-5f);
    u32x4 hv[8];
#pragma unroll
    for (int i = 0; i < 8; ++i)
#pragma unroll
        for (int j = 0; j < 4; ++j) {
            const float a = (bflo(xv[i][j]) - mean) * rstd * bflo(cv[i][j]) + bflo(sv[i][j]);
            const float b = (bfhi(xv[i][j]) - mean) * rstd * bfhi(cv[i][j]) + bfhi(sv[i][j]);
            hv[i][j] = cvtpk(a, b);
        }
    {
        char* hp = himg + ((size_t)tb * 64 + wid * 8) * 1024 + lane * 16;
        if (isv) {
#pragma unroll
            for (int i = 4; i < 8; ++i) *(u32x4*)(hp + i * 1024) = hv[i];
        } else {
#pragma unroll
            for (int i = 0; i < 4; ++i) *(u32x4*)(hp + i * 1024) = hv[i];
        }
    }
    WAITV(0);
    f32x16 acc = {};
    const unsigned wa = lds0 + wid * 8192 + (8 * hi + ((lane & 15) >> 2)) * 64 + ((lane >> 4) & 1) * 32 + (lane & 3) * 8;
#pragma unroll
    for (int i = 0; i < 8; ++i) {
        const bf16x8 wf = ldstr2(wa + i * 1024, wa + i * 1024 + 256);
        if (isv) acc = MFMA32(asb(hv[i]), wf, acc); else acc = MFMA32(wf, asb(hv[i]), acc);
    }
    LAS float* rp = (LAS float*)(size_t)(lds0 + wid * 8192);
#pragma unroll
    for (int r = 0; r < 16; ++r) rp[r * 64 + lane] = acc[r];
    WAITL0(); BAR();
    {
        const int r0 = 2 * wid;
        float a = 0.f, b = 0.f;
#pragma unroll
        for (int w = 0; w < 8; ++w) { const LAS float* pp = (const LAS float*)(size_t)(lds0 + w * 8192); a += pp[r0 * 64 + lane]; b += pp[(r0 + 1) * 64 + lane]; }
        *(unsigned*)(kvimg + (size_t)tb * 4096 + isv * 2048 + (wid >> 2) * 1024 + lane * 16 + (wid & 3) * 4) = cvtpk(a, b);
    }
}

template <int NRB, int NCB, int ORDER, int NSLOT>
DI f32x16 tgemm(unsigned lds0, const char* ximg  , size_t xrb_stride, const char* w  , unsigned ldw_bytes,
                int nstage, int wid, int lane) {
    static_assert(NRB * NCB == 8, "8 waves");
    constexpr unsigned SLOT = 24576, WOFF = NRB * 4096;
    constexpr int CPR = NCB * 4, RPP = 64 / CPR;
    constexpr unsigned PITCH = NCB * 64;
    const char* xs0; const char* xs1 = nullptr; unsigned wv0, wv1 = 0;
    auto wvoff = [&](int piece) { const int r = piece * RPP + lane / CPR, cp = lane % CPR;
        const int sw = (NCB == 2) ? ((r >> 1) & 1) : (r & 3); return (unsigned)r * ldw_bytes + (unsigned)(((cp >> 2) ^ sw) * 64 + (cp & 3) * 16); };
    xs0 = ximg + (size_t)(wid >> 2) * xrb_stride + (wid & 3) * 1024;
    if (NRB == 4) { xs1 = xs0 + 2 * xrb_stride; wv0 = wvoff(wid); } else { wv0 = wvoff(wid); wv1 = wvoff(wid + 8); }
    const unsigned xvo = lane * 16;
    auto stage = [&](int st, int slot) {
        const unsigned lb = lds0 + slot * SLOT;
        const char* wb = w + (size_t)st * 64 * ldw_bytes;
        glds16(xs0 + (size_t)st * 4096, xvo, lb + wid * 1024);
        if (NRB == 4) { glds16(xs1 + (size_t)st * 4096, xvo, lb + 8192 + wid * 1024); glds16(wb, wv0, lb + WOFF + wid * 1024); }
        else { glds16(wb, wv0, lb + WOFF + wid * 1024); glds16(wb, wv1, lb + WOFF + 8192 + wid * 1024); }
    };
    const int rb = (NRB == 4) ? (wid >> 1) : (wid & 1), cb = (NRB == 4) ? (wid & 1) : (wid >> 1);
    const int hi = lane >> 5, qq = (lane & 15) >> 2;
    const unsigned xa = rb * 4096 + lane * 16;
    const int rl = ((ORDER == 0) ? 8 * hi : 4 * hi) + qq;
    const int sw = (NCB == 2) ? ((qq >> 1) & 1) : qq;
    const unsigned wa = WOFF + rl * PITCH + ((cb ^ sw) * 64) + ((lane >> 4) & 1) * 32 + (lane & 3) * 8;
    f32x16 acc = {};
#pragma unroll
    for (int i = 0; i < NSLOT - 1; ++i) stage(i, i);
    int slot = 0, fill = NSLOT - 1;
    for (int st = 0; st < nstage; ++st) {
        if (st + NSLOT - 1 <= nstage) WAITV(3 * (NSLOT - 2)); else WAITV(0);
        BAR();
        if (st + NSLOT - 1 < nstage) stage(st + NSLOT - 1, fill);
        const unsigned lb = lds0 + slot * SLOT;
        bf16x8 xf[4], wf[4];
#pragma unroll
        for (int s = 0; s < 4; ++s) {
            const int r0 = (ORDER == 2) ? (32 * (s >> 1) + 8 * (s & 1)) : 16 * s, dt = (ORDER == 0) ? 4 : (ORDER == 1) ? 8 : 16;
            xf[s] = lds128(lb + xa + s * 1024);
            wf[s] = ldstr2(lb + wa + r0 * PITCH, lb + wa + (r0 + dt) * PITCH);
        }
#pragma unroll
        for (int s = 0; s < 4; ++s) acc = MFMA32(wf[s], xf[s], acc);
        WAITL0();
        fill = slot; slot = (slot == NSLOT - 1) ? 0 : slot + 1;
    }
    return acc;
}

constexpr int K3_LDS = 5 * 24576 + 32768;
__global__ __launch_bounds__(512) void k_attn(const char* __restrict__ himg, const u16* __restrict__ wq, const char* __restrict__ kvimg, char* __restrict__ oimg) {
    extern __shared__ __attribute__((aligned(16))) char smem[];
    const unsigned lds0 = (unsigned)(size_t)(LAS char*)smem;
    const int tid = threadIdx.x, lane = tid & 63, wid = __builtin_amdgcn_readfirstlane(tid >> 6);
    const int xcd = blockIdx.x & 7, idx = blockIdx.x >> 3;
    const int tg = (xcd >> 1) * 8 + (idx & 7), hg = (xcd & 1) * 4 + (idx >> 3);
    auto kvslot = [&](int c) { const int s = c & 3; return lds0 + (s == 0 ? 5 * 24576u : (unsigned)(s - 1) * 32768u); };
    auto kvstage = [&](int c) {
#pragma unroll
        for (int i = 0; i < 4; ++i) glds16(kvimg + (size_t)c * 32768 + (wid * 4 + i) * 1024, lane * 16, kvslot(c) + (wid * 4 + i) * 1024);
    };
    kvstage(0);
    f32x16 qa = tgemm<2, 4, 0, 5>(lds0, himg + (size_t)tg * 2 * 65536, 65536, (const char*)(wq + hg * 128), 2048, 16, wid, lane);
    BAR();
    kvstage(1); kvstage(2); kvstage(3);
    constexpr float QS = 0.17677669529663687f * 1.4426950408889634f;
    u32x4 q0, q1;
#pragma unroll
    for (int j = 0; j < 4; ++j) { q0[j] = cvtpk(qa[2 * j] * QS, qa[2 * j + 1] * QS); q1[j] = cvtpk(qa[8 + 2 * j] * QS, qa[9 + 2 * j] * QS); }
    const bf16x8 qf0 = asb(q0), qf1 = asb(q1);
    bf16x8 sel;
    { const int i = lane & 15, g = lane >> 4; const short v = ((i & 3) == 0 && ((i >> 2) & 1) == (g & 1)) ? (short)0x3f80 : (short)0;
#pragma unroll
      for (int j = 0; j < 8; ++j) sel[j] = v; }
    f32x16 negm;
    {
        const unsigned a = kvslot(0) + lane * 16;
        f32x16 s = {};
        s = MFMA32(lds128(a), qf0, s); s = MFMA32(lds128(a + 1024), qf1, s);
        float mx = s[0];
#pragma unroll
        for (int r = 1; r < 16; ++r) mx = fmaxf(mx, s[r]);
        mx = swapmax(mx);
#pragma unroll
        for (int r = 0; r < 16; ++r) negm[r] = -mx;
    }
    f32x16 o = {}; f32x4 l = {};
    for (int c = 0; c < 8; ++c) {
        if (c > 0) {
            if (c <= 5) WAITV(8); else if (c == 6) WAITV(4); else WAITV(0);
            WAITL0(); BAR();
            if (c + 3 < 8) kvstage(c + 3);
        }
        const unsigned a = kvslot(c) + lane * 16;
#pragma unroll
        for (int t = 0; t < 8; ++t) {
            const bf16x8 k0 = lds128(a + t * 4096), k1 = lds128(a + t * 4096 + 1024), v0 = lds128(a + t * 4096 + 2048), v1 = lds128(a + t * 4096 + 3072);
            f32x16 s = MFMA32(k0, qf0, negm); s = MFMA32(k1, qf1, s);
            u32x4 p0, p1;
#pragma unroll
            for (int j = 0; j < 4; ++j) {
                p0[j] = cvtpk(__builtin_amdgcn_exp2f(s[2 * j]), __builtin_amdgcn_exp2f(s[2 * j + 1]));
                p1[j] = cvtpk(__builtin_amdgcn_exp2f(s[8 + 2 * j]), __builtin_amdgcn_exp2f(s[9 + 2 * j]));
            }
            o = MFMA32(v0, asb(p0), o); o = MFMA32(v1, asb(p1), o);
            l = MFMA16(sel, asb(p0), l); l = MFMA16(sel, asb(p1), l);
        }
    }
    {
        bool bad = !(l[0] < 1.0e30f);
#pragma unroll
        for (int r = 0; r < 16; ++r) bad |= !(fabsf(o[r]) < 3.0e38f);
        if (__any(bad)) {
            const char* kb = kvimg + lane * 16;
            float mx = -3.0e38f;
            for (int t = 0; t < 64; ++t) {
                f32x16 s = {};
                s = MFMA32(*(const bf16x8*)(kb + t * 4096), qf0, s); s = MFMA32(*(const bf16x8*)(kb + t * 4096 + 1024), qf1, s);
#pragma unroll
                for (int r = 0; r < 16; ++r) mx = fmaxf(mx, s[r]);
            }
            mx = swapmax(mx);
#pragma unroll
            for (int r = 0; r < 16; ++r) { negm[r] = -mx; o[r] = 0.f; }
            l = f32x4{};
            for (int t = 0; t < 64; ++t) {
                f32x16 s = MFMA32(*(const bf16x8*)(kb + t * 4096), qf0, negm); s = MFMA32(*(const bf16x8*)(kb + t * 4096 + 1024), qf1, s);
                u32x4 p0, p1;
#pragma unroll
                for (int j = 0; j < 4; ++j) {
                    p0[j] = cvtpk(__builtin_amdgcn_exp2f(s[2 * j]), __builtin_amdgcn_exp2f(s[2 * j + 1]));
                    p1[j] = cvtpk(__builtin_amdgcn_exp2f(s[8 + 2 * j]), __builtin_amdgcn_exp2f(s[9 + 2 * j]));
                }
                o = MFMA32(*(const bf16x8*)(kb + t * 4096 + 2048), asb(p0), o); o = MFMA32(*(const bf16x8*)(kb + t * 4096 + 3072), asb(p1), o);
                l = MFMA16(sel, asb(p0), l); l = MFMA16(sel, asb(p1), l);
            }
        }
    }
    const float rl = 1.0f / l[0];
    u32x4 o0, o1;
#pragma unroll
    for (int j = 0; j < 4; ++j) { o0[j] = cvtpk(o[2 * j] * rl, o[2 * j + 1] * rl); o1[j] = cvtpk(o[8 + 2 * j] * rl, o[9 + 2 * j] * rl); }
    char* op = oimg + ((size_t)(tg * 2 + (wid & 1)) * 64 + (hg * 4 + (wid >> 1)) * 2) * 1024 + lane * 16;
    *(u32x4*)op = o0; *(u32x4*)(op + 1024) = o1;
}

DI size_t a16_addr(int row, int col32  , int s, int hi) { return ((size_t)(row >> 4) * 32 + col32) * 1024 + ((row & 15) + 16 * (2 * s + hi)) * 16; }

constexpr int TG_LDS = 6 * 24576;
__global__ __launch_bounds__(512) void k_oproj(const char* __restrict__ oimg, const u16* __restrict__ wo, const u16* __restrict__ x, const u16* __restrict__ mod,
                                               char* __restrict__ x1img) {
    extern __shared__ __attribute__((aligned(16))) char smem[];
    const unsigned lds0 = (unsigned)(size_t)(LAS char*)smem;
    const int tid = threadIdx.x, lane = tid & 63, wid = __builtin_amdgcn_readfirstlane(tid >> 6);
    const int xcd = blockIdx.x & 7, idx = blockIdx.x >> 3;
    const int mt = (xcd >> 1) * 4 + (idx & 3), nt = (xcd & 1) * 8 + (idx >> 2);
    f32x16 acc = tgemm<4, 2, 1, 6>(lds0, oimg + (size_t)mt * 4 * 65536, 65536, (const char*)(wo + nt * 64), 2048, 16, wid, lane);
    const int hi = lane >> 5, row = mt * 128 + (wid >> 1) * 32 + (lane & 31), cb = nt * 2 + (wid & 1), c0 = cb * 32 + 4 * hi;
#pragma unroll
    for (int s = 0; s < 2; ++s) {
        u32x4 ov;
#pragma unroll
        for (int g = 0; g < 2; ++g) {
            const int col = c0 + 16 * s + 8 * g;
            const u32x2 xv = *(const u32x2*)(x + (size_t)row * D_ + col), gv = *(const u32x2*)(mod + 2048 + col);
            const int r = 8 * s + 4 * g;
            ov[2 * g]     = cvtpk(bflo(xv[0]) + bflo(gv[0]) * acc[r],     bfhi(xv[0]) + bfhi(gv[0]) * acc[r + 1]);
            ov[2 * g + 1] = cvtpk(bflo(xv[1]) + bflo(gv[1]) * acc[r + 2], bfhi(xv[1]) + bfhi(gv[1]) * acc[r + 3]);
        }
        *(u32x4*)(x1img + a16_addr(row, cb, s, hi)) = ov;
    }
}

__global__ __launch_bounds__(256) void k_ln2(const char* __restrict__ x1img, const u16* __restrict__ mod, char* __restrict__ h2img) {
    __shared__ float st[4][16][2];
    const int tid = threadIdx.x, lane = tid & 63, wid = tid >> 6, m = lane & 15, g = lane >> 4;
    const size_t base = ((size_t)blockIdx.x * 32 + wid * 8) * 1024 + lane * 16;
    u32x4 xv[8];
#pragma unroll
    for (int i = 0; i < 8; ++i) xv[i] = *(const u32x4*)(x1img + base + i * 1024);
    u32x2 sv[8][2], cv[8][2];
#pragma unroll
    for (int i = 0; i < 8; ++i)
#pragma unroll
        for (int t = 0; t < 2; ++t) { const int col = (wid * 8 + i) * 32 + 16 * (g >> 1) + 8 * t + 4 * (g & 1);
            sv[i][t] = *(const u32x2*)(mod + 3072 + col); cv[i][t] = *(const u32x2*)(mod + 4096 + col); }
    float s = 0.f;
#pragma unroll
    for (int i = 0; i < 8; ++i)
#pragma unroll
        for (int j = 0; j < 4; ++j) s += bflo(xv[i][j]) + bfhi(xv[i][j]);
    s += __shfl_xor(s, 16); s += __shfl_xor(s, 32);
    const float mw = s * (1.f / 256.f);
    float q = 0.f;
#pragma unroll
    for (int i = 0; i < 8; ++i)
#pragma unroll
        for (int j = 0; j < 4; ++j) { const float a = bflo(xv[i][j]) - mw, b = bfhi(xv[i][j]) - mw; q += a * a + b * b; }
    q += __shfl_xor(q, 16); q += __shfl_xor(q, 32);
    if (g == 0) { st[wid][m][0] = mw; st[wid][m][1] = q; }
    __syncthreads();
    float mean = 0.25f * (st[0][m][0] + st[1][m][0] + st[2][m][0] + st[3][m][0]);
    float m2 = st[0][m][1] + st[1][m][1] + st[2][m][1] + st[3][m][1];
#pragma unroll
    for (int w = 0; w < 4; ++w) { const float d = st[w][m][0] - mean; m2 += 256.f * d * d; }
    const float rstd = rsqrtf(m2 * (1.f / 1024.f) + 1e-5f);
#pragma unroll
    for (int i = 0; i < 8; ++i) {
        u32x4 hv;
#pragma unroll
        for (int j = 0; j < 4; ++j) {
            const unsigned cc = cv[i][j >> 1][j & 1], ss = sv[i][j >> 1][j & 1];
            hv[j] = cvtpk((bflo(xv[i][j]) - mean) * rstd * bflo(cc) + bflo(ss), (bfhi(xv[i][j]) - mean) * rstd * bfhi(cc) + bfhi(ss));
        }
        *(u32x4*)(h2img + base + i * 1024) = hv;
    }
}

constexpr unsigned K5_G = 16384, K5_U = K5_G + 7168 + 16, K5_SLOT = K5_U + 7168;
constexpr int K5_NS = 5, K5_LDS = K5_NS * K5_SLOT;
DI float gelu_mul(float g, float u) {
    const float e = __builtin_amdgcn_exp2f(g * (-2.3022082f - 0.10294324f * g * g));
    return g * __builtin_amdgcn_rcpf(1.f + e) * u;
}
__global__ __launch_bounds__(512) void k_gateup(const char* __restrict__ h2img, const u16* __restrict__ wg, const u16* __restrict__ wu, char* __restrict__ actimg) {
    extern __shared__ __attribute__((aligned(16))) char smem[];
    const unsigned lds0 = (unsigned)(size_t)(LAS char*)smem;
    const int tid = threadIdx.x, lane = tid & 63, wid = __builtin_amdgcn_readfirstlane(tid >> 6);
    const int xcd = blockIdx.x & 7, idx = blockIdx.x >> 3;
    const int nt = xcd * 4 + (idx & 3), mt = idx >> 2;
    const int wm = wid >> 1, wc = wid & 1, odd = nt & 1;
    const char* xs = h2img + ((size_t)(mt * 16 + wid * 2) * 32) * 1024;
    const char* gs = (const char*)(wg + nt * 112); const char* us = (const char*)(wu + nt * 112);
    const int ci = wid * 64 + lane;
    const unsigned wvo = (unsigned)(ci / 14) * (F_ * 2) + (unsigned)(ci % 14) * 16, xvo = lane * 16;
    auto stage = [&](int st, int slot) {
        const unsigned lb = lds0 + slot * K5_SLOT;
        glds16(xs + (size_t)st * 1024, xvo, lb + wid * 2048);
        glds16(xs + (size_t)st * 1024 + 32768, xvo, lb + wid * 2048 + 1024);
        if (wid < 7) {
            glds16(gs + (size_t)st * 32 * F_ * 2, wvo, lb + K5_G + wid * 1024);
            glds16(us + (size_t)st * 32 * F_ * 2, wvo, lb + K5_U + wid * 1024);
        }
    };
    const int g = lane >> 4, qq = (lane & 15) >> 2, p = lane & 3;
    const unsigned xa = wm * 4096 + lane * 16;
    const unsigned rowb = (16 * (g >> 1) + 4 * (g & 1) + qq) * 224;
    const int e = odd ? 16 : 0;
    const unsigned wa01 = K5_G + rowb + (e + 32 * wc) * 2 + p * 8;
    const unsigned wa2 = K5_G + rowb + (e + 64 + 16 * wc) * 2 + p * 8;
    const unsigned wam = ((p < 2) ? K5_G : K5_U) + rowb + ((odd ? 0 : 96) + 8 * wc) * 2 + (p & 1) * 8;
    constexpr unsigned DU = K5_U - K5_G, DT = 8 * 224;
    f32x4 acc[7][4];
#pragma unroll
    for (int n = 0; n < 7; ++n)
#pragma unroll
        for (int m = 0; m < 4; ++m) acc[n][m] = f32x4{};
    bf16x8 xf[2][4], wf[2][7];
    auto readfrags = [&](int slot, bf16x8 (&X)[4], bf16x8 (&W)[7]) {
        const unsigned lb = lds0 + slot * K5_SLOT;
#pragma unroll
        for (int m = 0; m < 4; ++m) X[m] = lds128(lb + xa + m * 1024);
        W[0] = ldstr2(lb + wa01, lb + wa01 + DT);           W[1] = ldstr2(lb + wa01 + DU, lb + wa01 + DU + DT);
        W[2] = ldstr2(lb + wa01 + 32, lb + wa01 + 32 + DT); W[3] = ldstr2(lb + wa01 + DU + 32, lb + wa01 + DU + 32 + DT);
        W[4] = ldstr2(lb + wa2, lb + wa2 + DT);             W[5] = ldstr2(lb + wa2 + DU, lb + wa2 + DU + DT);
        W[6] = ldstr2(lb + wam, lb + wam + DT);
    };
    auto mma = [&](bf16x8 (&X)[4], bf16x8 (&W)[7]) {
#pragma unroll
        for (int n = 0; n < 7; ++n)
#pragma unroll
            for (int m = 0; m < 4; ++m) acc[n][m] = MFMA16(W[n], X[m], acc[n][m]);
    };
    auto waitstage = [&](int left) {
        if (wid < 7) { if (left >= 4) WAITV(16); else if (left == 3) WAITV(12); else if (left == 2) WAITV(8); else if (left == 1) WAITV(4); else WAITV(0); }
        else         { if (left >= 4) WAITV(8);  else if (left == 3) WAITV(6);  else if (left == 2) WAITV(4); else if (left == 1) WAITV(2); else WAITV(0); }
    };
#pragma unroll
    for (int i = 0; i < 5; ++i) stage(i, i);
    waitstage(4); BAR();
    readfrags(0, xf[0], wf[0]);
    int rs = 1, fs = 0;
    auto step = [&](int s, bf16x8 (&Xc)[4], bf16x8 (&Wc)[7], bf16x8 (&Xn)[4], bf16x8 (&Wn)[7]) {
        const int last = (s + 4 < 31) ? s + 4 : 31;
        WAITL0(); waitstage(last - (s + 1)); BAR();
        if (s + 5 < 32) stage(s + 5, fs);
        if (s + 1 < 32) readfrags(rs, Xn, Wn);
        mma(Xc, Wc);
        fs = rs; rs = (rs == K5_NS - 1) ? 0 : rs + 1;
    };
    for (int s = 0; s < 26; s += 2) { step(0 * s, xf[0], wf[0], xf[1], wf[1]); xs += 1024; gs += 32 * F_ * 2; us += 32 * F_ * 2;
                                      step(0 * s, xf[1], wf[1], xf[0], wf[0]); xs += 1024; gs += 32 * F_ * 2; us += 32 * F_ * 2; }
    xs -= 26 * 1024; gs -= (size_t)26 * 32 * F_ * 2; us -= (size_t)26 * 32 * F_ * 2;
#pragma unroll
    for (int s = 26; s < 32; s += 2) { step(s, xf[0], wf[0], xf[1], wf[1]); step(s + 1, xf[1], wf[1], xf[0], wf[0]); }
    const int m16 = lane & 15, q = lane >> 4;
    const int fb = (nt * 112 + e) >> 5;
    const int fsh = odd ? fb - 1 : fb + 3;
    char* ab = actimg + (size_t)(mt * 8 + wm * 2) * 224 * 1024;
#pragma unroll
    for (int m = 0; m < 4; ++m) {
        char* rbp = ab + (size_t)(m >> 1) * 224 * 1024 + (q >> 1) * 1024 + ((m & 1) * 16 + m16 + 32 * (q & 1)) * 16;
        u32x4 v;
        v[0] = cvtpk(gelu_mul(acc[0][m][0], acc[1][m][0]), gelu_mul(acc[0][m][1], acc[1][m][1]));
        v[1] = cvtpk(gelu_mul(acc[0][m][2], acc[1][m][2]), gelu_mul(acc[0][m][3], acc[1][m][3]));
        v[2] = cvtpk(gelu_mul(acc[2][m][0], acc[3][m][0]), gelu_mul(acc[2][m][1], acc[3][m][1]));
        v[3] = cvtpk(gelu_mul(acc[2][m][2], acc[3][m][2]), gelu_mul(acc[2][m][3], acc[3][m][3]));
        *(u32x4*)(rbp + (size_t)(fb + wc) * 2048) = v;
        u32x2 w;
        w[0] = cvtpk(gelu_mul(acc[4][m][0], acc[5][m][0]), gelu_mul(acc[4][m][1], acc[5][m][1]));
        w[1] = cvtpk(gelu_mul(acc[4][m][2], acc[5][m][2]), gelu_mul(acc[4][m][3], acc[5][m][3]));
        *(u32x2*)(rbp + (size_t)(fb + 2) * 2048 + wc * 8) = w;
    }
#pragma unroll
    for (int mp = 0; mp < 2; ++mp) {
        float a[4];
#pragma unroll
        for (int r = 0; r < 4; ++r) {
            auto sw = __builtin_amdgcn_permlane32_swap(__float_as_uint(acc[6][2 * mp][r]), __float_as_uint(acc[6][2 * mp + 1][r]), false, false);
            a[r] = gelu_mul(__uint_as_float(sw[0]), __uint_as_float(sw[1]));
        }
        u32x2 w; w[0] = cvtpk(a[0], a[1]); w[1] = cvtpk(a[2], a[3]);
        char* pp = ab + (size_t)mp * 224 * 1024 + (size_t)fsh * 2048 + wc * 1024 + ((q >> 1) * 16 + m16 + 32 * (q & 1)) * 16 + odd * 8;
        *(u32x2*)pp = w;
    }
}

__global__ __launch_bounds__(512) void k_down(const char* __restrict__ actimg, const u16* __restrict__ wd, const char* __restrict__ x1img, const u16* __restrict__ mod,
                                              u16* __restrict__ out) {
    extern __shared__ __attribute__((aligned(16))) char smem[];
    const unsigned lds0 = (unsigned)(size_t)(LAS char*)smem;
    const int tid = threadIdx.x, lane = tid & 63, wid = __builtin_amdgcn_readfirstlane(tid >> 6);
    const int xcd = blockIdx.x & 7, idx = blockIdx.x >> 3;
    const int mt = (xcd >> 1) * 4 + (idx & 3), nt = (xcd & 1) * 8 + (idx >> 2);
    f32x16 acc = tgemm<4, 2, 2, 6>(lds0, actimg + (size_t)mt * 4 * 224 * 1024, 224 * 1024, (const char*)(wd + nt * 64), 2048, 56, wid, lane);
    const int hi = lane >> 5, row = mt * 128 + (wid >> 1) * 32 + (lane & 31), cb = nt * 2 + (wid & 1), c0 = cb * 32 + 4 * hi;
#pragma unroll
    for (int s = 0; s < 2; ++s) {
        const u32x4 xv = *(const u32x4*)(x1img + a16_addr(row, cb, s, hi));
        u32x2 ov[2];
#pragma unroll
        for (int g = 0; g < 2; ++g) {
            const u32x2 gv = *(const u32x2*)(mod + 5120 + c0 + 16 * s + 8 * g);
            const int r = 8 * s + 4 * g;
            ov[g][0] = cvtpk(bflo(xv[2 * g]) + bflo(gv[0]) * acc[r],         bfhi(xv[2 * g]) + bfhi(gv[0]) * acc[r + 1]);
            ov[g][1] = cvtpk(bflo(xv[2 * g + 1]) + bflo(gv[1]) * acc[r + 2], bfhi(xv[2 * g + 1]) + bfhi(gv[1]) * acc[r + 3]);
        }
        auto r0 = __builtin_amdgcn_permlane32_swap(ov[0][0], ov[1][0], false, false);
        auto r1 = __builtin_amdgcn_permlane32_swap(ov[0][1], ov[1][1], false, false);
        u32x4 w; w[0] = r0[0]; w[1] = r1[0]; w[2] = r0[1]; w[3] = r1[1];
        *(u32x4*)(out + (size_t)row * D_ + cb * 32 + 16 * s + 8 * hi) = w;
    }
}

extern "C" void kernel_launch(void* const* d_in, const int* in_sizes, int n_in, void* d_out, int out_size, void* d_ws, size_t ws_size, hipStream_t stream) {
    const u16* x = (const u16*)d_in[0]; const u16* wq = (const u16*)d_in[1]; const u16* wk = (const u16*)d_in[2]; const u16* wv = (const u16*)d_in[3];
    const u16* wo = (const u16*)d_in[4]; const u16* wg = (const u16*)d_in[6]; const u16* wu = (const u16*)d_in[7]; const u16* wd = (const u16*)d_in[8];
    const u16* c = (const u16*)d_in[9]; const u16* wmod = (const u16*)d_in[10];
    char* ws = (char*)d_ws; u16* mod = (u16*)(ws + WS_MOD);
    (void)hipFuncSetAttribute((const void*)k_ln1_kv, hipFuncAttributeMaxDynamicSharedMemorySize, 65536 + 2048);
    (void)hipFuncSetAttribute((const void*)k_attn, hipFuncAttributeMaxDynamicSharedMemorySize, K3_LDS);
    (void)hipFuncSetAttribute((const void*)k_oproj, hipFuncAttributeMaxDynamicSharedMemorySize, TG_LDS);
    (void)hipFuncSetAttribute((const void*)k_gateup, hipFuncAttributeMaxDynamicSharedMemorySize, K5_LDS);
    (void)hipFuncSetAttribute((const void*)k_down, hipFuncAttributeMaxDynamicSharedMemorySize, TG_LDS);
    k_mod<<<192, 256, 0, stream>>>(c, wmod, mod);
    k_ln1_kv<<<128, 512, 65536 + 2048, stream>>>(x, wk, wv, mod, ws + WS_H, ws + WS_KV);
    k_attn<<<256, 512, K3_LDS, stream>>>(ws + WS_H, wq, ws + WS_KV, ws + WS_O);
    k_oproj<<<256, 512, TG_LDS, stream>>>(ws + WS_O, wo, x, mod, ws + WS_X1);
    k_ln2<<<128, 256, 0, stream>>>(ws + WS_X1, mod, ws + WS_H2);
    k_gateup<<<256, 512, K5_LDS, stream>>>(ws + WS_H2, wg, wu, ws + WS_ACT);
    k_down<<<256, 512, TG_LDS, stream>>>(ws + WS_ACT, wd, ws + WS_X1, mod, (u16*)d_out);
}
```

```cpp
#include <hip/hip_runtime.h>
#include <hip/hip_bf16.h>

typedef short bf16x8 __attribute__((ext_vector_type(8)));
typedef short s16x4 __attribute__((ext_vector_type(4)));
typedef float f32x2 __attribute__((ext_vector_type(2)));
typedef float f32x4 __attribute__((ext_vector_type(4)));
typedef float f32x16 __attribute__((ext_vector_type(16)));
typedef unsigned u32x4 __attribute__((ext_vector_type(4)));
typedef unsigned u32x2 __attribute__((ext_vector_type(2)));
typedef __bf16 bf16x2_t __attribute__((ext_vector_type(2)));
typedef unsigned short u16;
#define LAS __attribute__((address_space(3)))
#define GAS __attribute__((address_space(1)))
#define DI __device__ __forceinline__

constexpr int D_ = 1024, F_ = 3584;

#define TS(k, i)

constexpr size_t WS_MODP = 0;
constexpr size_t WS_MODQ = 32768;
constexpr size_t WS_MODF = 65536;
constexpr size_t WS_FLAG = 98304;
constexpr size_t WS_STAT = 131072;
constexpr size_t WS_UT   = 262144;
constexpr size_t WS_KV   = 524288;
constexpr size_t WS_N    = 1u << 20;
constexpr size_t WS_H    = WS_N + (4u << 20);
constexpr size_t WS_O    = WS_H + (4u << 20);
constexpr size_t WS_X1   = WS_O + (4u << 20);
constexpr size_t WS_Y    = WS_X1 + (4u << 20);
constexpr size_t WS_XB   = WS_Y + (4u << 20);
constexpr size_t WS_ACT  = WS_XB + (8u << 20);
constexpr size_t WS_WGU  = WS_ACT + (16u << 20);

DI unsigned cvtpk(float lo, float hi) { const f32x2 v = {lo, hi}; return __builtin_bit_cast(unsigned, __builtin_convertvector(v, bf16x2_t)); }
DI float bflo(unsigned u) { return __uint_as_float(u << 16); }
DI float bfhi(unsigned u) { return __uint_as_float(u & 0xffff0000u); }
DI float bfr(float x) { return bflo(cvtpk(x, x)); }
#define MFMA32(a, b, c) __builtin_amdgcn_mfma_f32_32x32x16_bf16((a), (b), (c), 0, 0, 0)
#define MFMA16(a, b, c) __builtin_amdgcn_mfma_f32_16x16x32_bf16((a), (b), (c), 0, 0, 0)
#define WAITV(n) asm volatile("s_waitcnt vmcnt(%0)" ::"i"(n) : "memory")
#define WAITL0() do { asm volatile("" ::: "memory"); __builtin_amdgcn_s_waitcnt(0xc07f); asm volatile("" ::: "memory"); } while (0)
#define SB0() __builtin_amdgcn_sched_barrier(0)
#define BAR() do { asm volatile("" ::: "memory"); __builtin_amdgcn_s_barrier(); asm volatile("" ::: "memory"); } while (0)

DI void glds16(const void* sbase, unsigned voff, unsigned lds) {
    asm volatile("s_mov_b32 m0, %2\n\ts_nop 0\n\tglobal_load_lds_dwordx4 %0, %1" ::"v"(voff), "s"(sbase), "s"(lds) : "memory");
}
DI void glds4(const void* sbase, unsigned voff, unsigned lds) {
    asm volatile("s_mov_b32 m0, %2\n\ts_nop 0\n\tglobal_load_lds_dword %0, %1" ::"v"(voff), "s"(sbase), "s"(lds) : "memory");
}
DI bf16x8 lds128(unsigned a) { return *(const LAS bf16x8*)(size_t)a; }
DI f32x4 ldsf4(unsigned a) { return *(const LAS f32x4*)(size_t)a; }
DI s16x4 ldstr(unsigned a) { return __builtin_amdgcn_ds_read_tr16_b64_v4i16((LAS s16x4*)(size_t)a); }
DI bf16x8 ldstr2(unsigned a0, unsigned a1) { s16x4 lo = ldstr(a0), hi = ldstr(a1); return __builtin_shufflevector(lo, hi, 0, 1, 2, 3, 4, 5, 6, 7); }
DI bf16x8 asb(u32x4 v) { return __builtin_bit_cast(bf16x8, v); }
DI float swapadd(float v) { auto r = __builtin_amdgcn_permlane32_swap(__float_as_uint(v), __float_as_uint(v), false, false); return __uint_as_float(r[0]) + __uint_as_float(r[1]); }
DI float swapmax(float v) { auto r = __builtin_amdgcn_permlane32_swap(__float_as_uint(v), __float_as_uint(v), false, false); return fmaxf(__uint_as_float(r[0]), __uint_as_float(r[1])); }
DI float swap16add(float v) { auto r = __builtin_amdgcn_permlane16_swap(__float_as_uint(v), __float_as_uint(v), false, false); return __uint_as_float(r[0]) + __uint_as_float(r[1]); }
DI float ror8add(float v) { return v + __uint_as_float(__builtin_amdgcn_update_dpp(0, __float_as_uint(v), 0x128  , 0xf, 0xf, false)); }
DI float sum_hi3(float v) { return swapadd(swap16add(ror8add(v))); }

template <int NL>
DI void mod_part(const u16* __restrict__ c, const u16* __restrict__ wmod, int row0, int col0, float* __restrict__ dst, LAS float* red) {
    const int tid = threadIdx.x, lane = tid & 63, wid = tid >> 6, r = tid >> 3, cq = tid & 7;
    const u16* wp = wmod + (size_t)(row0 + r) * 6144 + col0 + cq * 8;
    u32x4 w[NL]; u16 cv[NL];
#pragma unroll
    for (int i = 0; i < NL; ++i) cv[i] = c[row0 + r + 64 * i];
#pragma unroll
    for (int i = 0; i < NL; ++i) w[i] = *(const u32x4*)(wp + (size_t)i * 64 * 6144);
    float acc[8];
#pragma unroll
    for (int j = 0; j < 8; ++j) acc[j] = 0.f;
#pragma unroll
    for (int i = 0; i < NL; ++i) {
        const float x = bflo(cv[i]);
        const float s = bfr(x * __builtin_amdgcn_rcpf(1.f + __builtin_amdgcn_exp2f(-1.4426950408889634f * x)));
#pragma unroll
        for (int j = 0; j < 4; ++j) { acc[2 * j] += s * bflo(w[i][j]); acc[2 * j + 1] += s * bfhi(w[i][j]); }
    }
#pragma unroll
    for (int j = 0; j < 8; ++j) acc[j] = sum_hi3(acc[j]);
    if (lane < 8) { *(LAS f32x4*)(red + wid * 64 + lane * 8) = f32x4{acc[0], acc[1], acc[2], acc[3]}; *(LAS f32x4*)(red + wid * 64 + lane * 8 + 4) = f32x4{acc[4], acc[5], acc[6], acc[7]}; }
    WAITL0(); BAR();
    if (tid < 64) {
        float v = 0.f;
#pragma unroll
        for (int w8 = 0; w8 < 8; ++w8) v += red[w8 * 64 + tid];
        dst[tid] = v;
    }
}

__global__ __launch_bounds__(512) void k_pre(const u16* __restrict__ c, const u16* __restrict__ wmod, const u16* __restrict__ x, const char* __restrict__ wq,
                                             const char* __restrict__ wk, const char* __restrict__ wv, char* __restrict__ ws) {
    __shared__ __attribute__((aligned(16))) float sm[576];
    const int tid = threadIdx.x, lane = tid & 63, wid = __builtin_amdgcn_readfirstlane(tid >> 6);
    const int role = (blockIdx.x >> 3) & 1, id = (blockIdx.x & 7) + 8 * (blockIdx.x >> 4);
    TS(0, 0);
    if (role == 0) {
        if (tid < 16) ((u32x4*)(ws + WS_FLAG))[id * 16 + tid] = u32x4{0, 0, 0, 0};
        mod_part<4>(c, wmod, (id & 3) * 256, (id >> 2) * 64, (float*)(ws + WS_MODP) + (id & 3) * 2048 + (id >> 2) * 64, (LAS float*)sm);
    } else {
        const int r = lane & 7, cc = lane >> 3, rh = wid & 1, cqt = wid >> 1;
        const int row = id * 16 + rh * 8 + r;
        const u16* xp = x + (size_t)row * D_ + cqt * 256 + cc * 8;
        u32x4 xv[4];
#pragma unroll
        for (int i = 0; i < 4; ++i) xv[i] = *(const u32x4*)(xp + i * 64);
        const unsigned dump = (unsigned)(size_t)(LAS float*)sm + 2048;
        if (wid < 2) glds4(wq + (size_t)id * 16384 + wid * 8192, lane * 128, dump);
        else if (wid == 2) glds4((id & 64) ? wv : wk, (id & 63) * 1024 + (lane & 7) * 128, dump);
        float f[32], s = 0.f;
#pragma unroll
        for (int i = 0; i < 4; ++i)
#pragma unroll
            for (int j = 0; j < 4; ++j) { f[8 * i + 2 * j] = bflo(xv[i][j]); f[8 * i + 2 * j + 1] = bfhi(xv[i][j]); }
#pragma unroll
        for (int i = 0; i < 32; ++i) s += f[i];
        const float mw = sum_hi3(s) * (1.f / 256.f);
        float q = 0.f;
#pragma unroll
        for (int i = 0; i < 32; ++i) { const float d = f[i] - mw; q += d * d; }
        q = sum_hi3(q);
        LAS f32x2* st = (LAS f32x2*)sm;
        if (lane < 8) st[(rh * 8 + r) * 4 + cqt] = f32x2{mw, q};
        WAITL0(); BAR();
        const f32x4 a = *(LAS f32x4*)(st + (rh * 8 + r) * 4), b = *(LAS f32x4*)(st + (rh * 8 + r) * 4 + 2);
        const float mean = 0.25f * ((a[0] + a[2]) + (b[0] + b[2]));
        const float d0 = a[0] - mean, d1 = a[2] - mean, d2 = b[0] - mean, d3 = b[2] - mean;
        const float m2 = (a[1] + a[3]) + (b[1] + b[3]) + 256.f * ((d0 * d0 + d1 * d1) + (d2 * d2 + d3 * d3));
        const float mb = bfr(mean), den = bfr(sqrtf(bfr(bfr(m2 * (1.f / 1024.f)) + 1e-5f))), rs = 1.0f / den;
        char* np = ws + WS_N + ((size_t)(row >> 5) * 64 + cqt * 16 + (cc >> 1)) * 1024 + ((row & 31) + 32 * (cc & 1)) * 16;
#pragma unroll
        for (int i = 0; i < 4; ++i) {
            u32x4 o;
#pragma unroll
            for (int j = 0; j < 4; ++j) { const unsigned d = cvtpk(f[8 * i + 2 * j] - mb, f[8 * i + 2 * j + 1] - mb); o[j] = cvtpk(bflo(d) * rs, bfhi(d) * rs); }
            *(u32x4*)(np + i * 4096) = o;
        }
    }
    TS(0, 1);
}

constexpr int K2_LDS = 65536 + 8192;
__global__ __launch_bounds__(512) void k_kv(const u16* __restrict__ c, const u16* __restrict__ wmod, const char* __restrict__ wk, const char* __restrict__ wv,
                                            char* __restrict__ ws) {
    extern __shared__ __attribute__((aligned(16))) char smem[];
    const unsigned lds0 = (unsigned)(size_t)(LAS char*)smem;
    const int tid = threadIdx.x, lane = tid & 63, wid = __builtin_amdgcn_readfirstlane(tid >> 6);
    const int role = (blockIdx.x >> 3) & 1, id = (blockIdx.x & 7) + 8 * (blockIdx.x >> 4);
    TS(1, 0);
    if (role == 0) {
        mod_part<8>(c, wmod, (id & 1) * 512, 2048 + (id >> 1) * 64, (float*)(ws + WS_MODQ) + (id & 1) * 4096 + (id >> 1) * 64, (LAS float*)smem);
        return;
    }
    const int isv = id >> 6, tb = id & 63, hi = lane >> 5;
    const float* mp = (const float*)(ws + WS_MODP) + hi * 1024 + wid * 128 + (lane & 31) * 4;
    f32x4 mq[4];
#pragma unroll
    for (int i = 0; i < 4; ++i) mq[i] = *(const f32x4*)(mp + i * 2048);
    u32x4 nv[8];
    const char* np = ws + WS_N + ((size_t)tb * 64 + wid * 8) * 1024 + lane * 16;
#pragma unroll
    for (int i = 0; i < 8; ++i) nv[i] = *(const u32x4*)(np + i * 1024);
    const char* wsrc = isv ? wv : wk;
#pragma unroll
    for (int i = 0; i < 8; ++i) glds16(wsrc + wid * 8192 + i * 1024, lane * 16, lds0 + wid * 8192 + i * 1024);
    {
        f32x4 v = (mq[0] + mq[1]) + (mq[2] + mq[3]);
        const float one = hi ? 1.f : 0.f;
#pragma unroll
        for (int j = 0; j < 4; ++j) v[j] = bfr(bfr(v[j]) + one);
        *(LAS f32x4*)(size_t)(lds0 + 65536 + wid * 1024 + lane * 16) = v;
    }
    TS(1, 1);
    const unsigned ma = lds0 + 65536 + wid * 1024 + hi * 32;
    u32x4 hv[8];
#pragma unroll
    for (int i = 0; i < 8; ++i) {
        const f32x4 s0 = ldsf4(ma + i * 64), s1 = ldsf4(ma + i * 64 + 16), c0 = ldsf4(ma + 512 + i * 64), c1 = ldsf4(ma + 512 + i * 64 + 16);
        unsigned t;
        t = cvtpk(bflo(nv[i][0]) * c0[0], bfhi(nv[i][0]) * c0[1]); hv[i][0] = cvtpk(bflo(t) + s0[0], bfhi(t) + s0[1]);
        t = cvtpk(bflo(nv[i][1]) * c0[2], bfhi(nv[i][1]) * c0[3]); hv[i][1] = cvtpk(bflo(t) + s0[2], bfhi(t) + s0[3]);
        t = cvtpk(bflo(nv[i][2]) * c1[0], bfhi(nv[i][2]) * c1[1]); hv[i][2] = cvtpk(bflo(t) + s1[0], bfhi(t) + s1[1]);
        t = cvtpk(bflo(nv[i][3]) * c1[2], bfhi(nv[i][3]) * c1[3]); hv[i][3] = cvtpk(bflo(t) + s1[2], bfhi(t) + s1[3]);
    }
    TS(1, 2);
    WAITV(0);
    {
        char* hp = ws + WS_H + ((size_t)tb * 64 + wid * 8) * 1024 + lane * 16;
        if (isv) {
#pragma unroll
            for (int i = 4; i < 8; ++i) *(u32x4*)(hp + i * 1024) = hv[i];
        } else {
#pragma unroll
            for (int i = 0; i < 4; ++i) *(u32x4*)(hp + i * 1024) = hv[i];
        }
    }
    f32x16 acc = {};
    const unsigned wa = lds0 + wid * 8192 + (8 * hi + ((lane & 15) >> 2)) * 64 + ((lane >> 4) & 1) * 32 + (lane & 3) * 8;
    if (isv) {
#pragma unroll
        for (int i = 0; i < 8; ++i) acc = MFMA32(asb(hv[i]), ldstr2(wa + i * 1024, wa + i * 1024 + 256), acc);
    } else {
#pragma unroll
        for (int i = 0; i < 8; ++i) acc = MFMA32(ldstr2(wa + i * 1024, wa + i * 1024 + 256), asb(hv[i]), acc);
    }
    LAS f32x2* rp = (LAS f32x2*)(size_t)(lds0 + wid * 8192);
#pragma unroll
    for (int r = 0; r < 8; ++r) rp[r * 64 + lane] = f32x2{acc[2 * r], acc[2 * r + 1]};
    WAITL0(); BAR();
    {
        f32x2 a = {0.f, 0.f};
#pragma unroll
        for (int w = 0; w < 8; ++w) a += ((const LAS f32x2*)(size_t)(lds0 + w * 8192))[wid * 64 + lane];
        const int s = wid >> 2, rec = isv ? ((tb + 1 + s) & 63) : tb;
        *(unsigned*)(ws + WS_KV + (size_t)rec * 4096 + isv * 2048 + s * 1024 + lane * 16 + (wid & 3) * 4) = cvtpk(a[0], a[1]);
    }
    TS(1, 3);
}

template <int NRB, int ORDER, int NS, int EX, class Extra>
DI void tgemm(f32x16 (&acc)[NRB / 2], unsigned lds0, const char* ximg, size_t xrb_stride, const char* w, int wid, int lane, Extra extra) {
    constexpr int NSLOT = 4, WB = NRB / 2, PW = NRB / 2 + 2;
    constexpr unsigned WOFF = NRB * 4096, SLOT = WOFF + 16384;
    auto swz = [](int r) { return (ORDER == 3) ? ((r & 1) | (((r >> 3) & 1) << 1)) : (r & 3); };
    const char* xs = ximg + (size_t)(wid >> 2) * xrb_stride + (wid & 3) * 1024;
    unsigned wv0;
    { const int r = wid * 4 + (lane >> 4), cp = lane & 15; wv0 = (unsigned)r * 2048 + (unsigned)(((cp >> 2) ^ swz(r)) * 64 + (cp & 3) * 16); }
    const unsigned xvo = lane * 16;
    auto stage = [&](int st, int slot) {
        const unsigned lb = lds0 + slot * SLOT + wid * 1024;
        const char* wb = w + (size_t)st * 64 * 2048;
        glds16(xs + (size_t)st * 4096, xvo, lb);
        if (NRB == 4) glds16(xs + 2 * xrb_stride + (size_t)st * 4096, xvo, lb + 8192);
        glds16(wb, wv0, lb + WOFF);
        glds16(wb + 32 * 2048, wv0, lb + WOFF + 8192);
    };
    const int rb = (NRB == 4) ? (wid >> 1) : (wid & 1), cb = (NRB == 4) ? (wid & 1) : (wid >> 1);
    const int hi = lane >> 5, qq = (lane & 15) >> 2;
    const unsigned xa = rb * 4096 + lane * 16;
    const int rl = (ORDER == 0) ? 8 * hi + qq : (ORDER == 1) ? 4 * hi + qq : 16 * hi + (qq >> 1) + 8 * (qq & 1);
    const unsigned wa = WOFF + rl * 256 + ((cb ^ swz(rl)) * 64) + ((lane >> 4) & 1) * 32 + (lane & 3) * 8;
    bf16x8 xf[2][4], wf[2][4 * WB];
    auto rd = [&](int slot, bf16x8 (&X)[4], bf16x8 (&W)[4 * WB]) {
        unsigned ax = lds0 + slot * SLOT + xa, aw = lds0 + slot * SLOT + wa, aw2 = lds0 + slot * SLOT + (wa ^ 128);
        asm volatile("" : "+v"(ax), "+v"(aw));
        if (WB == 2) asm volatile("" : "+v"(aw2));
#pragma unroll
        for (int s = 0; s < 4; ++s) {
            const int r0 = (ORDER == 3) ? (32 * (s & 1) + 4 * (s >> 1)) : 16 * s, dt = (ORDER == 0) ? 4 : (ORDER == 1) ? 8 : 2;
            X[s] = lds128(ax + s * 1024);
            W[s * WB] = ldstr2(aw + r0 * 256, aw + (r0 + dt) * 256);
            if (WB == 2) W[s * WB + 1] = ldstr2(aw2 + r0 * 256, aw2 + (r0 + dt) * 256);
        }
    };
    auto mma = [&](bf16x8 (&X)[4], bf16x8 (&W)[4 * WB]) {
#pragma unroll
        for (int s = 0; s < 4; ++s)
#pragma unroll
            for (int b = 0; b < WB; ++b) acc[b] = MFMA32(W[s * WB + b], X[s], acc[b]);
#pragma unroll
        for (int i = 0; i < 4 * WB; ++i) { __builtin_amdgcn_sched_group_barrier(0x8, 1, 0); __builtin_amdgcn_sched_group_barrier(0x100, 3, 0); }
        __builtin_amdgcn_sched_barrier(0);
    };
#pragma unroll
    for (int i = 0; i < NSLOT; ++i) stage(i, i);
    WAITV(PW * (NSLOT - 1)); BAR();
    rd(0, xf[0], wf[0]);
    static_assert(NS % 4 == 0, "");
#pragma nounroll
    for (int st = 0; st < NS - 4; st += 4) {
#pragma unroll
        for (int u = 0; u < 4; ++u) {
            WAITL0(); WAITV(PW * (NSLOT - 2)); BAR(); SB0();
            rd((u + 1) & 3, xf[(u + 1) & 1], wf[(u + 1) & 1]);
            mma(xf[u & 1], wf[u & 1]);
            stage(st + u + NSLOT, u);
        }
    }
    WAITL0(); WAITV(PW * 2); BAR(); SB0();      rd(1, xf[1], wf[1]); mma(xf[0], wf[0]); extra(lds0);
    WAITL0(); WAITV(PW + EX); BAR(); SB0();     rd(2, xf[0], wf[0]); mma(xf[1], wf[1]);
    WAITL0(); WAITV(EX); BAR(); SB0();          rd(3, xf[1], wf[1]); mma(xf[0], wf[0]);
    WAITL0(); SB0();                                                 mma(xf[1], wf[1]);
    WAITV(0); BAR();
}

constexpr int K3_LDS = 163840;
__global__ __launch_bounds__(512) void k_attn(const char* __restrict__ wq, const char* __restrict__ wg, const char* __restrict__ wu, const char* __restrict__ wo,
                                              const char* __restrict__ wd, char* __restrict__ ws) {
    extern __shared__ __attribute__((aligned(16))) char smem[];
    const unsigned lds0 = (unsigned)(size_t)(LAS char*)smem;
    const int tid = threadIdx.x, lane = tid & 63, wid = __builtin_amdgcn_readfirstlane(tid >> 6);
    const int xcd = blockIdx.x & 7, idx = blockIdx.x >> 3;
    const int tg = (xcd >> 1) * 8 + (idx & 7), hg = (xcd & 1) * 4 + (idx >> 3);
    const char* kv = ws + WS_KV;
    auto kvslot = [&](int c) { const unsigned s = (unsigned)(c + 3) & 3u; return lds0 + (s == 3 ? 98304u : s == 0 ? 131072u : (s - 1) * 32768u); };
    auto kvstage = [&](int c) {
        const char* src = kv + (size_t)((2 + 8 * c + wid) & 63) * 4096; const unsigned dst = kvslot(c) + wid * 4096;
#pragma unroll
        for (int i = 0; i < 4; ++i) glds16(src + i * 1024, lane * 16, dst + i * 1024);
    };
    TS(2, 0);
    f32x16 qa[1] = {};
    tgemm<2, 0, 16, 5>(qa, lds0, ws + WS_H + (size_t)tg * 2 * 65536, 65536, wq + hg * 256, wid, lane,
                       [&](unsigned lb) { glds16(kv + wid * 1024, lane * 16, lb + wid * 1024); kvstage(0); });
    TS(2, 1);
    kvstage(1);
    constexpr float QS = 0.17677669529663687f * 1.4426950408889634f;
    u32x4 q0, q1;
#pragma unroll
    for (int j = 0; j < 4; ++j) { q0[j] = cvtpk(qa[0][2 * j] * QS, qa[0][2 * j + 1] * QS); q1[j] = cvtpk(qa[0][8 + 2 * j] * QS, qa[0][9 + 2 * j] * QS); }
    bf16x8 qf0 = asb(q0), qf1 = asb(q1);
    bf16x8 sel;
    { const int i = lane & 15, g = lane >> 4; const short v = ((i & 3) == 0 && ((i >> 2) & 1) == (g & 1)) ? (short)0x3f80 : (short)0;
#pragma unroll
      for (int j = 0; j < 8; ++j) sel[j] = v; }
    f32x16 negm, sa, sb = {};
    bf16x8 k0, k1, v0, v1 = {};
    {
        const unsigned a = lds0 + lane * 16;
        const bf16x8 ka = lds128(a), kb = lds128(a + 1024);
        k0 = lds128(a + 4096); k1 = lds128(a + 5120); v0 = lds128(a + 6144);
        f32x16 s = {};
        s = MFMA32(ka, qf0, s); s = MFMA32(kb, qf1, s);
        float mx = s[0];
#pragma unroll
        for (int r = 1; r < 16; ++r) mx = fmaxf(mx, s[r]);
        mx = swapmax(mx);
#pragma unroll
        for (int r = 0; r < 16; ++r) { negm[r] = -mx; sa[r] = s[r] - mx; }
    }
    WAITL0(); BAR();
    kvstage(2); kvstage(3);
    f32x16 o = {}; f32x4 l = {}, tt = {}; u32x4 p0 = {}, p1 = {};
    const int sj = blockIdx.x, cu = sj >> 2, kq = sj & 3, ss = kq * 8 + wid;
    u32x4 rw[8];
    for (int c = 0; c < 8; ++c) {
        if (c > 0) {
            if (c <= 5) WAITV(8); else if (c == 6) WAITV(4); else WAITV(0);
            BAR();
        }
        if (c == 0) {
            if (sj < 224) {
                const int i = lane & 15, g = lane >> 4;
                const char* src = ((i & 2) ? wu : wg) + (size_t)(32 * ss + 16 * (g >> 1) + 4 * (g & 1)) * (F_ * 2) + (64 * cu + 16 * (i >> 2) + 8 * (i & 1)) * 2;
#pragma unroll
                for (int j = 0; j < 8; ++j) rw[j] = *(const u32x4*)(src + (size_t)(8 * (j >> 2) + (j & 3)) * (F_ * 2));
            } else {
                const int u = (sj - 224) * 8 + wid;
#pragma unroll
                for (int j = 0; j < 4; ++j) glds4(wd + (size_t)u * 28672, (j * 56 + (lane < 56 ? lane : 55)) * 128, lds0 + 65536 + 8192);
                if (sj < 232) { const int col = (sj - 224) * 512 + tid; const float* mq = (const float*)(ws + WS_MODQ);
                    float v = bfr(mq[col] + mq[4096 + col]); if ((col >> 10) == 2) v = bfr(v + 1.f); ((float*)(ws + WS_MODF))[col] = v; }
            }
        } else if (c == 2) {
            if (sj < 224) {
                u32x4 bop = {0, 0, 0, 0};
                const int g = lane >> 4;
                if ((lane & 15) < 2) {
                    const float* mq = (const float*)(ws + WS_MODQ) + ((lane & 1) ? 1024 : 2048) + 32 * ss + 16 * (g >> 1) + 4 * (g & 1);
                    const float one = (lane & 1) ? 0.f : 1.f;
                    const f32x4 a = *(const f32x4*)mq + *(const f32x4*)(mq + 4096), b = *(const f32x4*)(mq + 8) + *(const f32x4*)(mq + 4104);
                    bop[0] = cvtpk(bfr(a[0]) + one, bfr(a[1]) + one); bop[1] = cvtpk(bfr(a[2]) + one, bfr(a[3]) + one);
                    bop[2] = cvtpk(bfr(b[0]) + one, bfr(b[1]) + one); bop[3] = cvtpk(bfr(b[2]) + one, bfr(b[3]) + one);
                }
#pragma unroll
                for (int jj = 0; jj < 8; ++jj) {
                    u32x4 f;
#pragma unroll
                    for (int d = 0; d < 4; ++d) f[d] = __builtin_amdgcn_perm(rw[2 * d + 1][jj >> 1], rw[2 * d][jj >> 1], (jj & 1) ? 0x07060302u : 0x05040100u);
                    const int phi = 8 * cu + jj, G = phi / 28, rr = phi - 28 * G, blk = rr >> 2, d = rr & 3;
                    const int wj = blk < 4 ? blk : blk < 6 ? 2 * (blk - 4) + (d >> 1) : d, sl = blk < 4 ? d : blk < 6 ? 4 + (d & 1) : 6;
                    *(u32x4*)(ws + WS_WGU + ((size_t)((2 * G + (wj >> 1)) * 32 + ss) * 14 + (wj & 1) * 7 + sl) * 1024 + lane * 16) = f;
                    const f32x4 ut = MFMA16(asb(f), asb(bop), (f32x4{}));
                    if ((lane & 15) < 2) *(LAS f32x4*)(size_t)(lds0 + 65536 + wid * 1024 + jj * 128 + (lane & 1) * 64 + g * 16) = ut;
                }
                WAITL0();
            }
            glds4(wo + (size_t)(idx * 8 + wid) * 8192, lane * 128, lds0 + 65536 + 8192);
        } else if (c == 3) {
            if (sj < 224 && tid < 256) {
                float v = 0.f;
#pragma unroll
                for (int w = 0; w < 8; ++w) v += *(const LAS float*)(size_t)(lds0 + 65536 + w * 1024 + tid * 4);
                const int phi = 8 * cu + (tid >> 5), G = phi / 28, rr = phi - 28 * G, blk = rr >> 2, d = rr & 3;
                const int wj = blk < 4 ? blk : blk < 6 ? 2 * (blk - 4) + (d >> 1) : d, sl = blk < 4 ? d : blk < 6 ? 4 + (d & 1) : 6;
                ((float*)(ws + WS_UT))[((2 * G + (wj >> 1)) * 4 + kq) * 448 + ((wj & 1) * 7 + sl) * 32 + (tid & 31)] = v;
            }
        }
        if (c > 0 && c + 3 < 8) kvstage(c + 3);
        TS(7, 2 * c);
        unsigned ad = kvslot(c) + lane * 16;
        asm volatile(
    "s_waitcnt lgkmcnt(1)\n\t"
    "v_mfma_f32_32x32x16_bf16 v[32:47], v[80:83], v[68:71], v[48:63]\n\t"
    "v_cvt_pk_bf16_f32 v103, v106, v107\n\t"
    "v_exp_f32 v104, v16\n\t"
    "v_exp_f32 v105, v17\n\t"
    "v_exp_f32 v106, v18\n\t"
    "v_cvt_pk_bf16_f32 v96, v104, v105\n\t"
    "v_mfma_f32_32x32x16_bf16 v[32:47], v[84:87], v[72:75], v[32:47]\n\t"
    "v_exp_f32 v107, v19\n\t"
    "ds_read_b128 v[80:83], v108 offset:0\n\t"
    "v_exp_f32 v104, v20\n\t"
    "v_cvt_pk_bf16_f32 v97, v106, v107\n\t"
    "ds_read_b128 v[84:87], v108 offset:1024\n\t"
    "v_exp_f32 v105, v21\n\t"
    "v_mfma_f32_32x32x16_bf16 v[0:15], v[92:95], v[100:103], v[0:15]\n\t"
    "v_exp_f32 v106, v22\n\t"
    "v_cvt_pk_bf16_f32 v98, v104, v105\n\t"
    "ds_read_b128 v[92:95], v108 offset:3072\n\t"
    "v_exp_f32 v107, v23\n\t"
    "v_exp_f32 v104, v24\n\t"
    "v_mfma_f32_16x16x32_bf16 v[64:67], v[76:79], v[100:103], v[64:67]\n\t"
    "v_cvt_pk_bf16_f32 v99, v106, v107\n\t"
    "v_exp_f32 v105, v25\n\t"
    "v_exp_f32 v106, v26\n\t"
    "s_waitcnt lgkmcnt(3)\n\t"
    "v_mfma_f32_32x32x16_bf16 v[0:15], v[88:91], v[96:99], v[0:15]\n\t"
    "v_cvt_pk_bf16_f32 v100, v104, v105\n\t"
    "v_exp_f32 v107, v27\n\t"
    "ds_read_b128 v[88:91], v108 offset:2048\n\t"
    "v_exp_f32 v104, v28\n\t"
    "v_cvt_pk_bf16_f32 v101, v106, v107\n\t"
    "v_exp_f32 v105, v29\n\t"
    "v_mfma_f32_16x16x32_bf16 v[64:67], v[76:79], v[96:99], v[64:67]\n\t"
    "v_exp_f32 v106, v30\n\t"
    "v_cvt_pk_bf16_f32 v102, v104, v105\n\t"
    "v_exp_f32 v107, v31\n\t"
    "s_waitcnt lgkmcnt(1)\n\t"
    "v_mfma_f32_32x32x16_bf16 v[16:31], v[80:83], v[68:71], v[48:63]\n\t"
    "v_cvt_pk_bf16_f32 v103, v106, v107\n\t"
    "v_exp_f32 v104, v32\n\t"
    "v_exp_f32 v105, v33\n\t"
    "v_exp_f32 v106, v34\n\t"
    "v_cvt_pk_bf16_f32 v96, v104, v105\n\t"
    "v_mfma_f32_32x32x16_bf16 v[16:31], v[84:87], v[72:75], v[16:31]\n\t"
    "v_exp_f32 v107, v35\n\t"
    "ds_read_b128 v[80:83], v108 offset:4096\n\t"
    "v_exp_f32 v104, v36\n\t"
    "v_cvt_pk_bf16_f32 v97, v106, v107\n\t"
    "ds_read_b128 v[84:87], v108 offset:5120\n\t"
    "v_exp_f32 v105, v37\n\t"
    "v_mfma_f32_32x32x16_bf16 v[0:15], v[92:95], v[100:103], v[0:15]\n\t"
    "v_exp_f32 v106, v38\n\t"
    "v_cvt_pk_bf16_f32 v98, v104, v105\n\t"
    "ds_read_b128 v[92:95], v108 offset:7168\n\t"
    "v_exp_f32 v107, v39\n\t"
    "v_exp_f32 v104, v40\n\t"
    "v_mfma_f32_16x16x32_bf16 v[64:67], v[76:79], v[100:103], v[64:67]\n\t"
    "v_cvt_pk_bf16_f32 v99, v106, v107\n\t"
    "v_exp_f32 v105, v41\n\t"
    "v_exp_f32 v106, v42\n\t"
    "s_waitcnt lgkmcnt(3)\n\t"
    "v_mfma_f32_32x32x16_bf16 v[0:15], v[88:91], v[96:99], v[0:15]\n\t"
    "v_cvt_pk_bf16_f32 v100, v104, v105\n\t"
    "v_exp_f32 v107, v43\n\t"
    "ds_read_b128 v[88:91], v108 offset:6144\n\t"
    "v_exp_f32 v104, v44\n\t"
    "v_cvt_pk_bf16_f32 v101, v106, v107\n\t"
    "v_exp_f32 v105, v45\n\t"
    "v_mfma_f32_16x16x32_bf16 v[64:67], v[76:79], v[96:99], v[64:67]\n\t"
    "v_exp_f32 v106, v46\n\t"
    "v_cvt_pk_bf16_f32 v102, v104, v105\n\t"
    "v_exp_f32 v107, v47\n\t"
    "s_waitcnt lgkmcnt(1)\n\t"
    "v_mfma_f32_32x32x16_bf16 v[32:47], v[80:83], v[68:71], v[48:63]\n\t"
    "v_cvt_pk_bf16_f32 v103, v106, v107\n\t"
    "v_exp_f32 v104, v16\n\t"
    "v_exp_f32 v105, v17\n\t"
    "v_exp_f32 v106, v18\n\t"
    "v_cvt_pk_bf16_f32 v96, v104, v105\n\t"
    "v_mfma_f32_32x32x16_bf16 v[32:47], v[84:87], v[72:75], v[32:47]\n\t"
    "v_exp_f32 v107, v19\n\t"
    "ds_read_b128 v[80:83], v108 offset:8192\n\t"
    "v_exp_f32 v104, v20\n\t"
    "v_cvt_pk_bf16_f32 v97, v106, v107\n\t"
    "ds_read_b128 v[84:87], v108 offset:9216\n\t"
    "v_exp_f32 v105, v21\n\t"
    "v_mfma_f32_32x32x16_bf16 v[0:15], v[92:95], v[100:103], v[0:15]\n\t"
    "v_exp_f32 v106, v22\n\t"
    "v_cvt_pk_bf16_f32 v98, v104, v105\n\t"
    "ds_read_b128 v[92:95], v108 offset:11264\n\t"
    "v_exp_f32 v107, v23\n\t"
    "v_exp_f32 v104, v24\n\t"
    "v_mfma_f32_16x16x32_bf16 v[64:67], v[76:79], v[100:103], v[64:67]\n\t"
    "v_cvt_pk_bf16_f32 v99, v106, v107\n\t"
    "v_exp_f32 v105, v25\n\t"
    "v_exp_f32 v106, v26\n\t"
    "s_waitcnt lgkmcnt(3)\n\t"
    "v_mfma_f32_32x32x16_bf16 v[0:15], v[88:91], v[96:99], v[0:15]\n\t"
    "v_cvt_pk_bf16_f32 v100, v104, v105\n\t"
    "v_exp_f32 v107, v27\n\t"
    "ds_read_b128 v[88:91], v108 offset:10240\n\t"
    "v_exp_f32 v104, v28\n\t"
    "v_cvt_pk_bf16_f32 v101, v106, v107\n\t"
    "v_exp_f32 v105, v29\n\t"
    "v_mfma_f32_16x16x32_bf16 v[64:67], v[76:79], v[96:99], v[64:67]\n\t"
    "v_exp_f32 v106, v30\n\t"
    "v_cvt_pk_bf16_f32 v102, v104, v105\n\t"
    "v_exp_f32 v107, v31\n\t"
    "s_waitcnt lgkmcnt(1)\n\t"
    "v_mfma_f32_32x32x16_bf16 v[16:31], v[80:83], v[68:71], v[48:63]\n\t"
    "v_cvt_pk_bf16_f32 v103, v106, v107\n\t"
    "v_exp_f32 v104, v32\n\t"
    "v_exp_f32 v105, v33\n\t"
    "v_exp_f32 v106, v34\n\t"
    "v_cvt_pk_bf16_f32 v96, v104, v105\n\t"
    "v_mfma_f32_32x32x16_bf16 v[16:31], v[84:87], v[72:75], v[16:31]\n\t"
    "v_exp_f32 v107, v35\n\t"
    "ds_read_b128 v[80:83], v108 offset:12288\n\t"
    "v_exp_f32 v104, v36\n\t"
    "v_cvt_pk_bf16_f32 v97, v106, v107\n\t"
    "ds_read_b128 v[84:87], v108 offset:13312\n\t"
    "v_exp_f32 v105, v37\n\t"
    "v_mfma_f32_32x32x16_bf16 v[0:15], v[92:95], v[100:103], v[0:15]\n\t"
    "v_exp_f32 v106, v38\n\t"
    "v_cvt_pk_bf16_f32 v98, v104, v105\n\t"
    "ds_read_b128 v[92:95], v108 offset:15360\n\t"
    "v_exp_f32 v107, v39\n\t"
    "v_exp_f32 v104, v40\n\t"
    "v_mfma_f32_16x16x32_bf16 v[64:67], v[76:79], v[100:103], v[64:67]\n\t"
    "v_cvt_pk_bf16_f32 v99, v106, v107\n\t"
    "v_exp_f32 v105, v41\n\t"
    "v_exp_f32 v106, v42\n\t"
    "s_waitcnt lgkmcnt(3)\n\t"
    "v_mfma_f32_32x32x16_bf16 v[0:15], v[88:91], v[96:99], v[0:15]\n\t"
    "v_cvt_pk_bf16_f32 v100, v104, v105\n\t"
    "v_exp_f32 v107, v43\n\t"
    "ds_read_b128 v[88:91], v108 offset:14336\n\t"
    "v_exp_f32 v104, v44\n\t"
    "v_cvt_pk_bf16_f32 v101, v106, v107\n\t"
    "v_exp_f32 v105, v45\n\t"
    "v_mfma_f32_16x16x32_bf16 v[64:67], v[76:79], v[96:99], v[64:67]\n\t"
    "v_exp_f32 v106, v46\n\t"
    "v_cvt_pk_bf16_f32 v102, v104, v105\n\t"
    "v_exp_f32 v107, v47\n\t"
    "s_waitcnt lgkmcnt(1)\n\t"
    "v_mfma_f32_32x32x16_bf16 v[32:47], v[80:83], v[68:71], v[48:63]\n\t"
    "v_cvt_pk_bf16_f32 v103, v106, v107\n\t"
    "v_exp_f32 v104, v16\n\t"
    "v_exp_f32 v105, v17\n\t"
    "v_exp_f32 v106, v18\n\t"
    "v_cvt_pk_bf16_f32 v96, v104, v105\n\t"
    "v_mfma_f32_32x32x16_bf16 v[32:47], v[84:87], v[72:75], v[32:47]\n\t"
    "v_exp_f32 v107, v19\n\t"
    "ds_read_b128 v[80:83], v108 offset:16384\n\t"
    "v_exp_f32 v104, v20\n\t"
    "v_cvt_pk_bf16_f32 v97, v106, v107\n\t"
    "ds_read_b128 v[84:87], v108 offset:17408\n\t"
    "v_exp_f32 v105, v21\n\t"
    "v_mfma_f32_32x32x16_bf16 v[0:15], v[92:95], v[100:103], v[0:15]\n\t"
    "v_exp_f32 v106, v22\n\t"
    "v_cvt_pk_bf16_f32 v98, v104, v105\n\t"
    "ds_read_b128 v[92:95], v108 offset:19456\n\t"
    "v_exp_f32 v107, v23\n\t"
    "v_exp_f32 v104, v24\n\t"
    "v_mfma_f32_16x16x32_bf16 v[64:67], v[76:79], v[100:103], v[64:67]\n\t"
    "v_cvt_pk_bf16_f32 v99, v106, v107\n\t"
    "v_exp_f32 v105, v25\n\t"
    "v_exp_f32 v106, v26\n\t"
    "s_waitcnt lgkmcnt(3)\n\t"
    "v_mfma_f32_32x32x16_bf16 v[0:15], v[88:91], v[96:99], v[0:15]\n\t"
    "v_cvt_pk_bf16_f32 v100, v104, v105\n\t"
    "v_exp_f32 v107, v27\n\t"
    "ds_read_b128 v[88:91], v108 offset:18432\n\t"
    "v_exp_f32 v104, v28\n\t"
    "v_cvt_pk_bf16_f32 v101, v106, v107\n\t"
    "v_exp_f32 v105, v29\n\t"
    "v_mfma_f32_16x16x32_bf16 v[64:67], v[76:79], v[96:99], v[64:67]\n\t"
    "v_exp_f32 v106, v30\n\t"
    "v_cvt_pk_bf16_f32 v102, v104, v105\n\t"
    "v_exp_f32 v107, v31\n\t"
    "s_waitcnt lgkmcnt(1)\n\t"
    "v_mfma_f32_32x32x16_bf16 v[16:31], v[80:83], v[68:71], v[48:63]\n\t"
    "v_cvt_pk_bf16_f32 v103, v106, v107\n\t"
    "v_exp_f32 v104, v32\n\t"
    "v_exp_f32 v105, v33\n\t"
    "v_exp_f32 v106, v34\n\t"
    "v_cvt_pk_bf16_f32 v96, v104, v105\n\t"
    "v_mfma_f32_32x32x16_bf16 v[16:31], v[84:87], v[72:75], v[16:31]\n\t"
    "v_exp_f32 v107, v35\n\t"
    "ds_read_b128 v[80:83], v108 offset:20480\n\t"
    "v_exp_f32 v104, v36\n\t"
    "v_cvt_pk_bf16_f32 v97, v106, v107\n\t"
    "ds_read_b128 v[84:87], v108 offset:21504\n\t"
    "v_exp_f32 v105, v37\n\t"
    "v_mfma_f32_32x32x16_bf16 v[0:15], v[92:95], v[100:103], v[0:15]\n\t"
    "v_exp_f32 v106, v38\n\t"
    "v_cvt_pk_bf16_f32 v98, v104, v105\n\t"
    "ds_read_b128 v[92:95], v108 offset:23552\n\t"
    "v_exp_f32 v107, v39\n\t"
    "v_exp_f32 v104, v40\n\t"
    "v_mfma_f32_16x16x32_bf16 v[64:67], v[76:79], v[100:103], v[64:67]\n\t"
    "v_cvt_pk_bf16_f32 v99, v106, v107\n\t"
    "v_exp_f32 v105, v41\n\t"
    "v_exp_f32 v106, v42\n\t"
    "s_waitcnt lgkmcnt(3)\n\t"
    "v_mfma_f32_32x32x16_bf16 v[0:15], v[88:91], v[96:99], v[0:15]\n\t"
    "v_cvt_pk_bf16_f32 v100, v104, v105\n\t"
    "v_exp_f32 v107, v43\n\t"
    "ds_read_b128 v[88:91], v108 offset:22528\n\t"
    "v_exp_f32 v104, v44\n\t"
    "v_cvt_pk_bf16_f32 v101, v106, v107\n\t"
    "v_exp_f32 v105, v45\n\t"
    "v_mfma_f32_16x16x32_bf16 v[64:67], v[76:79], v[96:99], v[64:67]\n\t"
    "v_exp_f32 v106, v46\n\t"
    "v_cvt_pk_bf16_f32 v102, v104, v105\n\t"
    "v_exp_f32 v107, v47\n\t"
    "s_waitcnt lgkmcnt(1)\n\t"
    "v_mfma_f32_32x32x16_bf16 v[32:47], v[80:83], v[68:71], v[48:63]\n\t"
    "v_cvt_pk_bf16_f32 v103, v106, v107\n\t"
    "v_exp_f32 v104, v16\n\t"
    "v_exp_f32 v105, v17\n\t"
    "v_exp_f32 v106, v18\n\t"
    "v_cvt_pk_bf16_f32 v96, v104, v105\n\t"
    "v_mfma_f32_32x32x16_bf16 v[32:47], v[84:87], v[72:75], v[32:47]\n\t"
    "v_exp_f32 v107, v19\n\t"
    "ds_read_b128 v[80:83], v108 offset:24576\n\t"
    "v_exp_f32 v104, v20\n\t"
    "v_cvt_pk_bf16_f32 v97, v106, v107\n\t"
    "ds_read_b128 v[84:87], v108 offset:25600\n\t"
    "v_exp_f32 v105, v21\n\t"
    "v_mfma_f32_32x32x16_bf16 v[0:15], v[92:95], v[100:103], v[0:15]\n\t"
    "v_exp_f32 v106, v22\n\t"
    "v_cvt_pk_bf16_f32 v98, v104, v105\n\t"
    "ds_read_b128 v[92:95], v108 offset:27648\n\t"
    "v_exp_f32 v107, v23\n\t"
    "v_exp_f32 v104, v24\n\t"
    "v_mfma_f32_16x16x32_bf16 v[64:67], v[76:79], v[100:103], v[64:67]\n\t"
    "v_cvt_pk_bf16_f32 v99, v106, v107\n\t"
    "v_exp_f32 v105, v25\n\t"
    "v_exp_f32 v106, v26\n\t"
    "s_waitcnt lgkmcnt(3)\n\t"
    "v_mfma_f32_32x32x16_bf16 v[0:15], v[88:91], v[96:99], v[0:15]\n\t"
    "v_cvt_pk_bf16_f32 v100, v104, v105\n\t"
    "v_exp_f32 v107, v27\n\t"
    "ds_read_b128 v[88:91], v108 offset:26624\n\t"
    "v_exp_f32 v104, v28\n\t"
    "v_cvt_pk_bf16_f32 v101, v106, v107\n\t"
    "v_exp_f32 v105, v29\n\t"
    "v_mfma_f32_16x16x32_bf16 v[64:67], v[76:79], v[96:99], v[64:67]\n\t"
    "v_exp_f32 v106, v30\n\t"
    "v_cvt_pk_bf16_f32 v102, v104, v105\n\t"
    "v_exp_f32 v107, v31\n\t"
    "s_waitcnt lgkmcnt(1)\n\t"
    "v_mfma_f32_32x32x16_bf16 v[16:31], v[80:83], v[68:71], v[48:63]\n\t"
    "v_cvt_pk_bf16_f32 v103, v106, v107\n\t"
    "v_exp_f32 v104, v32\n\t"
    "v_exp_f32 v105, v33\n\t"
    "v_exp_f32 v106, v34\n\t"
    "v_cvt_pk_bf16_f32 v96, v104, v105\n\t"
    "v_mfma_f32_32x32x16_bf16 v[16:31], v[84:87], v[72:75], v[16:31]\n\t"
    "v_exp_f32 v107, v35\n\t"
    "ds_read_b128 v[80:83], v108 offset:28672\n\t"
    "v_exp_f32 v104, v36\n\t"
    "v_cvt_pk_bf16_f32 v97, v106, v107\n\t"
    "ds_read_b128 v[84:87], v108 offset:29696\n\t"
    "v_exp_f32 v105, v37\n\t"
    "v_mfma_f32_32x32x16_bf16 v[0:15], v[92:95], v[100:103], v[0:15]\n\t"
    "v_exp_f32 v106, v38\n\t"
    "v_cvt_pk_bf16_f32 v98, v104, v105\n\t"
    "ds_read_b128 v[92:95], v108 offset:31744\n\t"
    "v_exp_f32 v107, v39\n\t"
    "v_exp_f32 v104, v40\n\t"
    "v_mfma_f32_16x16x32_bf16 v[64:67], v[76:79], v[100:103], v[64:67]\n\t"
    "v_cvt_pk_bf16_f32 v99, v106, v107\n\t"
    "v_exp_f32 v105, v41\n\t"
    "v_exp_f32 v106, v42\n\t"
    "s_waitcnt lgkmcnt(3)\n\t"
    "v_mfma_f32_32x32x16_bf16 v[0:15], v[88:91], v[96:99], v[0:15]\n\t"
    "v_cvt_pk_bf16_f32 v100, v104, v105\n\t"
    "v_exp_f32 v107, v43\n\t"
    "ds_read_b128 v[88:91], v108 offset:30720\n\t"
    "v_exp_f32 v104, v44\n\t"
    "v_cvt_pk_bf16_f32 v101, v106, v107\n\t"
    "v_exp_f32 v105, v45\n\t"
    "v_mfma_f32_16x16x32_bf16 v[64:67], v[76:79], v[96:99], v[64:67]\n\t"
    "v_exp_f32 v106, v46\n\t"
    "v_cvt_pk_bf16_f32 v102, v104, v105\n\t"
    "v_exp_f32 v107, v47\n\t"
    "s_waitcnt lgkmcnt(0)\n\t"
            "s_nop 7\n\ts_nop 3"
            : "+{v[0:15]}"(o), "+{v[16:31]}"(sa), "+{v[32:47]}"(sb), "+{v[64:67]}"(l), "+{v[80:83]}"(k0), "+{v[84:87]}"(k1), "+{v[88:91]}"(v0), "+{v[92:95]}"(v1),
              "+{v[96:99]}"(p0), "+{v[100:103]}"(p1), "+{v[104:107]}"(tt)
            : "{v[48:63]}"(negm), "{v[68:71]}"(qf0), "{v[72:75]}"(qf1), "{v[76:79]}"(sel), "{v108}"(ad)
            : "memory");
        TS(7, 2 * c + 1);
    }
    p1[3] = cvtpk(tt[2], tt[3]);
    o = MFMA32(v1, asb(p1), o); l = MFMA16(sel, asb(p1), l);
    TS(2, 2);
    {
        bool bad = !(l[0] < 1.0e30f);
#pragma unroll
        for (int r = 0; r < 16; ++r) bad |= !(fabsf(o[r]) < 3.0e38f);
        if (__any(bad)) {
            const char* kb = kv + lane * 16;
            float mx = -3.0e38f;
            for (int t = 0; t < 64; ++t) {
                f32x16 s = {};
                s = MFMA32(*(const bf16x8*)(kb + t * 4096), qf0, s); s = MFMA32(*(const bf16x8*)(kb + t * 4096 + 1024), qf1, s);
#pragma unroll
                for (int r = 0; r < 16; ++r) mx = fmaxf(mx, s[r]);
            }
            mx = swapmax(mx);
#pragma unroll
            for (int r = 0; r < 16; ++r) { negm[r] = -mx; o[r] = 0.f; }
            l = f32x4{};
            for (int t = 0; t < 64; ++t) {
                f32x16 s = MFMA32(*(const bf16x8*)(kb + t * 4096), qf0, negm); s = MFMA32(*(const bf16x8*)(kb + t * 4096 + 1024), qf1, s);
#pragma unroll
                for (int j = 0; j < 4; ++j) {
                    p0[j] = cvtpk(__builtin_amdgcn_exp2f(s[2 * j]), __builtin_amdgcn_exp2f(s[2 * j + 1]));
                    p1[j] = cvtpk(__builtin_amdgcn_exp2f(s[8 + 2 * j]), __builtin_amdgcn_exp2f(s[9 + 2 * j]));
                }
                o = MFMA32(*(const bf16x8*)(kb + ((t + 1) & 63) * 4096 + 2048), asb(p0), o); o = MFMA32(*(const bf16x8*)(kb + ((t + 2) & 63) * 4096 + 3072), asb(p1), o);
                l = MFMA16(sel, asb(p0), l); l = MFMA16(sel, asb(p1), l);
            }
        }
    }
    const float rl = 1.0f / l[0];
    u32x4 o0, o1;
#pragma unroll
    for (int j = 0; j < 4; ++j) { o0[j] = cvtpk(o[2 * j] * rl, o[2 * j + 1] * rl); o1[j] = cvtpk(o[8 + 2 * j] * rl, o[9 + 2 * j] * rl); }
    char* op = ws + WS_O + ((size_t)(tg * 2 + (wid & 1)) * 64 + (hg * 4 + (wid >> 1)) * 2) * 1024 + lane * 16;
    *(u32x4*)op = o0; *(u32x4*)(op + 1024) = o1;
    TS(2, 3);
}

constexpr int K4_LDS = 4 * 24576 + 2048;
__global__ __launch_bounds__(512) void k_oproj(const char* __restrict__ wo, const char* __restrict__ x, char* __restrict__ ws) {
    extern __shared__ __attribute__((aligned(16))) char smem[];
    const unsigned lds0 = (unsigned)(size_t)(LAS char*)smem;
    const int tid = threadIdx.x, lane = tid & 63, wid = __builtin_amdgcn_readfirstlane(tid >> 6);
    const int xcd = blockIdx.x & 7, idx = blockIdx.x >> 3;
    const int tg = (xcd >> 1) * 8 + (idx & 7), ng = (xcd & 1) * 4 + (idx >> 3);
    TS(3, 0);
    f32x16 acc[1] = {};
    tgemm<2, 1, 16, 3>(acc, lds0, ws + WS_O + (size_t)tg * 2 * 65536, 65536, wo + ng * 256, wid, lane, [&](unsigned lb) {
        const int r = wid * 4 + (lane >> 4);
        const unsigned vo = (unsigned)r * 2048 + (unsigned)(((lane & 15) ^ (r & 15)) * 16);
        const char* xb = x + (size_t)tg * 64 * 2048 + ng * 256;
        glds16(xb, vo, lb + wid * 1024); glds16(xb + 32 * 2048, vo, lb + 8192 + wid * 1024);
        glds16(ws + WS_MODF + ng * 512, (lane >> 5) * 8192 + (lane & 31) * 16, lb + 16384);
    });
    TS(3, 1);
    const int hi = lane >> 5, m = lane & 31, rb = wid & 1, cb = wid >> 1;
    float xo[16];
    u32x4 x1v[2];
#pragma unroll
    for (int g = 0; g < 4; ++g) {
        const u32x2 xv = *(const LAS u32x2*)(size_t)(lds0 + (rb * 32 + m) * 256 + (((cb * 4 + g) ^ (m & 15)) * 16) + hi * 8);
        const f32x4 ga = ldsf4(lds0 + 16384 + (cb * 32 + g * 8 + hi * 4) * 4);
        const unsigned a = cvtpk(bflo(xv[0]) + ga[0] * acc[0][4 * g], bfhi(xv[0]) + ga[1] * acc[0][4 * g + 1]);
        const unsigned b = cvtpk(bflo(xv[1]) + ga[2] * acc[0][4 * g + 2], bfhi(xv[1]) + ga[3] * acc[0][4 * g + 3]);
        x1v[g >> 1][2 * (g & 1)] = a; x1v[g >> 1][2 * (g & 1) + 1] = b;
        xo[4 * g] = bflo(a); xo[4 * g + 1] = bfhi(a); xo[4 * g + 2] = bflo(b); xo[4 * g + 3] = bfhi(b);
    }
    const int grb = tg * 2 + rb, gcb = ng * 4 + cb;
    char* xp = ws + WS_X1 + ((size_t)grb * 64 + gcb * 2) * 1024 + lane * 16;
    *(u32x4*)xp = x1v[0]; *(u32x4*)(xp + 1024) = x1v[1];
    char* yp = ws + WS_Y + ((size_t)(grb * 2 + (m >> 4)) * 32 + gcb) * 1024 + ((m & 15) + 16 * hi) * 16;
#pragma unroll
    for (int s = 0; s < 2; ++s) {
        u32x4 yv;
#pragma unroll
        for (int g2 = 0; g2 < 2; ++g2) {
            const int g = 2 * s + g2;
            const f32x4 cbv = ldsf4(lds0 + 16384 + 512 + (cb * 32 + g * 8 + hi * 4) * 4);
            yv[2 * g2] = cvtpk(xo[4 * g] * cbv[0], xo[4 * g + 1] * cbv[1]); yv[2 * g2 + 1] = cvtpk(xo[4 * g + 2] * cbv[2], xo[4 * g + 3] * cbv[3]);
        }
        *(u32x4*)(yp + s * 512) = yv;
    }
    float sm = 0.f;
#pragma unroll
    for (int r = 0; r < 16; ++r) sm += xo[r];
    const float mw = swapadd(sm) * (1.f / 32.f);
    float q = 0.f;
#pragma unroll
    for (int r = 0; r < 16; ++r) { const float d = xo[r] - mw; q += d * d; }
    q = swapadd(q);
    LAS f32x2* st = (LAS f32x2*)(size_t)(lds0 + 4 * 24576);
    if (hi == 0) st[(rb * 32 + m) * 4 + cb] = f32x2{mw, q};
    WAITL0(); BAR();
    if (tid < 64) {
        const f32x4 a = *(LAS f32x4*)(st + tid * 4), b = *(LAS f32x4*)(st + tid * 4 + 2);
        const float mean = 0.25f * ((a[0] + a[2]) + (b[0] + b[2]));
        const float d0 = a[0] - mean, d1 = a[2] - mean, d2 = b[0] - mean, d3 = b[2] - mean;
        ((f32x2*)(ws + WS_STAT))[(tg * 64 + tid) * 8 + ng] = f32x2{mean, (a[1] + a[3]) + (b[1] + b[3]) + 32.f * ((d0 * d0 + d1 * d1) + (d2 * d2 + d3 * d3))};
    }
    TS(3, 2);
}

constexpr unsigned K5_SLOT = 30720;
constexpr int K5_LDS = 163840;
DI float gelu_mul(float g, float u) {
    const float e = __builtin_amdgcn_exp2f(g * (-2.3022082f - 0.10294324f * g * g));
    return g * __builtin_amdgcn_rcpf(1.f + e) * u;
}
__global__ __launch_bounds__(512) void k_gateup(char* __restrict__ ws) {
    extern __shared__ __attribute__((aligned(16))) char smem[];
    const unsigned lds0 = (unsigned)(size_t)(LAS char*)smem;
    const int tid = threadIdx.x, lane = tid & 63, wid = __builtin_amdgcn_readfirstlane(tid >> 6);
    const int xcd = blockIdx.x & 7, idx = blockIdx.x >> 3;
    const int nt = xcd * 4 + (idx & 3), mt = idx >> 2;
    const int wm = wid >> 1, wc = wid & 1;
    const unsigned wmask = (unsigned)((wid - 7) >> 31);
    const char* xs0 = ws + WS_Y + (size_t)(mt * 16 + wid * 2) * 32768; const char* xs1 = xs0 + 32768;
    const char* ws0 = ws + WS_WGU + (size_t)nt * 32 * 14336 + ((wid * 2048) & wmask); const char* ws1 = ws0 + 1024;
    const unsigned xvo = lane * 16, wvo = (lane * 16) & wmask;
    const unsigned dx = lds0 + wid * 2048, dw = lds0 + (((16384 + wid * 2048) & wmask) | ((5 * K5_SLOT) & ~wmask)), wstep = 14336 & wmask, w2 = 1024 & wmask;
    TS(5, 0);
#pragma unroll
    for (int i = 0; i < 5; ++i) {
        glds16(xs0, xvo, dx + i * K5_SLOT); glds16(xs1, xvo, dx + i * K5_SLOT + 1024);
        glds16(ws0, wvo, dw + ((i * K5_SLOT) & wmask)); glds16(ws1, wvo, dw + ((i * K5_SLOT) & wmask) + w2);
        xs0 += 1024; xs1 += 1024; ws0 += wstep; ws1 += wstep;
    }
    const char* es0 = ws + WS_STAT + (size_t)mt * 16384 + wid * 2048; const char* es1 = es0 + 1024;
    const char* es2 = ws + WS_UT + (size_t)nt * 7168 + ((wid * 1024) & wmask);
    const unsigned xa = lds0 + wm * 4096 + lane * 16, wa = lds0 + 16384 + wc * 7168 + lane * 16;
    f32x16 acc[7];
#pragma unroll
    for (int n = 0; n < 7; ++n) acc[n] = f32x16{};
    unsigned w2v = w2;
    asm volatile(
    "s_waitcnt vmcnt(16)\n\t"
    "s_barrier\n\t"
    "ds_read_b128 v[112:115], v201 offset:0\n\t"
    "ds_read_b128 v[116:119], v201 offset:1024\n\t"
    "ds_read_b128 v[120:123], v201 offset:2048\n\t"
    "ds_read_b128 v[124:127], v201 offset:3072\n\t"
    "ds_read_b128 v[128:131], v202 offset:0\n\t"
    "ds_read_b128 v[132:135], v202 offset:1024\n\t"
    "ds_read_b128 v[136:139], v202 offset:2048\n\t"
    "ds_read_b128 v[140:143], v202 offset:3072\n\t"
    "ds_read_b128 v[144:147], v202 offset:4096\n\t"
    "ds_read_b128 v[148:151], v202 offset:5120\n\t"
    "ds_read_b128 v[152:155], v202 offset:6144\n\t"
    "s_mov_b32 s50, 30720\n\t"
    "s_mov_b32 s51, 0\n\t"
    "s_mov_b32 s52, 13\n\t"
    "1:\n\t"
    "v_add_u32 v203, s50, v201\n\t"
    "v_add_u32 v204, s50, v202\n\t"
    "s_waitcnt vmcnt(12) lgkmcnt(0)\n\t"
    "s_barrier\n\t"
    "v_mfma_f32_16x16x32_bf16 v[0:3], v[128:131], v[112:115], v[0:3]\n\t"
    "ds_read_b128 v[172:175], v204\n\t"
    "v_mfma_f32_16x16x32_bf16 v[4:7], v[128:131], v[116:119], v[4:7]\n\t"
    "ds_read_b128 v[156:159], v203 offset:0\n\t"
    "v_mfma_f32_16x16x32_bf16 v[8:11], v[128:131], v[120:123], v[8:11]\n\t"
    "ds_read_b128 v[160:163], v203 offset:1024\n\t"
    "v_mfma_f32_16x16x32_bf16 v[12:15], v[128:131], v[124:127], v[12:15]\n\t"
    "ds_read_b128 v[164:167], v203 offset:2048\n\t"
    "v_mfma_f32_16x16x32_bf16 v[16:19], v[132:135], v[112:115], v[16:19]\n\t"
    "ds_read_b128 v[168:171], v203 offset:3072\n\t"
    "v_mfma_f32_16x16x32_bf16 v[20:23], v[132:135], v[116:119], v[20:23]\n\t"
    "ds_read_b128 v[176:179], v204 offset:1024\n\t"
    "v_mfma_f32_16x16x32_bf16 v[24:27], v[132:135], v[120:123], v[24:27]\n\t"
    "ds_read_b128 v[180:183], v204 offset:2048\n\t"
    "v_mfma_f32_16x16x32_bf16 v[28:31], v[132:135], v[124:127], v[28:31]\n\t"
    "ds_read_b128 v[184:187], v204 offset:3072\n\t"
    "v_mfma_f32_16x16x32_bf16 v[32:35], v[136:139], v[112:115], v[32:35]\n\t"
    "ds_read_b128 v[188:191], v204 offset:4096\n\t"
    "v_mfma_f32_16x16x32_bf16 v[36:39], v[136:139], v[116:119], v[36:39]\n\t"
    "ds_read_b128 v[192:195], v204 offset:5120\n\t"
    "v_mfma_f32_16x16x32_bf16 v[40:43], v[136:139], v[120:123], v[40:43]\n\t"
    "ds_read_b128 v[196:199], v204 offset:6144\n\t"
    "v_mfma_f32_16x16x32_bf16 v[44:47], v[136:139], v[124:127], v[44:47]\n\t"
    "s_add_u32 m0, s51, s48\n\t"
    "v_mfma_f32_16x16x32_bf16 v[48:51], v[140:143], v[112:115], v[48:51]\n\t"
    "global_load_lds_dwordx4 v200, s[40:41]\n\t"
    "s_add_u32 s40, s40, 0x400\n\t"
    "s_addc_u32 s41, s41, 0\n\t"
    "v_mfma_f32_16x16x32_bf16 v[52:55], v[140:143], v[116:119], v[52:55]\n\t"
    "s_add_u32 m0, m0, 0x400\n\t"
    "v_mfma_f32_16x16x32_bf16 v[56:59], v[140:143], v[120:123], v[56:59]\n\t"
    "global_load_lds_dwordx4 v200, s[42:43]\n\t"
    "s_add_u32 s42, s42, 0x400\n\t"
    "s_addc_u32 s43, s43, 0\n\t"
    "v_mfma_f32_16x16x32_bf16 v[60:63], v[140:143], v[124:127], v[60:63]\n\t"
    "s_and_b32 s62, s51, s55\n\t"
    "s_add_u32 m0, s62, s49\n\t"
    "v_mfma_f32_16x16x32_bf16 v[64:67], v[144:147], v[112:115], v[64:67]\n\t"
    "global_load_lds_dwordx4 v205, s[44:45]\n\t"
    "s_add_u32 s44, s44, s53\n\t"
    "s_addc_u32 s45, s45, 0\n\t"
    "v_mfma_f32_16x16x32_bf16 v[68:71], v[144:147], v[116:119], v[68:71]\n\t"
    "s_add_u32 m0, m0, s54\n\t"
    "v_mfma_f32_16x16x32_bf16 v[72:75], v[144:147], v[120:123], v[72:75]\n\t"
    "global_load_lds_dwordx4 v205, s[46:47]\n\t"
    "s_add_u32 s46, s46, s53\n\t"
    "s_addc_u32 s47, s47, 0\n\t"
    "v_mfma_f32_16x16x32_bf16 v[76:79], v[144:147], v[124:127], v[76:79]\n\t"
    "s_mov_b32 s51, s50\n\t"
    "s_add_u32 s50, s50, 30720\n\t"
    "v_mfma_f32_16x16x32_bf16 v[80:83], v[148:151], v[112:115], v[80:83]\n\t"
    "s_cmp_eq_u32 s50, 153600\n\t"
    "s_cselect_b32 s50, 0, s50\n\t"
    "v_mfma_f32_16x16x32_bf16 v[84:87], v[148:151], v[116:119], v[84:87]\n\t"
    "v_mfma_f32_16x16x32_bf16 v[88:91], v[148:151], v[120:123], v[88:91]\n\t"
    "v_mfma_f32_16x16x32_bf16 v[92:95], v[148:151], v[124:127], v[92:95]\n\t"
    "v_mfma_f32_16x16x32_bf16 v[96:99], v[152:155], v[112:115], v[96:99]\n\t"
    "v_mfma_f32_16x16x32_bf16 v[100:103], v[152:155], v[116:119], v[100:103]\n\t"
    "v_mfma_f32_16x16x32_bf16 v[104:107], v[152:155], v[120:123], v[104:107]\n\t"
    "v_mfma_f32_16x16x32_bf16 v[108:111], v[152:155], v[124:127], v[108:111]\n\t"
    "v_add_u32 v203, s50, v201\n\t"
    "v_add_u32 v204, s50, v202\n\t"
    "s_waitcnt vmcnt(12) lgkmcnt(0)\n\t"
    "s_barrier\n\t"
    "v_mfma_f32_16x16x32_bf16 v[0:3], v[172:175], v[156:159], v[0:3]\n\t"
    "ds_read_b128 v[128:131], v204\n\t"
    "v_mfma_f32_16x16x32_bf16 v[4:7], v[172:175], v[160:163], v[4:7]\n\t"
    "ds_read_b128 v[112:115], v203 offset:0\n\t"
    "v_mfma_f32_16x16x32_bf16 v[8:11], v[172:175], v[164:167], v[8:11]\n\t"
    "ds_read_b128 v[116:119], v203 offset:1024\n\t"
    "v_mfma_f32_16x16x32_bf16 v[12:15], v[172:175], v[168:171], v[12:15]\n\t"
    "ds_read_b128 v[120:123], v203 offset:2048\n\t"
    "v_mfma_f32_16x16x32_bf16 v[16:19], v[176:179], v[156:159], v[16:19]\n\t"
    "ds_read_b128 v[124:127], v203 offset:3072\n\t"
    "v_mfma_f32_16x16x32_bf16 v[20:23], v[176:179], v[160:163], v[20:23]\n\t"
    "ds_read_b128 v[132:135], v204 offset:1024\n\t"
    "v_mfma_f32_16x16x32_bf16 v[24:27], v[176:179], v[164:167], v[24:27]\n\t"
    "ds_read_b128 v[136:139], v204 offset:2048\n\t"
    "v_mfma_f32_16x16x32_bf16 v[28:31], v[176:179], v[168:171], v[28:31]\n\t"
    "ds_read_b128 v[140:143], v204 offset:3072\n\t"
    "v_mfma_f32_16x16x32_bf16 v[32:35], v[180:183], v[156:159], v[32:35]\n\t"
    "ds_read_b128 v[144:147], v204 offset:4096\n\t"
    "v_mfma_f32_16x16x32_bf16 v[36:39], v[180:183], v[160:163], v[36:39]\n\t"
    "ds_read_b128 v[148:151], v204 offset:5120\n\t"
    "v_mfma_f32_16x16x32_bf16 v[40:43], v[180:183], v[164:167], v[40:43]\n\t"
    "ds_read_b128 v[152:155], v204 offset:6144\n\t"
    "v_mfma_f32_16x16x32_bf16 v[44:47], v[180:183], v[168:171], v[44:47]\n\t"
    "s_add_u32 m0, s51, s48\n\t"
    "v_mfma_f32_16x16x32_bf16 v[48:51], v[184:187], v[156:159], v[48:51]\n\t"
    "global_load_lds_dwordx4 v200, s[40:41]\n\t"
    "s_add_u32 s40, s40, 0x400\n\t"
    "s_addc_u32 s41, s41, 0\n\t"
    "v_mfma_f32_16x16x32_bf16 v[52:55], v[184:187], v[160:163], v[52:55]\n\t"
    "s_add_u32 m0, m0, 0x400\n\t"
    "v_mfma_f32_16x16x32_bf16 v[56:59], v[184:187], v[164:167], v[56:59]\n\t"
    "global_load_lds_dwordx4 v200, s[42:43]\n\t"
    "s_add_u32 s42, s42, 0x400\n\t"
    "s_addc_u32 s43, s43, 0\n\t"
    "v_mfma_f32_16x16x32_bf16 v[60:63], v[184:187], v[168:171], v[60:63]\n\t"
    "s_and_b32 s62, s51, s55\n\t"
    "s_add_u32 m0, s62, s49\n\t"
    "v_mfma_f32_16x16x32_bf16 v[64:67], v[188:191], v[156:159], v[64:67]\n\t"
    "global_load_lds_dwordx4 v205, s[44:45]\n\t"
    "s_add_u32 s44, s44, s53\n\t"
    "s_addc_u32 s45, s45, 0\n\t"
    "v_mfma_f32_16x16x32_bf16 v[68:71], v[188:191], v[160:163], v[68:71]\n\t"
    "s_add_u32 m0, m0, s54\n\t"
    "v_mfma_f32_16x16x32_bf16 v[72:75], v[188:191], v[164:167], v[72:75]\n\t"
    "global_load_lds_dwordx4 v205, s[46:47]\n\t"
    "s_add_u32 s46, s46, s53\n\t"
    "s_addc_u32 s47, s47, 0\n\t"
    "v_mfma_f32_16x16x32_bf16 v[76:79], v[188:191], v[168:171], v[76:79]\n\t"
    "s_mov_b32 s51, s50\n\t"
    "s_add_u32 s50, s50, 30720\n\t"
    "v_mfma_f32_16x16x32_bf16 v[80:83], v[192:195], v[156:159], v[80:83]\n\t"
    "s_cmp_eq_u32 s50, 153600\n\t"
    "s_cselect_b32 s50, 0, s50\n\t"
    "v_mfma_f32_16x16x32_bf16 v[84:87], v[192:195], v[160:163], v[84:87]\n\t"
    "v_mfma_f32_16x16x32_bf16 v[88:91], v[192:195], v[164:167], v[88:91]\n\t"
    "v_mfma_f32_16x16x32_bf16 v[92:95], v[192:195], v[168:171], v[92:95]\n\t"
    "v_mfma_f32_16x16x32_bf16 v[96:99], v[196:199], v[156:159], v[96:99]\n\t"
    "v_mfma_f32_16x16x32_bf16 v[100:103], v[196:199], v[160:163], v[100:103]\n\t"
    "v_mfma_f32_16x16x32_bf16 v[104:107], v[196:199], v[164:167], v[104:107]\n\t"
    "v_mfma_f32_16x16x32_bf16 v[108:111], v[196:199], v[168:171], v[108:111]\n\t"
    "s_sub_u32 s52, s52, 1\n\t"
    "s_cmp_lg_u32 s52, 0\n\t"
    "s_cbranch_scc1 1b\n\t"
    "v_add_u32 v203, s50, v201\n\t"
    "v_add_u32 v204, s50, v202\n\t"
    "s_waitcnt vmcnt(12) lgkmcnt(0)\n\t"
    "s_barrier\n\t"
    "v_mfma_f32_16x16x32_bf16 v[0:3], v[128:131], v[112:115], v[0:3]\n\t"
    "ds_read_b128 v[172:175], v204\n\t"
    "v_mfma_f32_16x16x32_bf16 v[4:7], v[128:131], v[116:119], v[4:7]\n\t"
    "ds_read_b128 v[156:159], v203 offset:0\n\t"
    "v_mfma_f32_16x16x32_bf16 v[8:11], v[128:131], v[120:123], v[8:11]\n\t"
    "ds_read_b128 v[160:163], v203 offset:1024\n\t"
    "v_mfma_f32_16x16x32_bf16 v[12:15], v[128:131], v[124:127], v[12:15]\n\t"
    "ds_read_b128 v[164:167], v203 offset:2048\n\t"
    "v_mfma_f32_16x16x32_bf16 v[16:19], v[132:135], v[112:115], v[16:19]\n\t"
    "ds_read_b128 v[168:171], v203 offset:3072\n\t"
    "v_mfma_f32_16x16x32_bf16 v[20:23], v[132:135], v[116:119], v[20:23]\n\t"
    "ds_read_b128 v[176:179], v204 offset:1024\n\t"
    "v_mfma_f32_16x16x32_bf16 v[24:27], v[132:135], v[120:123], v[24:27]\n\t"
    "ds_read_b128 v[180:183], v204 offset:2048\n\t"
    "v_mfma_f32_16x16x32_bf16 v[28:31], v[132:135], v[124:127], v[28:31]\n\t"
    "ds_read_b128 v[184:187], v204 offset:3072\n\t"
    "v_mfma_f32_16x16x32_bf16 v[32:35], v[136:139], v[112:115], v[32:35]\n\t"
    "ds_read_b128 v[188:191], v204 offset:4096\n\t"
    "v_mfma_f32_16x16x32_bf16 v[36:39], v[136:139], v[116:119], v[36:39]\n\t"
    "ds_read_b128 v[192:195], v204 offset:5120\n\t"
    "v_mfma_f32_16x16x32_bf16 v[40:43], v[136:139], v[120:123], v[40:43]\n\t"
    "ds_read_b128 v[196:199], v204 offset:6144\n\t"
    "v_mfma_f32_16x16x32_bf16 v[44:47], v[136:139], v[124:127], v[44:47]\n\t"
    "s_add_u32 m0, s51, s48\n\t"
    "v_mfma_f32_16x16x32_bf16 v[48:51], v[140:143], v[112:115], v[48:51]\n\t"
    "global_load_lds_dwordx4 v200, s[40:41]\n\t"
    "s_add_u32 s40, s40, 0x400\n\t"
    "s_addc_u32 s41, s41, 0\n\t"
    "v_mfma_f32_16x16x32_bf16 v[52:55], v[140:143], v[116:119], v[52:55]\n\t"
    "s_add_u32 m0, m0, 0x400\n\t"
    "v_mfma_f32_16x16x32_bf16 v[56:59], v[140:143], v[120:123], v[56:59]\n\t"
    "global_load_lds_dwordx4 v200, s[42:43]\n\t"
    "s_add_u32 s42, s42, 0x400\n\t"
    "s_addc_u32 s43, s43, 0\n\t"
    "v_mfma_f32_16x16x32_bf16 v[60:63], v[140:143], v[124:127], v[60:63]\n\t"
    "s_and_b32 s62, s51, s55\n\t"
    "s_add_u32 m0, s62, s49\n\t"
    "v_mfma_f32_16x16x32_bf16 v[64:67], v[144:147], v[112:115], v[64:67]\n\t"
    "global_load_lds_dwordx4 v205, s[44:45]\n\t"
    "s_add_u32 s44, s44, s53\n\t"
    "s_addc_u32 s45, s45, 0\n\t"
    "v_mfma_f32_16x16x32_bf16 v[68:71], v[144:147], v[116:119], v[68:71]\n\t"
    "s_add_u32 m0, m0, s54\n\t"
    "v_mfma_f32_16x16x32_bf16 v[72:75], v[144:147], v[120:123], v[72:75]\n\t"
    "global_load_lds_dwordx4 v205, s[46:47]\n\t"
    "s_add_u32 s46, s46, s53\n\t"
    "s_addc_u32 s47, s47, 0\n\t"
    "v_mfma_f32_16x16x32_bf16 v[76:79], v[144:147], v[124:127], v[76:79]\n\t"
    "s_mov_b32 s51, s50\n\t"
    "s_add_u32 s50, s50, 30720\n\t"
    "v_mfma_f32_16x16x32_bf16 v[80:83], v[148:151], v[112:115], v[80:83]\n\t"
    "s_cmp_eq_u32 s50, 153600\n\t"
    "s_cselect_b32 s50, 0, s50\n\t"
    "v_mfma_f32_16x16x32_bf16 v[84:87], v[148:151], v[116:119], v[84:87]\n\t"
    "v_mfma_f32_16x16x32_bf16 v[88:91], v[148:151], v[120:123], v[88:91]\n\t"
    "v_mfma_f32_16x16x32_bf16 v[92:95], v[148:151], v[124:127], v[92:95]\n\t"
    "v_mfma_f32_16x16x32_bf16 v[96:99], v[152:155], v[112:115], v[96:99]\n\t"
    "v_mfma_f32_16x16x32_bf16 v[100:103], v[152:155], v[116:119], v[100:103]\n\t"
    "v_mfma_f32_16x16x32_bf16 v[104:107], v[152:155], v[120:123], v[104:107]\n\t"
    "v_mfma_f32_16x16x32_bf16 v[108:111], v[152:155], v[124:127], v[108:111]\n\t"
    "s_mov_b64 s[40:41], s[56:57]\n\t"
    "s_mov_b64 s[42:43], s[58:59]\n\t"
    "s_mov_b64 s[44:45], s[60:61]\n\t"
    "s_mov_b64 s[46:47], s[60:61]\n\t"
    "s_mov_b32 s54, 0\n\t"
    "v_add_u32 v203, s50, v201\n\t"
    "v_add_u32 v204, s50, v202\n\t"
    "s_waitcnt vmcnt(12) lgkmcnt(0)\n\t"
    "s_barrier\n\t"
    "v_mfma_f32_16x16x32_bf16 v[0:3], v[172:175], v[156:159], v[0:3]\n\t"
    "ds_read_b128 v[128:131], v204\n\t"
    "v_mfma_f32_16x16x32_bf16 v[4:7], v[172:175], v[160:163], v[4:7]\n\t"
    "ds_read_b128 v[112:115], v203 offset:0\n\t"
    "v_mfma_f32_16x16x32_bf16 v[8:11], v[172:175], v[164:167], v[8:11]\n\t"
    "ds_read_b128 v[116:119], v203 offset:1024\n\t"
    "v_mfma_f32_16x16x32_bf16 v[12:15], v[172:175], v[168:171], v[12:15]\n\t"
    "ds_read_b128 v[120:123], v203 offset:2048\n\t"
    "v_mfma_f32_16x16x32_bf16 v[16:19], v[176:179], v[156:159], v[16:19]\n\t"
    "ds_read_b128 v[124:127], v203 offset:3072\n\t"
    "v_mfma_f32_16x16x32_bf16 v[20:23], v[176:179], v[160:163], v[20:23]\n\t"
    "ds_read_b128 v[132:135], v204 offset:1024\n\t"
    "v_mfma_f32_16x16x32_bf16 v[24:27], v[176:179], v[164:167], v[24:27]\n\t"
    "ds_read_b128 v[136:139], v204 offset:2048\n\t"
    "v_mfma_f32_16x16x32_bf16 v[28:31], v[176:179], v[168:171], v[28:31]\n\t"
    "ds_read_b128 v[140:143], v204 offset:3072\n\t"
    "v_mfma_f32_16x16x32_bf16 v[32:35], v[180:183], v[156:159], v[32:35]\n\t"
    "ds_read_b128 v[144:147], v204 offset:4096\n\t"
    "v_mfma_f32_16x16x32_bf16 v[36:39], v[180:183], v[160:163], v[36:39]\n\t"
    "ds_read_b128 v[148:151], v204 offset:5120\n\t"
    "v_mfma_f32_16x16x32_bf16 v[40:43], v[180:183], v[164:167], v[40:43]\n\t"
    "ds_read_b128 v[152:155], v204 offset:6144\n\t"
    "v_mfma_f32_16x16x32_bf16 v[44:47], v[180:183], v[168:171], v[44:47]\n\t"
    "s_add_u32 m0, s51, s48\n\t"
    "v_mfma_f32_16x16x32_bf16 v[48:51], v[184:187], v[156:159], v[48:51]\n\t"
    "global_load_lds_dwordx4 v200, s[40:41]\n\t"
    "s_add_u32 s40, s40, 0x400\n\t"
    "s_addc_u32 s41, s41, 0\n\t"
    "v_mfma_f32_16x16x32_bf16 v[52:55], v[184:187], v[160:163], v[52:55]\n\t"
    "s_add_u32 m0, m0, 0x400\n\t"
    "v_mfma_f32_16x16x32_bf16 v[56:59], v[184:187], v[164:167], v[56:59]\n\t"
    "global_load_lds_dwordx4 v200, s[42:43]\n\t"
    "s_add_u32 s42, s42, 0x400\n\t"
    "s_addc_u32 s43, s43, 0\n\t"
    "v_mfma_f32_16x16x32_bf16 v[60:63], v[184:187], v[168:171], v[60:63]\n\t"
    "s_and_b32 s62, s51, s55\n\t"
    "s_add_u32 m0, s62, s49\n\t"
    "v_mfma_f32_16x16x32_bf16 v[64:67], v[188:191], v[156:159], v[64:67]\n\t"
    "global_load_lds_dwordx4 v205, s[44:45]\n\t"
    "s_add_u32 s44, s44, s53\n\t"
    "s_addc_u32 s45, s45, 0\n\t"
    "v_mfma_f32_16x16x32_bf16 v[68:71], v[188:191], v[160:163], v[68:71]\n\t"
    "s_add_u32 m0, m0, s54\n\t"
    "v_mfma_f32_16x16x32_bf16 v[72:75], v[188:191], v[164:167], v[72:75]\n\t"
    "global_load_lds_dwordx4 v205, s[46:47]\n\t"
    "s_add_u32 s46, s46, s53\n\t"
    "s_addc_u32 s47, s47, 0\n\t"
    "v_mfma_f32_16x16x32_bf16 v[76:79], v[188:191], v[168:171], v[76:79]\n\t"
    "s_mov_b32 s51, s50\n\t"
    "s_add_u32 s50, s50, 30720\n\t"
    "v_mfma_f32_16x16x32_bf16 v[80:83], v[192:195], v[156:159], v[80:83]\n\t"
    "s_cmp_eq_u32 s50, 153600\n\t"
    "s_cselect_b32 s50, 0, s50\n\t"
    "v_mfma_f32_16x16x32_bf16 v[84:87], v[192:195], v[160:163], v[84:87]\n\t"
    "v_mfma_f32_16x16x32_bf16 v[88:91], v[192:195], v[164:167], v[88:91]\n\t"
    "v_mfma_f32_16x16x32_bf16 v[92:95], v[192:195], v[168:171], v[92:95]\n\t"
    "v_mfma_f32_16x16x32_bf16 v[96:99], v[196:199], v[156:159], v[96:99]\n\t"
    "v_mfma_f32_16x16x32_bf16 v[100:103], v[196:199], v[160:163], v[100:103]\n\t"
    "v_mfma_f32_16x16x32_bf16 v[104:107], v[196:199], v[164:167], v[104:107]\n\t"
    "v_mfma_f32_16x16x32_bf16 v[108:111], v[196:199], v[168:171], v[108:111]\n\t"
    "v_add_u32 v203, s50, v201\n\t"
    "v_add_u32 v204, s50, v202\n\t"
    "s_waitcnt vmcnt(12) lgkmcnt(0)\n\t"
    "s_barrier\n\t"
    "v_mfma_f32_16x16x32_bf16 v[0:3], v[128:131], v[112:115], v[0:3]\n\t"
    "ds_read_b128 v[172:175], v204\n\t"
    "v_mfma_f32_16x16x32_bf16 v[4:7], v[128:131], v[116:119], v[4:7]\n\t"
    "ds_read_b128 v[156:159], v203 offset:0\n\t"
    "v_mfma_f32_16x16x32_bf16 v[8:11], v[128:131], v[120:123], v[8:11]\n\t"
    "ds_read_b128 v[160:163], v203 offset:1024\n\t"
    "v_mfma_f32_16x16x32_bf16 v[12:15], v[128:131], v[124:127], v[12:15]\n\t"
    "ds_read_b128 v[164:167], v203 offset:2048\n\t"
    "v_mfma_f32_16x16x32_bf16 v[16:19], v[132:135], v[112:115], v[16:19]\n\t"
    "ds_read_b128 v[168:171], v203 offset:3072\n\t"
    "v_mfma_f32_16x16x32_bf16 v[20:23], v[132:135], v[116:119], v[20:23]\n\t"
    "ds_read_b128 v[176:179], v204 offset:1024\n\t"
    "v_mfma_f32_16x16x32_bf16 v[24:27], v[132:135], v[120:123], v[24:27]\n\t"
    "ds_read_b128 v[180:183], v204 offset:2048\n\t"
    "v_mfma_f32_16x16x32_bf16 v[28:31], v[132:135], v[124:127], v[28:31]\n\t"
    "ds_read_b128 v[184:187], v204 offset:3072\n\t"
    "v_mfma_f32_16x16x32_bf16 v[32:35], v[136:139], v[112:115], v[32:35]\n\t"
    "ds_read_b128 v[188:191], v204 offset:4096\n\t"
    "v_mfma_f32_16x16x32_bf16 v[36:39], v[136:139], v[116:119], v[36:39]\n\t"
    "ds_read_b128 v[192:195], v204 offset:5120\n\t"
    "v_mfma_f32_16x16x32_bf16 v[40:43], v[136:139], v[120:123], v[40:43]\n\t"
    "ds_read_b128 v[196:199], v204 offset:6144\n\t"
    "v_mfma_f32_16x16x32_bf16 v[44:47], v[136:139], v[124:127], v[44:47]\n\t"
    "s_mov_b32 s51, s50\n\t"
    "s_add_u32 s50, s50, 30720\n\t"
    "v_mfma_f32_16x16x32_bf16 v[48:51], v[140:143], v[112:115], v[48:51]\n\t"
    "s_cmp_eq_u32 s50, 153600\n\t"
    "s_cselect_b32 s50, 0, s50\n\t"
    "v_mfma_f32_16x16x32_bf16 v[52:55], v[140:143], v[116:119], v[52:55]\n\t"
    "v_mfma_f32_16x16x32_bf16 v[56:59], v[140:143], v[120:123], v[56:59]\n\t"
    "v_mfma_f32_16x16x32_bf16 v[60:63], v[140:143], v[124:127], v[60:63]\n\t"
    "v_mfma_f32_16x16x32_bf16 v[64:67], v[144:147], v[112:115], v[64:67]\n\t"
    "v_mfma_f32_16x16x32_bf16 v[68:71], v[144:147], v[116:119], v[68:71]\n\t"
    "v_mfma_f32_16x16x32_bf16 v[72:75], v[144:147], v[120:123], v[72:75]\n\t"
    "v_mfma_f32_16x16x32_bf16 v[76:79], v[144:147], v[124:127], v[76:79]\n\t"
    "v_mfma_f32_16x16x32_bf16 v[80:83], v[148:151], v[112:115], v[80:83]\n\t"
    "v_mfma_f32_16x16x32_bf16 v[84:87], v[148:151], v[116:119], v[84:87]\n\t"
    "v_mfma_f32_16x16x32_bf16 v[88:91], v[148:151], v[120:123], v[88:91]\n\t"
    "v_mfma_f32_16x16x32_bf16 v[92:95], v[148:151], v[124:127], v[92:95]\n\t"
    "v_mfma_f32_16x16x32_bf16 v[96:99], v[152:155], v[112:115], v[96:99]\n\t"
    "v_mfma_f32_16x16x32_bf16 v[100:103], v[152:155], v[116:119], v[100:103]\n\t"
    "v_mfma_f32_16x16x32_bf16 v[104:107], v[152:155], v[120:123], v[104:107]\n\t"
    "v_mfma_f32_16x16x32_bf16 v[108:111], v[152:155], v[124:127], v[108:111]\n\t"
    "v_add_u32 v203, s50, v201\n\t"
    "v_add_u32 v204, s50, v202\n\t"
    "s_waitcnt vmcnt(8) lgkmcnt(0)\n\t"
    "s_barrier\n\t"
    "v_mfma_f32_16x16x32_bf16 v[0:3], v[172:175], v[156:159], v[0:3]\n\t"
    "ds_read_b128 v[128:131], v204\n\t"
    "v_mfma_f32_16x16x32_bf16 v[4:7], v[172:175], v[160:163], v[4:7]\n\t"
    "ds_read_b128 v[112:115], v203 offset:0\n\t"
    "v_mfma_f32_16x16x32_bf16 v[8:11], v[172:175], v[164:167], v[8:11]\n\t"
    "ds_read_b128 v[116:119], v203 offset:1024\n\t"
    "v_mfma_f32_16x16x32_bf16 v[12:15], v[172:175], v[168:171], v[12:15]\n\t"
    "ds_read_b128 v[120:123], v203 offset:2048\n\t"
    "v_mfma_f32_16x16x32_bf16 v[16:19], v[176:179], v[156:159], v[16:19]\n\t"
    "ds_read_b128 v[124:127], v203 offset:3072\n\t"
    "v_mfma_f32_16x16x32_bf16 v[20:23], v[176:179], v[160:163], v[20:23]\n\t"
    "ds_read_b128 v[132:135], v204 offset:1024\n\t"
    "v_mfma_f32_16x16x32_bf16 v[24:27], v[176:179], v[164:167], v[24:27]\n\t"
    "ds_read_b128 v[136:139], v204 offset:2048\n\t"
    "v_mfma_f32_16x16x32_bf16 v[28:31], v[176:179], v[168:171], v[28:31]\n\t"
    "ds_read_b128 v[140:143], v204 offset:3072\n\t"
    "v_mfma_f32_16x16x32_bf16 v[32:35], v[180:183], v[156:159], v[32:35]\n\t"
    "ds_read_b128 v[144:147], v204 offset:4096\n\t"
    "v_mfma_f32_16x16x32_bf16 v[36:39], v[180:183], v[160:163], v[36:39]\n\t"
    "ds_read_b128 v[148:151], v204 offset:5120\n\t"
    "v_mfma_f32_16x16x32_bf16 v[40:43], v[180:183], v[164:167], v[40:43]\n\t"
    "ds_read_b128 v[152:155], v204 offset:6144\n\t"
    "v_mfma_f32_16x16x32_bf16 v[44:47], v[180:183], v[168:171], v[44:47]\n\t"
    "s_mov_b32 s51, s50\n\t"
    "s_add_u32 s50, s50, 30720\n\t"
    "v_mfma_f32_16x16x32_bf16 v[48:51], v[184:187], v[156:159], v[48:51]\n\t"
    "s_cmp_eq_u32 s50, 153600\n\t"
    "s_cselect_b32 s50, 0, s50\n\t"
    "v_mfma_f32_16x16x32_bf16 v[52:55], v[184:187], v[160:163], v[52:55]\n\t"
    "v_mfma_f32_16x16x32_bf16 v[56:59], v[184:187], v[164:167], v[56:59]\n\t"
    "v_mfma_f32_16x16x32_bf16 v[60:63], v[184:187], v[168:171], v[60:63]\n\t"
    "v_mfma_f32_16x16x32_bf16 v[64:67], v[188:191], v[156:159], v[64:67]\n\t"
    "v_mfma_f32_16x16x32_bf16 v[68:71], v[188:191], v[160:163], v[68:71]\n\t"
    "v_mfma_f32_16x16x32_bf16 v[72:75], v[188:191], v[164:167], v[72:75]\n\t"
    "v_mfma_f32_16x16x32_bf16 v[76:79], v[188:191], v[168:171], v[76:79]\n\t"
    "v_mfma_f32_16x16x32_bf16 v[80:83], v[192:195], v[156:159], v[80:83]\n\t"
    "v_mfma_f32_16x16x32_bf16 v[84:87], v[192:195], v[160:163], v[84:87]\n\t"
    "v_mfma_f32_16x16x32_bf16 v[88:91], v[192:195], v[164:167], v[88:91]\n\t"
    "v_mfma_f32_16x16x32_bf16 v[92:95], v[192:195], v[168:171], v[92:95]\n\t"
    "v_mfma_f32_16x16x32_bf16 v[96:99], v[196:199], v[156:159], v[96:99]\n\t"
    "v_mfma_f32_16x16x32_bf16 v[100:103], v[196:199], v[160:163], v[100:103]\n\t"
    "v_mfma_f32_16x16x32_bf16 v[104:107], v[196:199], v[164:167], v[104:107]\n\t"
    "v_mfma_f32_16x16x32_bf16 v[108:111], v[196:199], v[168:171], v[108:111]\n\t"
    "v_add_u32 v203, s50, v201\n\t"
    "v_add_u32 v204, s50, v202\n\t"
    "s_waitcnt vmcnt(4) lgkmcnt(0)\n\t"
    "s_barrier\n\t"
    "v_mfma_f32_16x16x32_bf16 v[0:3], v[128:131], v[112:115], v[0:3]\n\t"
    "ds_read_b128 v[172:175], v204\n\t"
    "v_mfma_f32_16x16x32_bf16 v[4:7], v[128:131], v[116:119], v[4:7]\n\t"
    "ds_read_b128 v[156:159], v203 offset:0\n\t"
    "v_mfma_f32_16x16x32_bf16 v[8:11], v[128:131], v[120:123], v[8:11]\n\t"
    "ds_read_b128 v[160:163], v203 offset:1024\n\t"
    "v_mfma_f32_16x16x32_bf16 v[12:15], v[128:131], v[124:127], v[12:15]\n\t"
    "ds_read_b128 v[164:167], v203 offset:2048\n\t"
    "v_mfma_f32_16x16x32_bf16 v[16:19], v[132:135], v[112:115], v[16:19]\n\t"
    "ds_read_b128 v[168:171], v203 offset:3072\n\t"
    "v_mfma_f32_16x16x32_bf16 v[20:23], v[132:135], v[116:119], v[20:23]\n\t"
    "ds_read_b128 v[176:179], v204 offset:1024\n\t"
    "v_mfma_f32_16x16x32_bf16 v[24:27], v[132:135], v[120:123], v[24:27]\n\t"
    "ds_read_b128 v[180:183], v204 offset:2048\n\t"
    "v_mfma_f32_16x16x32_bf16 v[28:31], v[132:135], v[124:127], v[28:31]\n\t"
    "ds_read_b128 v[184:187], v204 offset:3072\n\t"
    "v_mfma_f32_16x16x32_bf16 v[32:35], v[136:139], v[112:115], v[32:35]\n\t"
    "ds_read_b128 v[188:191], v204 offset:4096\n\t"
    "v_mfma_f32_16x16x32_bf16 v[36:39], v[136:139], v[116:119], v[36:39]\n\t"
    "ds_read_b128 v[192:195], v204 offset:5120\n\t"
    "v_mfma_f32_16x16x32_bf16 v[40:43], v[136:139], v[120:123], v[40:43]\n\t"
    "ds_read_b128 v[196:199], v204 offset:6144\n\t"
    "v_mfma_f32_16x16x32_bf16 v[44:47], v[136:139], v[124:127], v[44:47]\n\t"
    "s_mov_b32 s51, s50\n\t"
    "s_add_u32 s50, s50, 30720\n\t"
    "v_mfma_f32_16x16x32_bf16 v[48:51], v[140:143], v[112:115], v[48:51]\n\t"
    "s_cmp_eq_u32 s50, 153600\n\t"
    "s_cselect_b32 s50, 0, s50\n\t"
    "v_mfma_f32_16x16x32_bf16 v[52:55], v[140:143], v[116:119], v[52:55]\n\t"
    "v_mfma_f32_16x16x32_bf16 v[56:59], v[140:143], v[120:123], v[56:59]\n\t"
    "v_mfma_f32_16x16x32_bf16 v[60:63], v[140:143], v[124:127], v[60:63]\n\t"
    "v_mfma_f32_16x16x32_bf16 v[64:67], v[144:147], v[112:115], v[64:67]\n\t"
    "v_mfma_f32_16x16x32_bf16 v[68:71], v[144:147], v[116:119], v[68:71]\n\t"
    "v_mfma_f32_16x16x32_bf16 v[72:75], v[144:147], v[120:123], v[72:75]\n\t"
    "v_mfma_f32_16x16x32_bf16 v[76:79], v[144:147], v[124:127], v[76:79]\n\t"
    "v_mfma_f32_16x16x32_bf16 v[80:83], v[148:151], v[112:115], v[80:83]\n\t"
    "v_mfma_f32_16x16x32_bf16 v[84:87], v[148:151], v[116:119], v[84:87]\n\t"
    "v_mfma_f32_16x16x32_bf16 v[88:91], v[148:151], v[120:123], v[88:91]\n\t"
    "v_mfma_f32_16x16x32_bf16 v[92:95], v[148:151], v[124:127], v[92:95]\n\t"
    "v_mfma_f32_16x16x32_bf16 v[96:99], v[152:155], v[112:115], v[96:99]\n\t"
    "v_mfma_f32_16x16x32_bf16 v[100:103], v[152:155], v[116:119], v[100:103]\n\t"
    "v_mfma_f32_16x16x32_bf16 v[104:107], v[152:155], v[120:123], v[104:107]\n\t"
    "v_mfma_f32_16x16x32_bf16 v[108:111], v[152:155], v[124:127], v[108:111]\n\t"
    "s_waitcnt vmcnt(0) lgkmcnt(0)\n\t"
    "s_barrier\n\t"
    "v_mfma_f32_16x16x32_bf16 v[0:3], v[172:175], v[156:159], v[0:3]\n\t"
    "v_mfma_f32_16x16x32_bf16 v[4:7], v[172:175], v[160:163], v[4:7]\n\t"
    "v_mfma_f32_16x16x32_bf16 v[8:11], v[172:175], v[164:167], v[8:11]\n\t"
    "v_mfma_f32_16x16x32_bf16 v[12:15], v[172:175], v[168:171], v[12:15]\n\t"
    "v_mfma_f32_16x16x32_bf16 v[16:19], v[176:179], v[156:159], v[16:19]\n\t"
    "v_mfma_f32_16x16x32_bf16 v[20:23], v[176:179], v[160:163], v[20:23]\n\t"
    "v_mfma_f32_16x16x32_bf16 v[24:27], v[176:179], v[164:167], v[24:27]\n\t"
    "v_mfma_f32_16x16x32_bf16 v[28:31], v[176:179], v[168:171], v[28:31]\n\t"
    "v_mfma_f32_16x16x32_bf16 v[32:35], v[180:183], v[156:159], v[32:35]\n\t"
    "v_mfma_f32_16x16x32_bf16 v[36:39], v[180:183], v[160:163], v[36:39]\n\t"
    "v_mfma_f32_16x16x32_bf16 v[40:43], v[180:183], v[164:167], v[40:43]\n\t"
    "v_mfma_f32_16x16x32_bf16 v[44:47], v[180:183], v[168:171], v[44:47]\n\t"
    "s_mov_b32 s51, s50\n\t"
    "s_add_u32 s50, s50, 30720\n\t"
    "v_mfma_f32_16x16x32_bf16 v[48:51], v[184:187], v[156:159], v[48:51]\n\t"
    "s_cmp_eq_u32 s50, 153600\n\t"
    "s_cselect_b32 s50, 0, s50\n\t"
    "v_mfma_f32_16x16x32_bf16 v[52:55], v[184:187], v[160:163], v[52:55]\n\t"
    "v_mfma_f32_16x16x32_bf16 v[56:59], v[184:187], v[164:167], v[56:59]\n\t"
    "v_mfma_f32_16x16x32_bf16 v[60:63], v[184:187], v[168:171], v[60:63]\n\t"
    "v_mfma_f32_16x16x32_bf16 v[64:67], v[188:191], v[156:159], v[64:67]\n\t"
    "v_mfma_f32_16x16x32_bf16 v[68:71], v[188:191], v[160:163], v[68:71]\n\t"
    "v_mfma_f32_16x16x32_bf16 v[72:75], v[188:191], v[164:167], v[72:75]\n\t"
    "v_mfma_f32_16x16x32_bf16 v[76:79], v[188:191], v[168:171], v[76:79]\n\t"
    "v_mfma_f32_16x16x32_bf16 v[80:83], v[192:195], v[156:159], v[80:83]\n\t"
    "v_mfma_f32_16x16x32_bf16 v[84:87], v[192:195], v[160:163], v[84:87]\n\t"
    "v_mfma_f32_16x16x32_bf16 v[88:91], v[192:195], v[164:167], v[88:91]\n\t"
    "v_mfma_f32_16x16x32_bf16 v[92:95], v[192:195], v[168:171], v[92:95]\n\t"
    "v_mfma_f32_16x16x32_bf16 v[96:99], v[196:199], v[156:159], v[96:99]\n\t"
    "v_mfma_f32_16x16x32_bf16 v[100:103], v[196:199], v[160:163], v[100:103]\n\t"
    "v_mfma_f32_16x16x32_bf16 v[104:107], v[196:199], v[164:167], v[104:107]\n\t"
    "v_mfma_f32_16x16x32_bf16 v[108:111], v[196:199], v[168:171], v[108:111]\n\t"
    "s_nop 7\n\t"
        ""
        : "+{v[0:15]}"(acc[0]), "+{v[16:31]}"(acc[1]), "+{v[32:47]}"(acc[2]), "+{v[48:63]}"(acc[3]), "+{v[64:79]}"(acc[4]), "+{v[80:95]}"(acc[5]), "+{v[96:111]}"(acc[6]),
          "+{s[40:41]}"(xs0), "+{s[42:43]}"(xs1), "+{s[44:45]}"(ws0), "+{s[46:47]}"(ws1), "+{s54}"(w2v)
        : "{v200}"(xvo), "{v201}"(xa), "{v202}"(wa), "{v205}"(wvo), "{s48}"(dx), "{s49}"(dw), "{s53}"(wstep), "{s55}"(wmask), "{s[56:57]}"(es0), "{s[58:59]}"(es1), "{s[60:61]}"(es2)
        : "memory", "scc", "s50", "s51", "s52", "s62", "v203", "v204",
          "v112", "v113", "v114", "v115", "v116", "v117", "v118", "v119", "v120", "v121", "v122", "v123", "v124", "v125", "v126", "v127", "v128", "v129", "v130", "v131", "v132", "v133",
          "v134", "v135", "v136", "v137", "v138", "v139", "v140", "v141", "v142", "v143", "v144", "v145", "v146", "v147", "v148", "v149", "v150", "v151", "v152", "v153", "v154", "v155",
          "v156", "v157", "v158", "v159", "v160", "v161", "v162", "v163", "v164", "v165", "v166", "v167", "v168", "v169", "v170", "v171", "v172", "v173", "v174", "v175", "v176", "v177",
          "v178", "v179", "v180", "v181", "v182", "v183", "v184", "v185", "v186", "v187", "v188", "v189", "v190", "v191", "v192", "v193", "v194", "v195", "v196", "v197", "v198", "v199");
    TS(5, 1);
    const unsigned eb = lds0 + 2 * K5_SLOT, fin = lds0 + 5 * K5_SLOT + 1024;
    if (tid < 256) {
        f32x4 p[4];
#pragma unroll
        for (int i = 0; i < 4; ++i) p[i] = ldsf4(eb + tid * 64 + ((i + (tid >> 2)) & 3) * 16);
        const float mean = 0.125f * (((p[0][0] + p[0][2]) + (p[1][0] + p[1][2])) + ((p[2][0] + p[2][2]) + (p[3][0] + p[3][2])));
        float m2 = 0.f;
#pragma unroll
        for (int i = 0; i < 4; ++i) { const float d0 = p[i][0] - mean, d1 = p[i][2] - mean; m2 += (p[i][1] + p[i][3]) + 128.f * (d0 * d0 + d1 * d1); }
        const float rstd = rsqrtf(m2 * (1.f / 1024.f) + 1e-5f);
        *(LAS f32x2*)(size_t)(fin + tid * 8) = f32x2{rstd, -mean * rstd};
    } else if (tid < 256 + 112) {
        const int j = tid - 256;
        f32x4 v = {0.f, 0.f, 0.f, 0.f};
#pragma unroll
        for (int k = 0; k < 4; ++k) { const int b = k * 1792 + j * 16; v += ldsf4(eb + 16384 + (b >> 10) * 2048 + (b & 1023)); }
        *(LAS f32x4*)(size_t)(fin + 2048 + j * 16) = v;
    }
    WAITL0(); BAR();
    const int m16 = lane & 15, q = lane >> 4;
    f32x2 rs[4];
#pragma unroll
    for (int m = 0; m < 4; ++m) rs[m] = *(const LAS f32x2*)(size_t)(fin + (wm * 64 + m * 16 + m16) * 8);
    unsigned av[4][7];
#pragma unroll
    for (int n = 0; n < 7; ++n) {
        const f32x4 u = ldsf4(fin + 2048 + (wc * 7 + n) * 128 + q * 16), t = ldsf4(fin + 2048 + (wc * 7 + n) * 128 + 64 + q * 16);
#pragma unroll
        for (int m = 0; m < 4; ++m) {
            float z[4];
#pragma unroll
            for (int r = 0; r < 4; ++r) z[r] = rs[m][0] * acc[n][4 * m + r] + (rs[m][1] * u[r] + t[r]);
            av[m][n] = cvtpk(gelu_mul(z[0], z[2]), gelu_mul(z[1], z[3]));
        }
    }
    const int wj = (nt & 1) * 2 + wc, b0 = (nt >> 1) * 7;
    char* ab = ws + WS_ACT + (size_t)(mt * 8 + wm * 2) * 224 * 1024 + (size_t)b0 * 2048 + (q >> 1) * 1024 + (m16 + 32 * (q & 1)) * 16;
#pragma unroll
    for (int m = 0; m < 4; ++m) {
        char* rp = ab + (size_t)(m >> 1) * 224 * 1024 + (m & 1) * 256;
        *(u32x4*)(rp + wj * 2048) = u32x4{av[m][0], av[m][1], av[m][2], av[m][3]};
        *(u32x2*)(rp + (4 + (wj >> 1)) * 2048 + (wj & 1) * 8) = u32x2{av[m][4], av[m][5]};
        *(unsigned*)(rp + 6 * 2048 + wj * 4) = av[m][6];
    }
    TS(5, 2);
}

constexpr int K6_LDS = 4 * 32768;
__global__ __launch_bounds__(512) void k_down(const char* __restrict__ wd, char* __restrict__ ws, char* __restrict__ out) {
    extern __shared__ __attribute__((aligned(16))) char smem[];
    const unsigned lds0 = (unsigned)(size_t)(LAS char*)smem;
    const int tid = threadIdx.x, lane = tid & 63, wid = __builtin_amdgcn_readfirstlane(tid >> 6);
    const int xcd = blockIdx.x & 7, idx = blockIdx.x >> 3;
    const int ks = xcd >> 2, mt = (xcd & 3) * 4 + (idx & 3), nt = idx >> 2;
    const int me = blockIdx.x, partner = blockIdx.x ^ 4;
    const int hi = lane >> 5, m = lane & 31, rb = wid >> 1, cw = wid & 1;
    const int gcb = nt * 4 + ks * 2 + cw;
    TS(6, 0);
    f32x16 acc[2] = {};
    tgemm<4, 3, 28, 3>(acc, lds0, ws + WS_ACT + (size_t)mt * 4 * 224 * 1024 + (size_t)ks * 112 * 1024, 224 * 1024, wd + (size_t)ks * 1792 * 2048 + nt * 256, wid, lane,
                       [&](unsigned lb) {
        const char* xp = ws + WS_X1 + ((size_t)(mt * 4 + rb) * 64 + gcb * 2) * 1024;
        glds16(xp, lane * 16, lb + wid * 2048); glds16(xp + 1024, lane * 16, lb + wid * 2048 + 1024);
        glds16(ws + WS_MODF + 12288 + nt * 512, (lane & 31) * 16, lb + 16384);
    });
    TS(6, 1);
    {
        const f32x16 snd = ks ? acc[0] : acc[1];
        const __amdgpu_buffer_rsrc_t rs = __builtin_amdgcn_make_buffer_rsrc(ws + WS_XB + (size_t)me * 32768, 0, 32768, 0x00020000);
#pragma unroll
        for (int i = 0; i < 4; ++i)
            __builtin_amdgcn_raw_buffer_store_b128(__builtin_bit_cast(u32x4, f32x4{snd[4 * i], snd[4 * i + 1], snd[4 * i + 2], snd[4 * i + 3]}), rs, wid * 4096 + i * 1024 + lane * 16, 0, 16);
        WAITV(0); BAR();
        if (wid == 0) {
            GAS unsigned* fl = (GAS unsigned*)(ws + WS_FLAG);
            if (lane == 0) __hip_atomic_store(fl + me * 32, 1u, __ATOMIC_RELAXED, __HIP_MEMORY_SCOPE_AGENT);
            for (unsigned spins = 0; spins < (1u << 22); ++spins) {
                if (__builtin_amdgcn_readfirstlane(__hip_atomic_load(fl + partner * 32, __ATOMIC_RELAXED, __HIP_MEMORY_SCOPE_AGENT)) == 1u) break;
                __builtin_amdgcn_s_sleep(1);
            }
        }
        BAR();
    }
    TS(6, 2);
    f32x16 kp = ks ? acc[1] : acc[0];
    {
        const __amdgpu_buffer_rsrc_t rs = __builtin_amdgcn_make_buffer_rsrc(ws + WS_XB + (size_t)partner * 32768, 0, 32768, 0x00020000);
        f32x4 pv[4];
#pragma unroll
        for (int i = 0; i < 4; ++i) pv[i] = __builtin_bit_cast(f32x4, __builtin_amdgcn_raw_buffer_load_b128(rs, wid * 4096 + i * 1024 + lane * 16, 0, 16));
#pragma unroll
        for (int i = 0; i < 4; ++i)
#pragma unroll
            for (int j = 0; j < 4; ++j) kp[4 * i + j] += pv[i][j];
    }
    const unsigned ob = lds0 + 32768;
#pragma unroll
    for (int g = 0; g < 4; ++g) {
        const u32x2 xv = *(const LAS u32x2*)(size_t)(lds0 + wid * 2048 + (g >> 1) * 1024 + lane * 16 + (g & 1) * 8);
        const f32x4 gb = ldsf4(lds0 + 16384 + ((ks * 2 + cw) * 32 + g * 8 + hi * 4) * 4);
        *(LAS u32x2*)(size_t)(ob + (rb * 32 + m) * 144 + cw * 64 + g * 16 + hi * 8) =
            u32x2{cvtpk(bflo(xv[0]) + gb[0] * kp[4 * g], bfhi(xv[0]) + gb[1] * kp[4 * g + 1]), cvtpk(bflo(xv[1]) + gb[2] * kp[4 * g + 2], bfhi(xv[1]) + gb[3] * kp[4 * g + 3])};
    }
    WAITL0(); BAR();
#pragma unroll
    for (int i = 0; i < 2; ++i) {
        const int r = i * 64 + (tid >> 3);
        *(u32x4*)(out + (size_t)(mt * 128 + r) * 2048 + nt * 256 + ks * 128 + (tid & 7) * 16) = *(const LAS u32x4*)(size_t)(ob + r * 144 + (tid & 7) * 16);
    }
    TS(6, 3);
}

extern "C" void kernel_launch(void* const* d_in, const int* in_sizes, int n_in, void* d_out, int out_size, void* d_ws, size_t ws_size, hipStream_t stream) {
    const char* x = (const char*)d_in[0]; const char* wq = (const char*)d_in[1]; const char* wk = (const char*)d_in[2]; const char* wv = (const char*)d_in[3];
    const char* wo = (const char*)d_in[4]; const char* wg = (const char*)d_in[6]; const char* wu = (const char*)d_in[7]; const char* wd = (const char*)d_in[8];
    const u16* c = (const u16*)d_in[9]; const u16* wmod = (const u16*)d_in[10];
    char* ws = (char*)d_ws;
    (void)hipFuncSetAttribute((const void*)k_kv, hipFuncAttributeMaxDynamicSharedMemorySize, K2_LDS);
    (void)hipFuncSetAttribute((const void*)k_attn, hipFuncAttributeMaxDynamicSharedMemorySize, K3_LDS);
    (void)hipFuncSetAttribute((const void*)k_oproj, hipFuncAttributeMaxDynamicSharedMemorySize, K4_LDS);
    (void)hipFuncSetAttribute((const void*)k_gateup, hipFuncAttributeMaxDynamicSharedMemorySize, K5_LDS);
    (void)hipFuncSetAttribute((const void*)k_down, hipFuncAttributeMaxDynamicSharedMemorySize, K6_LDS);
    k_pre<<<256, 512, 0, stream>>>(c, wmod, (const u16*)x, wq, wk, wv, ws);
    k_kv<<<256, 512, K2_LDS, stream>>>(c, wmod, wk, wv, ws);
    k_attn<<<256, 512, K3_LDS, stream>>>(wq, wg, wu, wo, wd, ws);
    k_oproj<<<256, 512, K4_LDS, stream>>>(wo, x, ws);
    k_gateup<<<256, 512, K5_LDS, stream>>>(ws);
    k_down<<<256, 512, K6_LDS, stream>>>(wd, ws, (char*)d_out);
}
```

```cpp
#include <hip/hip_runtime.h>
#include <hip/hip_bf16.h>

typedef short bf16x8 __attribute__((ext_vector_type(8)));
typedef short s16x4 __attribute__((ext_vector_type(4)));
typedef float f32x2 __attribute__((ext_vector_type(2)));
typedef float f32x4 __attribute__((ext_vector_type(4)));
typedef float f32x16 __attribute__((ext_vector_type(16)));
typedef unsigned u32x4 __attribute__((ext_vector_type(4)));
typedef unsigned u32x2 __attribute__((ext_vector_type(2)));
typedef __bf16 bf16x2_t __attribute__((ext_vector_type(2)));
typedef unsigned short u16;
#define LAS __attribute__((address_space(3)))
#define GAS __attribute__((address_space(1)))
#define DI __device__ __forceinline__

constexpr int F_ = 3584;


constexpr size_t WS_MODP = 622592;
constexpr size_t WS_MODG = 98304;
constexpr size_t WS_FLAG = 163840;
constexpr size_t WS_SILU = 589824;
constexpr size_t WS_TAG  = 598016;
constexpr size_t WS_STAT = 196608;
constexpr size_t WS_UT   = 327680;
constexpr size_t WS_KV   = 786432;
constexpr size_t WS_H    = 1u << 20;
constexpr size_t WS_O    = WS_H + (4u << 20);
constexpr size_t WS_X1   = WS_O + (4u << 20);
constexpr size_t WS_Y    = WS_X1 + (4u << 20);
constexpr size_t WS_XB   = WS_Y + (4u << 20);
constexpr size_t WS_ACT  = WS_XB + (4u << 20);
constexpr size_t WS_WGU  = WS_ACT + (14u << 20);


#define OUT_AUX 19
#define POLL_SLEEP 6
#define SPIN_MAX (1u << 18)
DI unsigned cvtpk(float lo, float hi) { const f32x2 v = {lo, hi}; return __builtin_bit_cast(unsigned, __builtin_convertvector(v, bf16x2_t)); }
DI float bflo(unsigned u) { return __uint_as_float(u << 16); }
DI float bfhi(unsigned u) { return __uint_as_float(u & 0xffff0000u); }
DI float bfr(float x) { return bflo(cvtpk(x, x)); }
#define MFMA32(a, b, c) __builtin_amdgcn_mfma_f32_32x32x16_bf16((a), (b), (c), 0, 0, 0)
#define MFMA16(a, b, c) __builtin_amdgcn_mfma_f32_16x16x32_bf16((a), (b), (c), 0, 0, 0)
#define WAITV(n) asm volatile("s_waitcnt vmcnt(%0)" ::"i"(n) : "memory")
#define WAITL0() do { asm volatile("" ::: "memory"); __builtin_amdgcn_s_waitcnt(0xc07f); asm volatile("" ::: "memory"); } while (0)
#define SB0() __builtin_amdgcn_sched_barrier(0)
#define BAR() do { asm volatile("" ::: "memory"); __builtin_amdgcn_s_barrier(); asm volatile("" ::: "memory"); } while (0)

DI void glds16(const void* sbase, unsigned voff, unsigned lds) {
    asm volatile("s_mov_b32 m0, %2\n\ts_nop 0\n\tglobal_load_lds_dwordx4 %0, %1" ::"v"(voff), "s"(sbase), "s"(lds) : "memory");
}
DI void glds16c(const void* sbase, unsigned voff, unsigned lds) {
    asm volatile("s_mov_b32 m0, %2\n\ts_nop 0\n\tglobal_load_lds_dwordx4 %0, %1 sc1" ::"v"(voff), "s"(sbase), "s"(lds) : "memory");
}
#define GLDS4X(sfx) asm volatile("s_mov_b32 m0, %2\n\ts_nop 0\n\tglobal_load_lds_dwordx4 %0, %1" sfx "\n\tglobal_load_lds_dwordx4 %0, %1 offset:1024" sfx \
    "\n\tglobal_load_lds_dwordx4 %0, %1 offset:2048" sfx "\n\tglobal_load_lds_dwordx4 %0, %1 offset:3072" sfx ::"v"(voff), "s"(sbase), "s"(lds) : "memory")
DI void glds64(const void* sbase, unsigned voff, unsigned lds) { GLDS4X(""); }
DI void glds64c(const void* sbase, unsigned voff, unsigned lds) { GLDS4X(" sc1"); }
DI void glds4(const void* sbase, unsigned voff, unsigned lds) {
    asm volatile("s_mov_b32 m0, %2\n\ts_nop 0\n\tglobal_load_lds_dword %0, %1" ::"v"(voff), "s"(sbase), "s"(lds) : "memory");
}
#define CODE_PF(sym, bytes, w, ln, dump) do { if ((w) * 8192 < (bytes)) { const char* _p; \
    asm volatile("s_getpc_b64 s[36:37]\n\ts_add_u32 s36, s36, " #sym "@rel32@lo+4\n\ts_addc_u32 s37, s37, " #sym "@rel32@hi+12" : "={s[36:37]}"(_p) :: "scc"); \
    const unsigned _o = (w) * 8192 + (ln) * 128; glds4(_p, _o < (bytes) - 4 ? _o : (bytes) - 4, dump); } } while (0)
constexpr unsigned CL_MAIN = 57216;
DI __amdgpu_buffer_rsrc_t mkrs(const void* p, unsigned bytes) { return __builtin_amdgcn_make_buffer_rsrc((void*)p, 0, bytes, 0x00020000); }
DI void st16wt(__amdgpu_buffer_rsrc_t rs, unsigned voff, unsigned off, u32x4 v) { __builtin_amdgcn_raw_buffer_store_b128(v, rs, voff + off, 0, 16); }
DI bf16x8 lds128(unsigned a) { return *(const LAS bf16x8*)(size_t)a; }
DI f32x4 ldsf4(unsigned a) { return *(const LAS f32x4*)(size_t)a; }
DI s16x4 ldstr(unsigned a) { return __builtin_amdgcn_ds_read_tr16_b64_v4i16((LAS s16x4*)(size_t)a); }
DI bf16x8 ldstr2(unsigned a0, unsigned a1) { s16x4 lo = ldstr(a0), hi = ldstr(a1); return __builtin_shufflevector(lo, hi, 0, 1, 2, 3, 4, 5, 6, 7); }
DI bf16x8 asb(u32x4 v) { return __builtin_bit_cast(bf16x8, v); }
DI float swapadd(float v) { auto r = __builtin_amdgcn_permlane32_swap(__float_as_uint(v), __float_as_uint(v), false, false); return __uint_as_float(r[0]) + __uint_as_float(r[1]); }
DI float swapmax(float v) { auto r = __builtin_amdgcn_permlane32_swap(__float_as_uint(v), __float_as_uint(v), false, false); return fmaxf(__uint_as_float(r[0]), __uint_as_float(r[1])); }
DI float swap16add(float v) { auto r = __builtin_amdgcn_permlane16_swap(__float_as_uint(v), __float_as_uint(v), false, false); return __uint_as_float(r[0]) + __uint_as_float(r[1]); }
DI float ror8add(float v) { return v + __uint_as_float(__builtin_amdgcn_update_dpp(0, __float_as_uint(v), 0x128  , 0xf, 0xf, false)); }
DI float sum_hi3(float v) { return swapadd(swap16add(ror8add(v))); }

DI float silu(float x) { return x * __builtin_amdgcn_rcpf(1.f + __builtin_amdgcn_exp2f(-1.4426950408889634f * x)); }

constexpr int K3_LDS = 163840;
#define WBUF(i) ((i) < 4 ? 8192u + (i) * 24576u : 32768u + (i) * 16384u)
constexpr unsigned K5_SLOT = 30720, K5_EXSLOT = 2;

DI u32x4 modulate(u32x4 n, float tv) {
    unsigned a = n[0], b = n[1], c = n[2], d = n[3]; float t0, t1, t2, t3, t4, t5, t6, t7;
    asm volatile("s_nop 1\n\t"
    "v_lshlrev_b32 %4, 16, %0\n\t"
    "v_and_b32 %5, %13, %0\n\t"
    "v_lshlrev_b32 %6, 16, %1\n\t"
    "v_and_b32 %7, %13, %1\n\t"
    "v_lshlrev_b32 %8, 16, %2\n\t"
    "v_and_b32 %9, %13, %2\n\t"
    "v_lshlrev_b32 %10, 16, %3\n\t"
    "v_and_b32 %11, %13, %3\n\t"
    "v_mul_f32_dpp %4, %12, %4 row_newbcast:0 row_mask:0xf bank_mask:0xf\n\t"
    "v_mul_f32_dpp %5, %12, %5 row_newbcast:1 row_mask:0xf bank_mask:0xf\n\t"
    "v_mul_f32_dpp %6, %12, %6 row_newbcast:2 row_mask:0xf bank_mask:0xf\n\t"
    "v_mul_f32_dpp %7, %12, %7 row_newbcast:3 row_mask:0xf bank_mask:0xf\n\t"
    "v_mul_f32_dpp %8, %12, %8 row_newbcast:4 row_mask:0xf bank_mask:0xf\n\t"
    "v_mul_f32_dpp %9, %12, %9 row_newbcast:5 row_mask:0xf bank_mask:0xf\n\t"
    "v_mul_f32_dpp %10, %12, %10 row_newbcast:6 row_mask:0xf bank_mask:0xf\n\t"
    "v_mul_f32_dpp %11, %12, %11 row_newbcast:7 row_mask:0xf bank_mask:0xf\n\t"
    "v_cvt_pk_bf16_f32 %0, %4, %5\n\t"
    "v_cvt_pk_bf16_f32 %1, %6, %7\n\t"
    "v_cvt_pk_bf16_f32 %2, %8, %9\n\t"
    "v_cvt_pk_bf16_f32 %3, %10, %11\n\t"
    "v_lshlrev_b32 %4, 16, %0\n\t"
    "v_and_b32 %5, %13, %0\n\t"
    "v_lshlrev_b32 %6, 16, %1\n\t"
    "v_and_b32 %7, %13, %1\n\t"
    "v_lshlrev_b32 %8, 16, %2\n\t"
    "v_and_b32 %9, %13, %2\n\t"
    "v_lshlrev_b32 %10, 16, %3\n\t"
    "v_and_b32 %11, %13, %3\n\t"
    "v_add_f32_dpp %4, %12, %4 row_newbcast:8 row_mask:0xf bank_mask:0xf\n\t"
    "v_add_f32_dpp %5, %12, %5 row_newbcast:9 row_mask:0xf bank_mask:0xf\n\t"
    "v_add_f32_dpp %6, %12, %6 row_newbcast:10 row_mask:0xf bank_mask:0xf\n\t"
    "v_add_f32_dpp %7, %12, %7 row_newbcast:11 row_mask:0xf bank_mask:0xf\n\t"
    "v_add_f32_dpp %8, %12, %8 row_newbcast:12 row_mask:0xf bank_mask:0xf\n\t"
    "v_add_f32_dpp %9, %12, %9 row_newbcast:13 row_mask:0xf bank_mask:0xf\n\t"
    "v_add_f32_dpp %10, %12, %10 row_newbcast:14 row_mask:0xf bank_mask:0xf\n\t"
    "v_add_f32_dpp %11, %12, %11 row_newbcast:15 row_mask:0xf bank_mask:0xf\n\t"
    "v_cvt_pk_bf16_f32 %0, %4, %5\n\t"
    "v_cvt_pk_bf16_f32 %1, %6, %7\n\t"
    "v_cvt_pk_bf16_f32 %2, %8, %9\n\t"
    "v_cvt_pk_bf16_f32 %3, %10, %11\n\t"
        "s_nop 1"
        : "+v"(a), "+v"(b), "+v"(c), "+v"(d), "=&v"(t0), "=&v"(t1), "=&v"(t2), "=&v"(t3), "=&v"(t4), "=&v"(t5), "=&v"(t6), "=&v"(t7) : "v"(tv), "s"(0xffff0000u));
    return u32x4{a, b, c, d};
}
DI float ldsf(unsigned a) { return *(const LAS float*)(size_t)a; }
extern "C" __global__ __launch_bounds__(512, 2) void k_main(const char* __restrict__ wq, const char* __restrict__ wk, const char* __restrict__ wv, const char* __restrict__ wg,
                                                         const char* __restrict__ wu, const char* __restrict__ wo, const char* __restrict__ wd,
                                                         const char* __restrict__ wmod, const char* __restrict__ cvec, const char* __restrict__ x, char* __restrict__ ws,
                                                         char* __restrict__ out) {
    extern __shared__ __attribute__((aligned(16))) char smem[];
    const unsigned lds0 = (unsigned)(size_t)(LAS char*)smem;
    const int tid = threadIdx.x, lane = tid & 63, wid = __builtin_amdgcn_readfirstlane(tid >> 6);
    const int xcd = blockIdx.x & 7, idx = blockIdx.x >> 3;
    const int tg = xcd * 4 + (idx & 3), hg = idx >> 2;
    const char* kv = ws + WS_KV;
    const int hi = lane >> 5, li = lane & 15, lg = lane >> 4, qq = li >> 2;
    const int sj = blockIdx.x, cu = min(sj >> 2, 55), kq = sj & 3, ss = kq * 8 + wid;
    u32x4 bop;
    const unsigned pollw = lds0 + 155648;
    GAS unsigned* cnt = (GAS unsigned*)(ws + WS_FLAG);
    const char* xs = ws + WS_H + (size_t)(tg * 2 + (wid >> 2)) * 65536 + (wid & 3) * 1024;
    const char* wqb = wq + hg * 256;
    const unsigned wv0 = (unsigned)(wid * 4 + lg) * 2048 + (unsigned)(((li >> 1) ^ (lg | ((wid & 2) << 1))) * 32 + (li & 1) * 16);
        const int tk = tg * 4 + (hg & 3), isv = hg >> 2;
        u32x4 nv[4]; u32x2 ep, c4;
        const unsigned nvo = (lg >> 1) * 1024 + ((tk & 1) * 16 + li + 32 * (lg & 1)) * 16;
        const unsigned scr = lds0 + 24576;
        {
            asm volatile("global_load_dwordx2 %0, %2, %4 sc1\n\tglobal_load_dwordx2 %1, %3, %5" : "=&v"(ep), "=&v"(c4) : "v"(0), "v"((sj & 3) * 512 + lane * 8), "s"(ws + WS_TAG), "s"(cvec) : "memory");
            const char* gb = wmod + ((sj >> 2) * 64 + ((sj >> 7) << 10)) * 2 + (size_t)((sj & 3) * 256 + wid * 32) * 12288;
            const unsigned gvo = (lane >> 3) * 12288 + (((lane & 7) * 16) ^ ((lane & 16) * 2)), gd = lds0 + 8192 + (wid >> 2) * 24576 + (wid & 3) * 4096;
#pragma unroll
            for (int i = 0; i < 4; ++i) asm volatile("s_mov_b32 m0, %2\n\ts_nop 0\n\tglobal_load_lds_dwordx4 %0, %1 nt" :: "v"(gvo ^ ((i & 1) * 64)), "s"(gb + (size_t)i * 8 * 12288), "s"(gd + i * 1024) : "memory");
            if (tid < 128 && lane < 32 && sj < 128) __hip_atomic_store(cnt + (sj * 2 + wid) * 32 + lane, 0u, __ATOMIC_RELAXED, __HIP_MEMORY_SCOPE_AGENT);
            if (wid == 7) CODE_PF(k_main, CL_MAIN, 0, idx * 3 + (lane & 3), scr + 4096);
            const char* xb = x + (size_t)tk * 32768 + wid * 256;
#pragma unroll
            for (int i = 0; i < 4; ++i) asm volatile("global_load_dwordx4 %0, %1, %2" : "=&v"(nv[i]) : "v"(li * 2048 + lg * 16), "s"(xb + ((i * 64) ^ (isv * 128))) : "memory");
        }
    const bool rep = sj < 224;
    bf16x8 sel;
    { const short v = ((li & 3) == 0 && (qq & 1) == (lg & 1)) ? (short)0x3f80 : (short)0;
#pragma unroll
      for (int j = 0; j < 8; ++j) sel[j] = v; }
    const char* src = ((li & 2) ? wu : wg) + (size_t)(32 * ss + 4 * lg) * (F_ * 2) + (64 * cu + 16 * qq + 8 * (li & 1)) * 2 + 4096;
    unsigned ua = lds0 + 73728 + wid * 1024 + (lane & 1) * 64 + lg * 16;
    auto rsw = [](const void* p, unsigned bytes) { const unsigned long long a = (unsigned long long)p; return u32x4{(unsigned)a, (unsigned)(a >> 32) & 0xffffu, bytes, 0x00020000u}; };
    u32x4 wrs = rsw(ws + WS_WGU + (size_t)(cu * 32 + ss) * 8192, rep ? 8192 : 0), xrs = rsw(ws + WS_XB + (size_t)sj * 16384 + wid * 2048, 2048);
    const char* pfa = (lane & 4) ? wo + (size_t)(sj * 8 + wid) * 1024 + (lane & 1) * 128 : wd + (size_t)(sj * 8 + wid) * 3584 + (lane & 3) * 128;
    unsigned pfs = (lane & 4) ? 128 : 512;
    const char* gsrc = wmod + ((sj >> 7) ? 5120 - 1024 : 2048) * 2 + (sj >> 3) * 128 + (size_t)((sj & 7) * 128 + wid * 16) * 12288;
    asm volatile("" : "+v"(sel), "+v"(src), "+v"(ua), "+v"(pfa), "+v"(pfs));
    asm volatile("" : "+s"(wrs), "+s"(xrs), "+s"(gsrc));
        const unsigned wreg = lds0 + 98304u + wid * 8192;
        const char* wsrc = (isv ? wv : wk) + wid * 8192;
        asm volatile("s_waitcnt vmcnt(8)" : "+v"(ep), "+v"(c4) :: "memory");
        const unsigned long long tag = (((unsigned long long)ep[1] << 32) | ep[0]) + 1;
        const unsigned tagh = __builtin_amdgcn_readfirstlane((unsigned)(tag >> 4)), tagl = __builtin_amdgcn_readfirstlane((unsigned)tag & 15u);
        if (wid == 4) {
            const u32x2 sv = {cvtpk(silu(bflo(c4[0])), silu(bfhi(c4[0]))), cvtpk(silu(bflo(c4[1])), silu(bfhi(c4[1])))};
            *(LAS u32x2*)(size_t)(scr + lane * 8) = sv;
        }
        WAITL0(); WAITV(4); BAR();
        if (wid < 4) {
            f32x4 g = {}, g2 = {};
            const unsigned a = lds0 + 8192 + (8 * lg + qq) * 128 + ((wid ^ (qq >> 1) ^ ((lg & 1) << 1)) * 32) + (li & 3) * 8;
            bf16x8 wf[8], sf8[8];
#pragma unroll
            for (int i = 0; i < 8; ++i) { const unsigned b = a + (i >> 2) * 24576 + (i & 3) * 4096; wf[i] = ldstr2(b, b + 512); sf8[i] = lds128(scr + i * 64 + lg * 16); }
            SB0();
#pragma unroll
            for (int i = 0; i < 4; ++i) { g = MFMA16(wf[2 * i], sf8[2 * i], g); g2 = MFMA16(wf[2 * i + 1], sf8[2 * i + 1], g2); }
            g += g2;
            const float gv = (li & 2) ? ((li & 1) ? g[3] : g[2]) : ((li & 1) ? g[1] : g[0]);
            if (li < 4) __hip_atomic_store((GAS unsigned long long*)(ws + WS_MODP) + (sj & 3) * 4096 + (sj >> 2) * 64 + wid * 16 + lg * 4 + li,
                                           ((unsigned long long)tagh << 32) | (((__float_as_uint(gv) + 8u) & ~15u) | tagl), __ATOMIC_RELAXED, __HIP_MEMORY_SCOPE_AGENT);
            WAITV(1);
        } else WAITV(0);
        asm volatile("" : "+v"(nv[0]), "+v"(nv[1]), "+v"(nv[2]), "+v"(nv[3]));
        if (wid == 5) glds4(wq + (size_t)sj * 8192, lane * 128, scr + 4096);
        if (wid == 6) CODE_PF(k_main, CL_MAIN, 0, 96 + idx * 8 + (lane & 7), scr + 4096);
        glds64(wsrc, lane * 16, wreg);
        float xr[32];
        {
            float sum = 0.f, sum2 = 0.f, q = 0.f, q2 = 0.f;
#pragma unroll
            for (int i = 0; i < 16; ++i) { xr[2 * i] = bflo(nv[i >> 2][i & 3]); xr[2 * i + 1] = bfhi(nv[i >> 2][i & 3]); }
#pragma unroll
            for (int i = 0; i < 16; ++i) { sum += xr[i]; sum2 += xr[16 + i]; }
            const float mw = swapadd(swap16add(sum + sum2)) * (1.f / 128.f);
#pragma unroll
            for (int i = 0; i < 16; ++i) { const float d = xr[i] - mw, e = xr[16 + i] - mw; q += d * d; q2 += e * e; }
            q = swapadd(swap16add(q + q2));
            if (lane < 16) *(LAS f32x2*)(size_t)(scr + 2048 + lane * 64 + wid * 8) = f32x2{mw, q};
        }
        WAITL0(); BAR();
        glds64(wsrc + 4096, lane * 16, wreg + 4096);
        if (wid == 4 && sj < 4) __hip_atomic_store((GAS unsigned long long*)(ws + WS_SILU) + sj * 64 + lane, *(const LAS unsigned long long*)(size_t)(scr + lane * 8), __ATOMIC_RELAXED, __HIP_MEMORY_SCOPE_AGENT);
        float lnm, lnr;
        {
            const f32x4 pw = ldsf4(scr + 2048 + li * 64 + lg * 16);
            const float mean = swapadd(swap16add(pw[0] + pw[2])) * 0.125f, d0 = pw[0] - mean, d1 = pw[2] - mean;
            const float m2 = swapadd(swap16add((pw[1] + pw[3]) + 128.f * (d0 * d0 + d1 * d1)));
            lnm = bfr(mean); lnr = 1.0f / bfr(sqrtf(bfr(bfr(m2 * (1.f / 1024.f)) + 1e-5f)));
        }
        auto norm8 = [&](int h) {
#pragma unroll
            for (int i = 8 * h; i < 8 * h + 8; ++i) { const unsigned d = cvtpk(xr[2 * i] - lnm, xr[2 * i + 1] - lnm); nv[i >> 2][i & 3] = cvtpk(bflo(d) * lnr, bfhi(d) * lnr); }
        };
        norm8(0);
        asm volatile("" : "+v"(nv[0]), "+v"(nv[1]));
        const unsigned tab = lds0 + wid * 1024;
        f32x4 sbv;
        {
            const __amdgpu_buffer_rsrc_t mrs = mkrs(ws + WS_MODP, 131072);
            u32x4 mq[10];
#pragma nounroll
            for (unsigned spins = 0; spins < SPIN_MAX; ++spins) {
                asm volatile("" ::: "memory");
                unsigned bad = 0;
#pragma unroll
                for (int i = 0; i < 8; ++i) mq[i] = __builtin_amdgcn_raw_buffer_load_b128(mrs, lane * 16, wid * 1024 + (i >> 1) * 32768 + (i & 1) * 8192, 16);
#pragma unroll
                for (int i = 0; i < 2; ++i) mq[8 + i] = __builtin_amdgcn_raw_buffer_load_b128(mrs, lg * 32768 + li * 16, ss * 256 + 16384 + i * 8192, 16);
#pragma unroll
                for (int i = 0; i < 10; ++i) bad |= (mq[i][1] ^ tagh) | (mq[i][3] ^ tagh) | (((mq[i][0] ^ tagl) | (mq[i][2] ^ tagl)) & 15u);
                if (!__any(bad != 0)) break;
                __builtin_amdgcn_s_sleep(POLL_SLEEP);
            }
            auto gv = [&](int i, int e) { return __uint_as_float(mq[i][2 * e] & ~15u); };
            const unsigned ta = tab + (lane >> 2) * 64 + (lane & 3) * 8;
            *(LAS f32x2*)(size_t)(ta + 32) = f32x2{bfr((gv(0, 0) + gv(2, 0)) + (gv(4, 0) + gv(6, 0))), bfr((gv(0, 1) + gv(2, 1)) + (gv(4, 1) + gv(6, 1)))};
            *(LAS f32x2*)(size_t)ta = f32x2{bfr(bfr((gv(1, 0) + gv(3, 0)) + (gv(5, 0) + gv(7, 0))) + 1.f), bfr(bfr((gv(1, 1) + gv(3, 1)) + (gv(5, 1) + gv(7, 1))) + 1.f)};
            sbv = f32x4{gv(8, 0), gv(8, 1), gv(9, 0), gv(9, 1)};
        }
        float tv[4];
#pragma unroll
        for (int i = 0; i < 4; ++i) tv[i] = ldsf(tab + ((i * 256) ^ (isv * 512)) + lane * 4);
        const __amdgpu_buffer_rsrc_t hrs = mkrs(ws + WS_H + (size_t)(tk >> 1) * 65536 + wid * 8192 + isv * 4096, 4096);
        nv[0] = modulate(nv[0], tv[0]); st16wt(hrs, nvo, 0, nv[0]);
        nv[1] = modulate(nv[1], tv[1]); st16wt(hrs, nvo, 2048, nv[1]);
        auto wstage = [&](int i) { const unsigned d = lds0 + WBUF(i) + wid * 1024; glds16(wqb + (size_t)i * 131072, wv0, d); glds16(wqb + (size_t)i * 131072 + 65536, wv0, d + 8192); };
        wstage(0); wstage(1);
        asm volatile("" : "+v"(lnm) :: "memory");
        norm8(1);
        nv[2] = modulate(nv[2], tv[2]); nv[3] = modulate(nv[3], tv[3]);
        f32x4 a0 = {}, a1 = {};
        const unsigned wkf = wreg + (8 * lg + qq) * 64 + (li & 3) * 8;
        bf16x8 w0[4], w1[4];
#pragma unroll
        for (int i = 0; i < 4; ++i) { const unsigned a = wkf + ((i * 2048) ^ (isv * 4096)); w0[i] = ldstr2(a, a + 256); w1[i] = ldstr2(a + 32, a + 288); }
        if (isv) {
#pragma unroll
            for (int i = 0; i < 4; ++i) { a0 = MFMA16(asb(nv[i]), w0[i], a0); a1 = MFMA16(asb(nv[i]), w1[i], a1); }
        } else {
#pragma unroll
            for (int i = 0; i < 4; ++i) { a0 = MFMA16(w0[i], asb(nv[i]), a0); a1 = MFMA16(w1[i], asb(nv[i]), a1); }
        }
        *(LAS f32x4*)(size_t)(lds0 + 49152 + wid * 1024 + lane * 16) = a0; *(LAS f32x4*)(size_t)(lds0 + 73728 + wid * 1024 + lane * 16) = a1;
        wstage(2); wstage(3);
        WAITL0(); WAITV(8); BAR();
        if (tid == 448) __hip_atomic_store(cnt + (8 + tg) * 32 + hg, 1u, __ATOMIC_RELAXED, __HIP_MEMORY_SCOPE_AGENT);
        if (wid < 2) {
            f32x4 r = {};
#pragma unroll
            for (int w = 0; w < 8; ++w) r += ldsf4(lds0 + 49152 + wid * 24576 + w * 1024 + lane * 16);
            const int tb = tk >> 1, th = tk & 1;
            const int rec = isv ? ((tb + 1 + th) & 63) : tb, sub = isv ? 2 + th : wid, pos = (isv ? wid : th) * 16 + li + 32 * (isv ? lg & 1 : lg >> 1);
            const unsigned long long pk = ((unsigned long long)cvtpk(r[2], r[3]) << 32) | cvtpk(r[0], r[1]);
            __hip_atomic_store((GAS unsigned long long*)(ws + WS_KV + (size_t)rec * 4096 + sub * 1024 + pos * 16 + (isv ? lg >> 1 : lg & 1) * 8), pk, __ATOMIC_RELAXED, __HIP_MEMORY_SCOPE_AGENT);
        }
        if (wid != 7) { wstage(4); wstage(5); wstage(6); }
        else {
#pragma nounroll
            for (unsigned spins = 0; spins < SPIN_MAX; ++spins) {
                if (__all(__hip_atomic_load(cnt + (8 + tg) * 32 + (lane & 7), __ATOMIC_RELAXED, __HIP_MEMORY_SCOPE_AGENT) == 1u)) break;
                __builtin_amdgcn_s_sleep(POLL_SLEEP);
            }
        }
        WAITL0(); BAR();
        if (wid == 7) { wstage(4); wstage(5); wstage(6); }
#pragma unroll
        for (int i = 0; i < 4; ++i) glds16c(xs + i * 4096, lane * 16, lds0 + i * 24576 + wid * 1024);
        {
#pragma unroll
            for (int j = 0; j < 4; ++j) sbv[j] = swapadd(swap16add(sbv[j]));
            const unsigned ba = lds0 + 147456 + wid * 256;
            if (lane < 32) *(LAS unsigned*)(size_t)(ba + lane * 4) = lane < 16 ? cvtpk(bfr(sbv[2]) + 1.f, bfr(sbv[3]) + 1.f) : cvtpk(sbv[0], sbv[1]);
            const unsigned bb = ba + (lane & 1) * 64 + lg * 8;
            const u32x2 b0 = *(const LAS u32x2*)(size_t)bb, b1 = *(const LAS u32x2*)(size_t)(bb + 32);
            bop = u32x4{b0[0], b0[1], b1[0], b1[1]};
            asm volatile("" : "+v"(bop));
        }
    f32x16 qa = {};
    const unsigned xa = lds0 + (wid & 1) * 4096 + (lg >> 1) * 1024 + (li + 32 * (lg & 1)) * 16, wa = lds0 + 8192 + (8 * lg + qq) * 256 + (((wid & 6) ^ (qq | ((lg & 1) << 2))) * 32) + (li & 3) * 8;
    const unsigned l16q = lane * 16, dstb = lds0 + wid * 1024;
#define QCLOB "memory", "scc", "s44", "s45", "v100", "v101", "v102"
#define QOUT "+{v[0:15]}"(qa), "+{v[16:31]}"(fa), "+{v[32:47]}"(fb), "+{v[48:63]}"(fc), "+{v[64:79]}"(fd)
    {
        f32x16 fa, fb, fc, fd;
        asm volatile("" : "=v"(fa), "=v"(fb), "=v"(fc), "=v"(fd));
#define QIN "{v94}"(xa), "{v95}"(wa), "{v98}"(l16q), "{v99}"(wv0), "{s[40:41]}"(xs), "{s[42:43]}"(wqb), "{s46}"(dstb), "{s47}"(0)
        asm volatile(
    "v_add_u32 v100, 0, v94\n\t"
    "v_add_u32 v101, 0, v95\n\t"
    "v_xor_b32 v102, 32, v101\n\t"
    "s_waitcnt vmcnt(3)\n\t"
    "s_barrier\n\t"
    "ds_read_b128 v[16:19], v100 offset:0\n\t"
    "ds_read_b64_tr_b16 v[32:33], v101 offset:0\n\t"
    "ds_read_b64_tr_b16 v[34:35], v101 offset:1024\n\t"
    "ds_read_b128 v[20:23], v100 offset:256\n\t"
    "ds_read_b64_tr_b16 v[36:37], v102 offset:0\n\t"
    "ds_read_b64_tr_b16 v[38:39], v102 offset:1024\n\t"
    "ds_read_b128 v[24:27], v100 offset:2048\n\t"
    "ds_read_b64_tr_b16 v[40:41], v101 offset:8192\n\t"
    "ds_read_b64_tr_b16 v[42:43], v101 offset:9216\n\t"
    "ds_read_b128 v[28:31], v100 offset:2304\n\t"
    "ds_read_b64_tr_b16 v[44:45], v102 offset:8192\n\t"
    "ds_read_b64_tr_b16 v[46:47], v102 offset:9216\n\t"
    "v_add_u32 v100, 24576, v94\n\t"
    "v_add_u32 v101, 24576, v95\n\t"
    "v_xor_b32 v102, 32, v101\n\t"
    "s_waitcnt vmcnt(2) lgkmcnt(0)\n\t"
    "s_barrier\n\t"
    "s_add_u32 s44, s47, 4\n\t"
    "s_and_b32 s44, s44, 15\n\t"
    "s_lshl_b32 s44, s44, 12\n\t"
    "s_add_u32 s44, s40, s44\n\t"
    "s_addc_u32 s45, s41, 0\n\t"
    "s_add_u32 m0, s46, 0\n\t"
    "v_mfma_f32_16x16x32_bf16 v[0:3], v[32:35], v[16:19], v[0:3]\n\t"
    "global_load_lds_dwordx4 v98, s[44:45] sc1\n\t"
    "ds_read_b128 v[48:51], v100 offset:0\n\t"
    "ds_read_b64_tr_b16 v[64:65], v101 offset:0\n\t"
    "v_mfma_f32_16x16x32_bf16 v[4:7], v[32:35], v[20:23], v[4:7]\n\t"
    "ds_read_b64_tr_b16 v[66:67], v101 offset:1024\n\t"
    "ds_read_b128 v[52:55], v100 offset:256\n\t"
    "s_add_u32 s44, s47, 7\n\t"
    "s_and_b32 s44, s44, 15\n\t"
    "s_lshl_b32 s44, s44, 17\n\t"
    "s_add_u32 s44, s42, s44\n\t"
    "s_addc_u32 s45, s43, 0\n\t"
    "s_add_u32 m0, s46, 8192\n\t"
    "v_mfma_f32_16x16x32_bf16 v[12:15], v[36:39], v[20:23], v[12:15]\n\t"
    "global_load_lds_dwordx4 v99, s[44:45]\n\t"
    "ds_read_b64_tr_b16 v[68:69], v102 offset:0\n\t"
    "ds_read_b64_tr_b16 v[70:71], v102 offset:1024\n\t"
    "v_mfma_f32_16x16x32_bf16 v[8:11], v[36:39], v[16:19], v[8:11]\n\t"
    "ds_read_b128 v[56:59], v100 offset:2048\n\t"
    "ds_read_b64_tr_b16 v[72:73], v101 offset:8192\n\t"
    "s_add_u32 s44, s44, 0x10000\n\t"
    "s_addc_u32 s45, s45, 0\n\t"
    "s_add_u32 m0, m0, 0x2000\n\t"
    "v_mfma_f32_16x16x32_bf16 v[0:3], v[40:43], v[24:27], v[0:3]\n\t"
    "global_load_lds_dwordx4 v99, s[44:45]\n\t"
    "ds_read_b64_tr_b16 v[74:75], v101 offset:9216\n\t"
    "ds_read_b128 v[60:63], v100 offset:2304\n\t"
    "v_mfma_f32_16x16x32_bf16 v[4:7], v[40:43], v[28:31], v[4:7]\n\t"
    "ds_read_b64_tr_b16 v[76:77], v102 offset:8192\n\t"
    "ds_read_b64_tr_b16 v[78:79], v102 offset:9216\n\t"
    "v_mfma_f32_16x16x32_bf16 v[12:15], v[44:47], v[28:31], v[12:15]\n\t"
    "v_mfma_f32_16x16x32_bf16 v[8:11], v[44:47], v[24:27], v[8:11]\n\t"
    "s_waitcnt lgkmcnt(0)\n\t"
            "" : QOUT : QIN : QCLOB);
        if (tid == 0) __hip_atomic_fetch_add(cnt + xcd * 32, 1u, __ATOMIC_RELAXED, __HIP_MEMORY_SCOPE_AGENT);
        asm volatile(
    "v_add_u32 v100, 49152, v94\n\t"
    "v_add_u32 v101, 49152, v95\n\t"
    "v_xor_b32 v102, 32, v101\n\t"
    "s_waitcnt vmcnt(4) lgkmcnt(0)\n\t"
    "s_barrier\n\t"
    "s_add_u32 s44, s47, 5\n\t"
    "s_and_b32 s44, s44, 15\n\t"
    "s_lshl_b32 s44, s44, 12\n\t"
    "s_add_u32 s44, s40, s44\n\t"
    "s_addc_u32 s45, s41, 0\n\t"
    "s_add_u32 m0, s46, 24576\n\t"
    "v_mfma_f32_16x16x32_bf16 v[0:3], v[64:67], v[48:51], v[0:3]\n\t"
    "global_load_lds_dwordx4 v98, s[44:45] sc1\n\t"
    "ds_read_b128 v[16:19], v100 offset:0\n\t"
    "ds_read_b64_tr_b16 v[32:33], v101 offset:0\n\t"
    "v_mfma_f32_16x16x32_bf16 v[4:7], v[64:67], v[52:55], v[4:7]\n\t"
    "ds_read_b64_tr_b16 v[34:35], v101 offset:1024\n\t"
    "ds_read_b128 v[20:23], v100 offset:256\n\t"
    "s_add_u32 s44, s47, 8\n\t"
    "s_and_b32 s44, s44, 15\n\t"
    "s_lshl_b32 s44, s44, 17\n\t"
    "s_add_u32 s44, s42, s44\n\t"
    "s_addc_u32 s45, s43, 0\n\t"
    "s_add_u32 m0, s46, 32768\n\t"
    "v_mfma_f32_16x16x32_bf16 v[12:15], v[68:71], v[52:55], v[12:15]\n\t"
    "global_load_lds_dwordx4 v99, s[44:45]\n\t"
    "ds_read_b64_tr_b16 v[36:37], v102 offset:0\n\t"
    "ds_read_b64_tr_b16 v[38:39], v102 offset:1024\n\t"
    "v_mfma_f32_16x16x32_bf16 v[8:11], v[68:71], v[48:51], v[8:11]\n\t"
    "ds_read_b128 v[24:27], v100 offset:2048\n\t"
    "ds_read_b64_tr_b16 v[40:41], v101 offset:8192\n\t"
    "s_add_u32 s44, s44, 0x10000\n\t"
    "s_addc_u32 s45, s45, 0\n\t"
    "s_add_u32 m0, m0, 0x2000\n\t"
    "v_mfma_f32_16x16x32_bf16 v[0:3], v[72:75], v[56:59], v[0:3]\n\t"
    "global_load_lds_dwordx4 v99, s[44:45]\n\t"
    "ds_read_b64_tr_b16 v[42:43], v101 offset:9216\n\t"
    "ds_read_b128 v[28:31], v100 offset:2304\n\t"
    "v_mfma_f32_16x16x32_bf16 v[4:7], v[72:75], v[60:63], v[4:7]\n\t"
    "ds_read_b64_tr_b16 v[44:45], v102 offset:8192\n\t"
    "ds_read_b64_tr_b16 v[46:47], v102 offset:9216\n\t"
    "v_mfma_f32_16x16x32_bf16 v[12:15], v[76:79], v[60:63], v[12:15]\n\t"
    "v_mfma_f32_16x16x32_bf16 v[8:11], v[76:79], v[56:59], v[8:11]\n\t"
    "v_add_u32 v100, 73728, v94\n\t"
    "v_add_u32 v101, 73728, v95\n\t"
    "v_xor_b32 v102, 32, v101\n\t"
    "s_waitcnt vmcnt(6) lgkmcnt(0)\n\t"
    "s_barrier\n\t"
    "s_add_u32 s44, s47, 6\n\t"
    "s_and_b32 s44, s44, 15\n\t"
    "s_lshl_b32 s44, s44, 12\n\t"
    "s_add_u32 s44, s40, s44\n\t"
    "s_addc_u32 s45, s41, 0\n\t"
    "s_add_u32 m0, s46, 49152\n\t"
    "v_mfma_f32_16x16x32_bf16 v[0:3], v[32:35], v[16:19], v[0:3]\n\t"
    "global_load_lds_dwordx4 v98, s[44:45] sc1\n\t"
    "ds_read_b128 v[48:51], v100 offset:0\n\t"
    "ds_read_b64_tr_b16 v[64:65], v101 offset:0\n\t"
    "v_mfma_f32_16x16x32_bf16 v[4:7], v[32:35], v[20:23], v[4:7]\n\t"
    "ds_read_b64_tr_b16 v[66:67], v101 offset:1024\n\t"
    "ds_read_b128 v[52:55], v100 offset:256\n\t"
    "s_add_u32 s44, s47, 9\n\t"
    "s_and_b32 s44, s44, 15\n\t"
    "s_lshl_b32 s44, s44, 17\n\t"
    "s_add_u32 s44, s42, s44\n\t"
    "s_addc_u32 s45, s43, 0\n\t"
    "s_add_u32 m0, s46, 57344\n\t"
    "v_mfma_f32_16x16x32_bf16 v[12:15], v[36:39], v[20:23], v[12:15]\n\t"
    "global_load_lds_dwordx4 v99, s[44:45]\n\t"
    "ds_read_b64_tr_b16 v[68:69], v102 offset:0\n\t"
    "ds_read_b64_tr_b16 v[70:71], v102 offset:1024\n\t"
    "v_mfma_f32_16x16x32_bf16 v[8:11], v[36:39], v[16:19], v[8:11]\n\t"
    "ds_read_b128 v[56:59], v100 offset:2048\n\t"
    "ds_read_b64_tr_b16 v[72:73], v101 offset:8192\n\t"
    "s_add_u32 s44, s44, 0x10000\n\t"
    "s_addc_u32 s45, s45, 0\n\t"
    "s_add_u32 m0, m0, 0x2000\n\t"
    "v_mfma_f32_16x16x32_bf16 v[0:3], v[40:43], v[24:27], v[0:3]\n\t"
    "global_load_lds_dwordx4 v99, s[44:45]\n\t"
    "ds_read_b64_tr_b16 v[74:75], v101 offset:9216\n\t"
    "ds_read_b128 v[60:63], v100 offset:2304\n\t"
    "v_mfma_f32_16x16x32_bf16 v[4:7], v[40:43], v[28:31], v[4:7]\n\t"
    "ds_read_b64_tr_b16 v[76:77], v102 offset:8192\n\t"
    "ds_read_b64_tr_b16 v[78:79], v102 offset:9216\n\t"
    "v_mfma_f32_16x16x32_bf16 v[12:15], v[44:47], v[28:31], v[12:15]\n\t"
    "v_mfma_f32_16x16x32_bf16 v[8:11], v[44:47], v[24:27], v[8:11]\n\t"
    "v_add_u32 v100, 0, v94\n\t"
    "v_add_u32 v101, 90112, v95\n\t"
    "v_xor_b32 v102, 32, v101\n\t"
    "s_waitcnt vmcnt(8) lgkmcnt(0)\n\t"
    "s_barrier\n\t"
    "s_add_u32 s44, s47, 7\n\t"
    "s_and_b32 s44, s44, 15\n\t"
    "s_lshl_b32 s44, s44, 12\n\t"
    "s_add_u32 s44, s40, s44\n\t"
    "s_addc_u32 s45, s41, 0\n\t"
    "s_add_u32 m0, s46, 73728\n\t"
    "v_mfma_f32_16x16x32_bf16 v[0:3], v[64:67], v[48:51], v[0:3]\n\t"
    "global_load_lds_dwordx4 v98, s[44:45] sc1\n\t"
    "ds_read_b128 v[16:19], v100 offset:0\n\t"
    "ds_read_b64_tr_b16 v[32:33], v101 offset:0\n\t"
    "v_mfma_f32_16x16x32_bf16 v[4:7], v[64:67], v[52:55], v[4:7]\n\t"
    "ds_read_b64_tr_b16 v[34:35], v101 offset:1024\n\t"
    "ds_read_b128 v[20:23], v100 offset:256\n\t"
    "s_add_u32 s44, s47, 10\n\t"
    "s_and_b32 s44, s44, 15\n\t"
    "s_lshl_b32 s44, s44, 17\n\t"
    "s_add_u32 s44, s42, s44\n\t"
    "s_addc_u32 s45, s43, 0\n\t"
    "s_add_u32 m0, s46, 81920\n\t"
    "v_mfma_f32_16x16x32_bf16 v[12:15], v[68:71], v[52:55], v[12:15]\n\t"
    "global_load_lds_dwordx4 v99, s[44:45]\n\t"
    "ds_read_b64_tr_b16 v[36:37], v102 offset:0\n\t"
    "ds_read_b64_tr_b16 v[38:39], v102 offset:1024\n\t"
    "v_mfma_f32_16x16x32_bf16 v[8:11], v[68:71], v[48:51], v[8:11]\n\t"
    "ds_read_b128 v[24:27], v100 offset:2048\n\t"
    "ds_read_b64_tr_b16 v[40:41], v101 offset:8192\n\t"
    "s_add_u32 s44, s44, 0x10000\n\t"
    "s_addc_u32 s45, s45, 0\n\t"
    "s_add_u32 m0, m0, 0x2000\n\t"
    "v_mfma_f32_16x16x32_bf16 v[0:3], v[72:75], v[56:59], v[0:3]\n\t"
    "global_load_lds_dwordx4 v99, s[44:45]\n\t"
    "ds_read_b64_tr_b16 v[42:43], v101 offset:9216\n\t"
    "ds_read_b128 v[28:31], v100 offset:2304\n\t"
    "v_mfma_f32_16x16x32_bf16 v[4:7], v[72:75], v[60:63], v[4:7]\n\t"
    "ds_read_b64_tr_b16 v[44:45], v102 offset:8192\n\t"
    "ds_read_b64_tr_b16 v[46:47], v102 offset:9216\n\t"
    "v_mfma_f32_16x16x32_bf16 v[12:15], v[76:79], v[60:63], v[12:15]\n\t"
    "v_mfma_f32_16x16x32_bf16 v[8:11], v[76:79], v[56:59], v[8:11]\n\t"
    "v_add_u32 v100, 24576, v94\n\t"
    "v_add_u32 v101, 106496, v95\n\t"
    "v_xor_b32 v102, 32, v101\n\t"
    "s_waitcnt vmcnt(8) lgkmcnt(0)\n\t"
    "s_barrier\n\t"
    "s_add_u32 s44, s47, 8\n\t"
    "s_and_b32 s44, s44, 15\n\t"
    "s_lshl_b32 s44, s44, 12\n\t"
    "s_add_u32 s44, s40, s44\n\t"
    "s_addc_u32 s45, s41, 0\n\t"
    "s_add_u32 m0, s46, 0\n\t"
    "v_mfma_f32_16x16x32_bf16 v[0:3], v[32:35], v[16:19], v[0:3]\n\t"
    "global_load_lds_dwordx4 v98, s[44:45] sc1\n\t"
    "ds_read_b128 v[48:51], v100 offset:0\n\t"
    "ds_read_b64_tr_b16 v[64:65], v101 offset:0\n\t"
    "v_mfma_f32_16x16x32_bf16 v[4:7], v[32:35], v[20:23], v[4:7]\n\t"
    "ds_read_b64_tr_b16 v[66:67], v101 offset:1024\n\t"
    "ds_read_b128 v[52:55], v100 offset:256\n\t"
    "v_mfma_f32_16x16x32_bf16 v[12:15], v[36:39], v[20:23], v[12:15]\n\t"
    "ds_read_b64_tr_b16 v[68:69], v102 offset:0\n\t"
    "ds_read_b64_tr_b16 v[70:71], v102 offset:1024\n\t"
    "v_mfma_f32_16x16x32_bf16 v[8:11], v[36:39], v[16:19], v[8:11]\n\t"
    "ds_read_b128 v[56:59], v100 offset:2048\n\t"
    "ds_read_b64_tr_b16 v[72:73], v101 offset:8192\n\t"
    "v_mfma_f32_16x16x32_bf16 v[0:3], v[40:43], v[24:27], v[0:3]\n\t"
    "ds_read_b64_tr_b16 v[74:75], v101 offset:9216\n\t"
    "ds_read_b128 v[60:63], v100 offset:2304\n\t"
    "v_mfma_f32_16x16x32_bf16 v[4:7], v[40:43], v[28:31], v[4:7]\n\t"
    "ds_read_b64_tr_b16 v[76:77], v102 offset:8192\n\t"
    "ds_read_b64_tr_b16 v[78:79], v102 offset:9216\n\t"
    "v_mfma_f32_16x16x32_bf16 v[12:15], v[44:47], v[28:31], v[12:15]\n\t"
    "v_mfma_f32_16x16x32_bf16 v[8:11], v[44:47], v[24:27], v[8:11]\n\t"
    "v_add_u32 v100, 49152, v94\n\t"
    "v_add_u32 v101, 122880, v95\n\t"
    "v_xor_b32 v102, 32, v101\n\t"
    "s_waitcnt vmcnt(6) lgkmcnt(0)\n\t"
    "s_barrier\n\t"
    "s_add_u32 s44, s47, 9\n\t"
    "s_and_b32 s44, s44, 15\n\t"
    "s_lshl_b32 s44, s44, 12\n\t"
    "s_add_u32 s44, s40, s44\n\t"
    "s_addc_u32 s45, s41, 0\n\t"
    "s_add_u32 m0, s46, 24576\n\t"
    "v_mfma_f32_16x16x32_bf16 v[0:3], v[64:67], v[48:51], v[0:3]\n\t"
    "global_load_lds_dwordx4 v98, s[44:45] sc1\n\t"
    "ds_read_b128 v[16:19], v100 offset:0\n\t"
    "ds_read_b64_tr_b16 v[32:33], v101 offset:0\n\t"
    "v_mfma_f32_16x16x32_bf16 v[4:7], v[64:67], v[52:55], v[4:7]\n\t"
    "ds_read_b64_tr_b16 v[34:35], v101 offset:1024\n\t"
    "ds_read_b128 v[20:23], v100 offset:256\n\t"
    "v_mfma_f32_16x16x32_bf16 v[12:15], v[68:71], v[52:55], v[12:15]\n\t"
    "ds_read_b64_tr_b16 v[36:37], v102 offset:0\n\t"
    "ds_read_b64_tr_b16 v[38:39], v102 offset:1024\n\t"
    "v_mfma_f32_16x16x32_bf16 v[8:11], v[68:71], v[48:51], v[8:11]\n\t"
    "ds_read_b128 v[24:27], v100 offset:2048\n\t"
    "ds_read_b64_tr_b16 v[40:41], v101 offset:8192\n\t"
    "v_mfma_f32_16x16x32_bf16 v[0:3], v[72:75], v[56:59], v[0:3]\n\t"
    "ds_read_b64_tr_b16 v[42:43], v101 offset:9216\n\t"
    "ds_read_b128 v[28:31], v100 offset:2304\n\t"
    "v_mfma_f32_16x16x32_bf16 v[4:7], v[72:75], v[60:63], v[4:7]\n\t"
    "ds_read_b64_tr_b16 v[44:45], v102 offset:8192\n\t"
    "ds_read_b64_tr_b16 v[46:47], v102 offset:9216\n\t"
    "v_mfma_f32_16x16x32_bf16 v[12:15], v[76:79], v[60:63], v[12:15]\n\t"
    "v_mfma_f32_16x16x32_bf16 v[8:11], v[76:79], v[56:59], v[8:11]\n\t"
    "v_add_u32 v100, 73728, v94\n\t"
    "v_add_u32 v101, 0, v95\n\t"
    "v_xor_b32 v102, 32, v101\n\t"
    "s_waitcnt vmcnt(4) lgkmcnt(0)\n\t"
    "s_barrier\n\t"
    "s_add_u32 s44, s47, 10\n\t"
    "s_and_b32 s44, s44, 15\n\t"
    "s_lshl_b32 s44, s44, 12\n\t"
    "s_add_u32 s44, s40, s44\n\t"
    "s_addc_u32 s45, s41, 0\n\t"
    "s_add_u32 m0, s46, 49152\n\t"
    "v_mfma_f32_16x16x32_bf16 v[0:3], v[32:35], v[16:19], v[0:3]\n\t"
    "global_load_lds_dwordx4 v98, s[44:45] sc1\n\t"
    "ds_read_b128 v[48:51], v100 offset:0\n\t"
    "ds_read_b64_tr_b16 v[64:65], v101 offset:0\n\t"
    "v_mfma_f32_16x16x32_bf16 v[4:7], v[32:35], v[20:23], v[4:7]\n\t"
    "ds_read_b64_tr_b16 v[66:67], v101 offset:1024\n\t"
    "ds_read_b128 v[52:55], v100 offset:256\n\t"
    "s_add_u32 s44, s47, 11\n\t"
    "s_and_b32 s44, s44, 15\n\t"
    "s_lshl_b32 s44, s44, 17\n\t"
    "s_add_u32 s44, s42, s44\n\t"
    "s_addc_u32 s45, s43, 0\n\t"
    "s_add_u32 m0, s46, 131072\n\t"
    "v_mfma_f32_16x16x32_bf16 v[12:15], v[36:39], v[20:23], v[12:15]\n\t"
    "global_load_lds_dwordx4 v99, s[44:45]\n\t"
    "ds_read_b64_tr_b16 v[68:69], v102 offset:0\n\t"
    "ds_read_b64_tr_b16 v[70:71], v102 offset:1024\n\t"
    "v_mfma_f32_16x16x32_bf16 v[8:11], v[36:39], v[16:19], v[8:11]\n\t"
    "ds_read_b128 v[56:59], v100 offset:2048\n\t"
    "ds_read_b64_tr_b16 v[72:73], v101 offset:8192\n\t"
    "s_add_u32 s44, s44, 0x10000\n\t"
    "s_addc_u32 s45, s45, 0\n\t"
    "s_add_u32 m0, m0, 0x2000\n\t"
    "v_mfma_f32_16x16x32_bf16 v[0:3], v[40:43], v[24:27], v[0:3]\n\t"
    "global_load_lds_dwordx4 v99, s[44:45]\n\t"
    "ds_read_b64_tr_b16 v[74:75], v101 offset:9216\n\t"
    "ds_read_b128 v[60:63], v100 offset:2304\n\t"
    "v_mfma_f32_16x16x32_bf16 v[4:7], v[40:43], v[28:31], v[4:7]\n\t"
    "ds_read_b64_tr_b16 v[76:77], v102 offset:8192\n\t"
    "ds_read_b64_tr_b16 v[78:79], v102 offset:9216\n\t"
    "v_mfma_f32_16x16x32_bf16 v[12:15], v[44:47], v[28:31], v[12:15]\n\t"
    "v_mfma_f32_16x16x32_bf16 v[8:11], v[44:47], v[24:27], v[8:11]\n\t"
    "s_waitcnt lgkmcnt(0)\n\t"
            "" : QOUT : QIN : QCLOB);
        if (wid == 0) asm volatile("s_mov_b32 m0, %2\n\ts_nop 0\n\tglobal_load_lds_dword %0, %1 sc1" ::"v"((lane & 7) * 128), "s"(cnt), "s"(pollw) : "memory");
        asm volatile(
    "v_add_u32 v100, 0, v94\n\t"
    "v_add_u32 v101, 24576, v95\n\t"
    "v_xor_b32 v102, 32, v101\n\t"
    "s_waitcnt vmcnt(4) lgkmcnt(0)\n\t"
    "s_barrier\n\t"
    "s_add_u32 s44, s47, 11\n\t"
    "s_and_b32 s44, s44, 15\n\t"
    "s_lshl_b32 s44, s44, 12\n\t"
    "s_add_u32 s44, s40, s44\n\t"
    "s_addc_u32 s45, s41, 0\n\t"
    "s_add_u32 m0, s46, 73728\n\t"
    "v_mfma_f32_16x16x32_bf16 v[0:3], v[64:67], v[48:51], v[0:3]\n\t"
    "global_load_lds_dwordx4 v98, s[44:45] sc1\n\t"
    "ds_read_b128 v[16:19], v100 offset:0\n\t"
    "ds_read_b64_tr_b16 v[32:33], v101 offset:0\n\t"
    "v_mfma_f32_16x16x32_bf16 v[4:7], v[64:67], v[52:55], v[4:7]\n\t"
    "ds_read_b64_tr_b16 v[34:35], v101 offset:1024\n\t"
    "ds_read_b128 v[20:23], v100 offset:256\n\t"
    "s_add_u32 s44, s47, 12\n\t"
    "s_and_b32 s44, s44, 15\n\t"
    "s_lshl_b32 s44, s44, 17\n\t"
    "s_add_u32 s44, s42, s44\n\t"
    "s_addc_u32 s45, s43, 0\n\t"
    "s_add_u32 m0, s46, 8192\n\t"
    "v_mfma_f32_16x16x32_bf16 v[12:15], v[68:71], v[52:55], v[12:15]\n\t"
    "global_load_lds_dwordx4 v99, s[44:45]\n\t"
    "ds_read_b64_tr_b16 v[36:37], v102 offset:0\n\t"
    "ds_read_b64_tr_b16 v[38:39], v102 offset:1024\n\t"
    "v_mfma_f32_16x16x32_bf16 v[8:11], v[68:71], v[48:51], v[8:11]\n\t"
    "ds_read_b128 v[24:27], v100 offset:2048\n\t"
    "ds_read_b64_tr_b16 v[40:41], v101 offset:8192\n\t"
    "s_add_u32 s44, s44, 0x10000\n\t"
    "s_addc_u32 s45, s45, 0\n\t"
    "s_add_u32 m0, m0, 0x2000\n\t"
    "v_mfma_f32_16x16x32_bf16 v[0:3], v[72:75], v[56:59], v[0:3]\n\t"
    "global_load_lds_dwordx4 v99, s[44:45]\n\t"
    "ds_read_b64_tr_b16 v[42:43], v101 offset:9216\n\t"
    "ds_read_b128 v[28:31], v100 offset:2304\n\t"
    "v_mfma_f32_16x16x32_bf16 v[4:7], v[72:75], v[60:63], v[4:7]\n\t"
    "ds_read_b64_tr_b16 v[44:45], v102 offset:8192\n\t"
    "ds_read_b64_tr_b16 v[46:47], v102 offset:9216\n\t"
    "v_mfma_f32_16x16x32_bf16 v[12:15], v[76:79], v[60:63], v[12:15]\n\t"
    "v_mfma_f32_16x16x32_bf16 v[8:11], v[76:79], v[56:59], v[8:11]\n\t"
    "v_add_u32 v100, 24576, v94\n\t"
    "v_add_u32 v101, 49152, v95\n\t"
    "v_xor_b32 v102, 32, v101\n\t"
    "s_waitcnt vmcnt(6) lgkmcnt(0)\n\t"
    "s_barrier\n\t"
    "s_add_u32 s44, s47, 12\n\t"
    "s_and_b32 s44, s44, 15\n\t"
    "s_lshl_b32 s44, s44, 12\n\t"
    "s_add_u32 s44, s40, s44\n\t"
    "s_addc_u32 s45, s41, 0\n\t"
    "s_add_u32 m0, s46, 0\n\t"
    "v_mfma_f32_16x16x32_bf16 v[0:3], v[32:35], v[16:19], v[0:3]\n\t"
    "global_load_lds_dwordx4 v98, s[44:45] sc1\n\t"
    "ds_read_b128 v[48:51], v100 offset:0\n\t"
    "ds_read_b64_tr_b16 v[64:65], v101 offset:0\n\t"
    "v_mfma_f32_16x16x32_bf16 v[4:7], v[32:35], v[20:23], v[4:7]\n\t"
    "ds_read_b64_tr_b16 v[66:67], v101 offset:1024\n\t"
    "ds_read_b128 v[52:55], v100 offset:256\n\t"
    "s_add_u32 s44, s47, 13\n\t"
    "s_and_b32 s44, s44, 15\n\t"
    "s_lshl_b32 s44, s44, 17\n\t"
    "s_add_u32 s44, s42, s44\n\t"
    "s_addc_u32 s45, s43, 0\n\t"
    "s_add_u32 m0, s46, 32768\n\t"
    "v_mfma_f32_16x16x32_bf16 v[12:15], v[36:39], v[20:23], v[12:15]\n\t"
    "global_load_lds_dwordx4 v99, s[44:45]\n\t"
    "ds_read_b64_tr_b16 v[68:69], v102 offset:0\n\t"
    "ds_read_b64_tr_b16 v[70:71], v102 offset:1024\n\t"
    "v_mfma_f32_16x16x32_bf16 v[8:11], v[36:39], v[16:19], v[8:11]\n\t"
    "ds_read_b128 v[56:59], v100 offset:2048\n\t"
    "ds_read_b64_tr_b16 v[72:73], v101 offset:8192\n\t"
    "s_add_u32 s44, s44, 0x10000\n\t"
    "s_addc_u32 s45, s45, 0\n\t"
    "s_add_u32 m0, m0, 0x2000\n\t"
    "v_mfma_f32_16x16x32_bf16 v[0:3], v[40:43], v[24:27], v[0:3]\n\t"
    "global_load_lds_dwordx4 v99, s[44:45]\n\t"
    "ds_read_b64_tr_b16 v[74:75], v101 offset:9216\n\t"
    "ds_read_b128 v[60:63], v100 offset:2304\n\t"
    "v_mfma_f32_16x16x32_bf16 v[4:7], v[40:43], v[28:31], v[4:7]\n\t"
    "ds_read_b64_tr_b16 v[76:77], v102 offset:8192\n\t"
    "ds_read_b64_tr_b16 v[78:79], v102 offset:9216\n\t"
    "v_mfma_f32_16x16x32_bf16 v[12:15], v[44:47], v[28:31], v[12:15]\n\t"
    "v_mfma_f32_16x16x32_bf16 v[8:11], v[44:47], v[24:27], v[8:11]\n\t"
    "v_add_u32 v100, 49152, v94\n\t"
    "v_add_u32 v101, 73728, v95\n\t"
    "v_xor_b32 v102, 32, v101\n\t"
    "s_waitcnt vmcnt(8) lgkmcnt(0)\n\t"
    "s_barrier\n\t"
    "s_add_u32 s44, s47, 13\n\t"
    "s_and_b32 s44, s44, 15\n\t"
    "s_lshl_b32 s44, s44, 12\n\t"
    "s_add_u32 s44, s40, s44\n\t"
    "s_addc_u32 s45, s41, 0\n\t"
    "s_add_u32 m0, s46, 24576\n\t"
    "v_mfma_f32_16x16x32_bf16 v[0:3], v[64:67], v[48:51], v[0:3]\n\t"
    "global_load_lds_dwordx4 v98, s[44:45] sc1\n\t"
    "ds_read_b128 v[16:19], v100 offset:0\n\t"
    "ds_read_b64_tr_b16 v[32:33], v101 offset:0\n\t"
    "v_mfma_f32_16x16x32_bf16 v[4:7], v[64:67], v[52:55], v[4:7]\n\t"
    "ds_read_b64_tr_b16 v[34:35], v101 offset:1024\n\t"
    "ds_read_b128 v[20:23], v100 offset:256\n\t"
    "s_add_u32 s44, s47, 14\n\t"
    "s_and_b32 s44, s44, 15\n\t"
    "s_lshl_b32 s44, s44, 17\n\t"
    "s_add_u32 s44, s42, s44\n\t"
    "s_addc_u32 s45, s43, 0\n\t"
    "s_add_u32 m0, s46, 57344\n\t"
    "v_mfma_f32_16x16x32_bf16 v[12:15], v[68:71], v[52:55], v[12:15]\n\t"
    "global_load_lds_dwordx4 v99, s[44:45]\n\t"
    "ds_read_b64_tr_b16 v[36:37], v102 offset:0\n\t"
    "ds_read_b64_tr_b16 v[38:39], v102 offset:1024\n\t"
    "v_mfma_f32_16x16x32_bf16 v[8:11], v[68:71], v[48:51], v[8:11]\n\t"
    "ds_read_b128 v[24:27], v100 offset:2048\n\t"
    "ds_read_b64_tr_b16 v[40:41], v101 offset:8192\n\t"
    "s_add_u32 s44, s44, 0x10000\n\t"
    "s_addc_u32 s45, s45, 0\n\t"
    "s_add_u32 m0, m0, 0x2000\n\t"
    "v_mfma_f32_16x16x32_bf16 v[0:3], v[72:75], v[56:59], v[0:3]\n\t"
    "global_load_lds_dwordx4 v99, s[44:45]\n\t"
    "ds_read_b64_tr_b16 v[42:43], v101 offset:9216\n\t"
    "ds_read_b128 v[28:31], v100 offset:2304\n\t"
    "v_mfma_f32_16x16x32_bf16 v[4:7], v[72:75], v[60:63], v[4:7]\n\t"
    "ds_read_b64_tr_b16 v[44:45], v102 offset:8192\n\t"
    "ds_read_b64_tr_b16 v[46:47], v102 offset:9216\n\t"
    "v_mfma_f32_16x16x32_bf16 v[12:15], v[76:79], v[60:63], v[12:15]\n\t"
    "v_mfma_f32_16x16x32_bf16 v[8:11], v[76:79], v[56:59], v[8:11]\n\t"
    "s_waitcnt lgkmcnt(0)\n\t"
            "" : QOUT : QIN : QCLOB);
        if (wid == 0) {
            WAITV(9);
            if (__builtin_expect(!__all(*(const LAS unsigned*)(size_t)(pollw + lane * 4) == 32u), 0)) {
#pragma nounroll
                for (unsigned spins = 0; spins < SPIN_MAX; ++spins) {
                    if (__all(__hip_atomic_load(cnt + (0 + (lane & 7)) * 32, __ATOMIC_RELAXED, __HIP_MEMORY_SCOPE_AGENT) == 32u)) break;
                    __builtin_amdgcn_s_sleep(POLL_SLEEP);
                }
            }
        }
        asm volatile(
    "v_add_u32 v100, 73728, v94\n\t"
    "v_add_u32 v101, 122880, v95\n\t"
    "v_xor_b32 v102, 32, v101\n\t"
    "s_waitcnt vmcnt(8) lgkmcnt(0)\n\t"
    "s_barrier\n\t"
    "s_add_u32 s44, s47, 14\n\t"
    "s_and_b32 s44, s44, 15\n\t"
    "s_lshl_b32 s44, s44, 12\n\t"
    "s_add_u32 s44, s40, s44\n\t"
    "s_addc_u32 s45, s41, 0\n\t"
    "s_add_u32 m0, s46, 49152\n\t"
    "v_mfma_f32_16x16x32_bf16 v[0:3], v[32:35], v[16:19], v[0:3]\n\t"
    "global_load_lds_dwordx4 v98, s[44:45] sc1\n\t"
    "ds_read_b128 v[48:51], v100 offset:0\n\t"
    "ds_read_b64_tr_b16 v[64:65], v101 offset:0\n\t"
    "v_mfma_f32_16x16x32_bf16 v[4:7], v[32:35], v[20:23], v[4:7]\n\t"
    "ds_read_b64_tr_b16 v[66:67], v101 offset:1024\n\t"
    "ds_read_b128 v[52:55], v100 offset:256\n\t"
    "s_add_u32 s44, s47, 15\n\t"
    "s_and_b32 s44, s44, 15\n\t"
    "s_lshl_b32 s44, s44, 17\n\t"
    "s_add_u32 s44, s42, s44\n\t"
    "s_addc_u32 s45, s43, 0\n\t"
    "s_add_u32 m0, s46, 81920\n\t"
    "v_mfma_f32_16x16x32_bf16 v[12:15], v[36:39], v[20:23], v[12:15]\n\t"
    "global_load_lds_dwordx4 v99, s[44:45]\n\t"
    "ds_read_b64_tr_b16 v[68:69], v102 offset:0\n\t"
    "ds_read_b64_tr_b16 v[70:71], v102 offset:1024\n\t"
    "v_mfma_f32_16x16x32_bf16 v[8:11], v[36:39], v[16:19], v[8:11]\n\t"
    "ds_read_b128 v[56:59], v100 offset:2048\n\t"
    "ds_read_b64_tr_b16 v[72:73], v101 offset:8192\n\t"
    "s_add_u32 s44, s44, 0x10000\n\t"
    "s_addc_u32 s45, s45, 0\n\t"
    "s_add_u32 m0, m0, 0x2000\n\t"
    "v_mfma_f32_16x16x32_bf16 v[0:3], v[40:43], v[24:27], v[0:3]\n\t"
    "global_load_lds_dwordx4 v99, s[44:45]\n\t"
    "ds_read_b64_tr_b16 v[74:75], v101 offset:9216\n\t"
    "ds_read_b128 v[60:63], v100 offset:2304\n\t"
    "v_mfma_f32_16x16x32_bf16 v[4:7], v[40:43], v[28:31], v[4:7]\n\t"
    "ds_read_b64_tr_b16 v[76:77], v102 offset:8192\n\t"
    "ds_read_b64_tr_b16 v[78:79], v102 offset:9216\n\t"
    "v_mfma_f32_16x16x32_bf16 v[12:15], v[44:47], v[28:31], v[12:15]\n\t"
    "v_mfma_f32_16x16x32_bf16 v[8:11], v[44:47], v[24:27], v[8:11]\n\t"
    "v_add_u32 v100, 0, v94\n\t"
    "v_add_u32 v101, 0, v95\n\t"
    "v_xor_b32 v102, 32, v101\n\t"
    "s_waitcnt vmcnt(8) lgkmcnt(0)\n\t"
    "s_barrier\n\t"
    "s_add_u32 s44, s47, 15\n\t"
    "s_and_b32 s44, s44, 15\n\t"
    "s_lshl_b32 s44, s44, 12\n\t"
    "s_add_u32 s44, s40, s44\n\t"
    "s_addc_u32 s45, s41, 0\n\t"
    "s_add_u32 m0, s46, 73728\n\t"
    "v_mfma_f32_16x16x32_bf16 v[0:3], v[64:67], v[48:51], v[0:3]\n\t"
    "global_load_lds_dwordx4 v98, s[44:45] sc1\n\t"
    "ds_read_b128 v[16:19], v100 offset:0\n\t"
    "ds_read_b64_tr_b16 v[32:33], v101 offset:0\n\t"
    "v_mfma_f32_16x16x32_bf16 v[4:7], v[64:67], v[52:55], v[4:7]\n\t"
    "ds_read_b64_tr_b16 v[34:35], v101 offset:1024\n\t"
    "ds_read_b128 v[20:23], v100 offset:256\n\t"
    "s_add_u32 s44, s48, 8192\n\t"
    "s_addc_u32 s45, s49, 0\n\t"
    "s_add_u32 m0, s50, 98304\n\t"
    "v_mfma_f32_16x16x32_bf16 v[12:15], v[68:71], v[52:55], v[12:15]\n\t"
    "global_load_lds_dwordx4 v98, s[44:45] offset:0 sc1\n\t"
    "ds_read_b64_tr_b16 v[36:37], v102 offset:0\n\t"
    "ds_read_b64_tr_b16 v[38:39], v102 offset:1024\n\t"
    "v_mfma_f32_16x16x32_bf16 v[8:11], v[68:71], v[48:51], v[8:11]\n\t"
    "ds_read_b128 v[24:27], v100 offset:2048\n\t"
    "ds_read_b64_tr_b16 v[40:41], v101 offset:8192\n\t"
    "s_add_u32 s44, s48, 8192\n\t"
    "s_addc_u32 s45, s49, 0\n\t"
    "s_add_u32 m0, s50, 98304\n\t"
    "v_mfma_f32_16x16x32_bf16 v[0:3], v[72:75], v[56:59], v[0:3]\n\t"
    "global_load_lds_dwordx4 v98, s[44:45] offset:1024 sc1\n\t"
    "ds_read_b64_tr_b16 v[42:43], v101 offset:9216\n\t"
    "ds_read_b128 v[28:31], v100 offset:2304\n\t"
    "v_mfma_f32_16x16x32_bf16 v[4:7], v[72:75], v[60:63], v[4:7]\n\t"
    "ds_read_b64_tr_b16 v[44:45], v102 offset:8192\n\t"
    "ds_read_b64_tr_b16 v[46:47], v102 offset:9216\n\t"
    "s_add_u32 s44, s48, 8192\n\t"
    "s_addc_u32 s45, s49, 0\n\t"
    "s_add_u32 m0, s50, 98304\n\t"
    "v_mfma_f32_16x16x32_bf16 v[12:15], v[76:79], v[60:63], v[12:15]\n\t"
    "global_load_lds_dwordx4 v98, s[44:45] offset:2048 sc1\n\t"
    "v_mfma_f32_16x16x32_bf16 v[8:11], v[76:79], v[56:59], v[8:11]\n\t"
    "v_add_u32 v100, 24576, v94\n\t"
    "v_add_u32 v101, 24576, v95\n\t"
    "v_xor_b32 v102, 32, v101\n\t"
    "s_waitcnt vmcnt(9) lgkmcnt(0)\n\t"
    "s_barrier\n\t"
    "s_add_u32 s44, s48, 8192\n\t"
    "s_addc_u32 s45, s49, 0\n\t"
    "s_add_u32 m0, s50, 98304\n\t"
    "v_mfma_f32_16x16x32_bf16 v[0:3], v[32:35], v[16:19], v[0:3]\n\t"
    "global_load_lds_dwordx4 v98, s[44:45] offset:3072 sc1\n\t"
    "ds_read_b128 v[48:51], v100 offset:0\n\t"
    "ds_read_b64_tr_b16 v[64:65], v101 offset:0\n\t"
    "v_mfma_f32_16x16x32_bf16 v[4:7], v[32:35], v[20:23], v[4:7]\n\t"
    "ds_read_b64_tr_b16 v[66:67], v101 offset:1024\n\t"
    "ds_read_b128 v[52:55], v100 offset:256\n\t"
    "s_mov_b32 m0, s46\n\t"
    "v_mfma_f32_16x16x32_bf16 v[12:15], v[36:39], v[20:23], v[12:15]\n\t"
    "global_load_lds_dwordx4 v98, s[52:53] sc1\n\t"
    "ds_read_b64_tr_b16 v[68:69], v102 offset:0\n\t"
    "ds_read_b64_tr_b16 v[70:71], v102 offset:1024\n\t"
    "v_mfma_f32_16x16x32_bf16 v[8:11], v[36:39], v[16:19], v[8:11]\n\t"
    "ds_read_b128 v[56:59], v100 offset:2048\n\t"
    "ds_read_b64_tr_b16 v[72:73], v101 offset:8192\n\t"
    "v_mfma_f32_16x16x32_bf16 v[0:3], v[40:43], v[24:27], v[0:3]\n\t"
    "ds_read_b64_tr_b16 v[74:75], v101 offset:9216\n\t"
    "ds_read_b128 v[60:63], v100 offset:2304\n\t"
    "v_mfma_f32_16x16x32_bf16 v[4:7], v[40:43], v[28:31], v[4:7]\n\t"
    "ds_read_b64_tr_b16 v[76:77], v102 offset:8192\n\t"
    "ds_read_b64_tr_b16 v[78:79], v102 offset:9216\n\t"
    "v_mfma_f32_16x16x32_bf16 v[12:15], v[44:47], v[28:31], v[12:15]\n\t"
    "v_mfma_f32_16x16x32_bf16 v[8:11], v[44:47], v[24:27], v[8:11]\n\t"
    "v_add_u32 v100, 49152, v94\n\t"
    "v_add_u32 v101, 49152, v95\n\t"
    "v_xor_b32 v102, 32, v101\n\t"
    "s_waitcnt vmcnt(8) lgkmcnt(0)\n\t"
    "s_barrier\n\t"
    "s_add_u32 s44, s48, 40960\n\t"
    "s_addc_u32 s45, s49, 0\n\t"
    "s_add_u32 m0, s50, 131072\n\t"
    "v_mfma_f32_16x16x32_bf16 v[0:3], v[64:67], v[48:51], v[0:3]\n\t"
    "global_load_lds_dwordx4 v98, s[44:45] offset:0 sc1\n\t"
    "ds_read_b128 v[16:19], v100 offset:0\n\t"
    "ds_read_b64_tr_b16 v[32:33], v101 offset:0\n\t"
    "v_mfma_f32_16x16x32_bf16 v[4:7], v[64:67], v[52:55], v[4:7]\n\t"
    "ds_read_b64_tr_b16 v[34:35], v101 offset:1024\n\t"
    "ds_read_b128 v[20:23], v100 offset:256\n\t"
    "s_add_u32 s44, s48, 40960\n\t"
    "s_addc_u32 s45, s49, 0\n\t"
    "s_add_u32 m0, s50, 131072\n\t"
    "v_mfma_f32_16x16x32_bf16 v[12:15], v[68:71], v[52:55], v[12:15]\n\t"
    "global_load_lds_dwordx4 v98, s[44:45] offset:1024 sc1\n\t"
    "ds_read_b64_tr_b16 v[36:37], v102 offset:0\n\t"
    "ds_read_b64_tr_b16 v[38:39], v102 offset:1024\n\t"
    "v_mfma_f32_16x16x32_bf16 v[8:11], v[68:71], v[48:51], v[8:11]\n\t"
    "ds_read_b128 v[24:27], v100 offset:2048\n\t"
    "ds_read_b64_tr_b16 v[40:41], v101 offset:8192\n\t"
    "v_mfma_f32_16x16x32_bf16 v[0:3], v[72:75], v[56:59], v[0:3]\n\t"
    "ds_read_b64_tr_b16 v[42:43], v101 offset:9216\n\t"
    "ds_read_b128 v[28:31], v100 offset:2304\n\t"
    "v_mfma_f32_16x16x32_bf16 v[4:7], v[72:75], v[60:63], v[4:7]\n\t"
    "ds_read_b64_tr_b16 v[44:45], v102 offset:8192\n\t"
    "ds_read_b64_tr_b16 v[46:47], v102 offset:9216\n\t"
    "v_mfma_f32_16x16x32_bf16 v[12:15], v[76:79], v[60:63], v[12:15]\n\t"
    "v_mfma_f32_16x16x32_bf16 v[8:11], v[76:79], v[56:59], v[8:11]\n\t"
    "v_add_u32 v100, 73728, v94\n\t"
    "v_add_u32 v101, 73728, v95\n\t"
    "v_xor_b32 v102, 32, v101\n\t"
    "s_waitcnt vmcnt(7) lgkmcnt(0)\n\t"
    "s_barrier\n\t"
    "s_add_u32 s44, s48, 40960\n\t"
    "s_addc_u32 s45, s49, 0\n\t"
    "s_add_u32 m0, s50, 131072\n\t"
    "v_mfma_f32_16x16x32_bf16 v[0:3], v[32:35], v[16:19], v[0:3]\n\t"
    "global_load_lds_dwordx4 v98, s[44:45] offset:2048 sc1\n\t"
    "ds_read_b128 v[48:51], v100 offset:0\n\t"
    "ds_read_b64_tr_b16 v[64:65], v101 offset:0\n\t"
    "v_mfma_f32_16x16x32_bf16 v[4:7], v[32:35], v[20:23], v[4:7]\n\t"
    "ds_read_b64_tr_b16 v[66:67], v101 offset:1024\n\t"
    "ds_read_b128 v[52:55], v100 offset:256\n\t"
    "s_add_u32 s44, s48, 40960\n\t"
    "s_addc_u32 s45, s49, 0\n\t"
    "s_add_u32 m0, s50, 131072\n\t"
    "v_mfma_f32_16x16x32_bf16 v[12:15], v[36:39], v[20:23], v[12:15]\n\t"
    "global_load_lds_dwordx4 v98, s[44:45] offset:3072 sc1\n\t"
    "ds_read_b64_tr_b16 v[68:69], v102 offset:0\n\t"
    "ds_read_b64_tr_b16 v[70:71], v102 offset:1024\n\t"
    "v_mfma_f32_16x16x32_bf16 v[8:11], v[36:39], v[16:19], v[8:11]\n\t"
    "ds_read_b128 v[56:59], v100 offset:2048\n\t"
    "ds_read_b64_tr_b16 v[72:73], v101 offset:8192\n\t"
    "v_mfma_f32_16x16x32_bf16 v[0:3], v[40:43], v[24:27], v[0:3]\n\t"
    "ds_read_b64_tr_b16 v[74:75], v101 offset:9216\n\t"
    "ds_read_b128 v[60:63], v100 offset:2304\n\t"
    "v_mfma_f32_16x16x32_bf16 v[4:7], v[40:43], v[28:31], v[4:7]\n\t"
    "ds_read_b64_tr_b16 v[76:77], v102 offset:8192\n\t"
    "ds_read_b64_tr_b16 v[78:79], v102 offset:9216\n\t"
    "v_mfma_f32_16x16x32_bf16 v[12:15], v[44:47], v[28:31], v[12:15]\n\t"
    "v_mfma_f32_16x16x32_bf16 v[8:11], v[44:47], v[24:27], v[8:11]\n\t"
    "s_waitcnt lgkmcnt(0)\n\t"
    "v_mfma_f32_16x16x32_bf16 v[0:3], v[64:67], v[48:51], v[0:3]\n\t"
    "v_mfma_f32_16x16x32_bf16 v[4:7], v[64:67], v[52:55], v[4:7]\n\t"
    "v_mfma_f32_16x16x32_bf16 v[12:15], v[68:71], v[52:55], v[12:15]\n\t"
    "v_mfma_f32_16x16x32_bf16 v[8:11], v[68:71], v[48:51], v[8:11]\n\t"
    "v_mfma_f32_16x16x32_bf16 v[0:3], v[72:75], v[56:59], v[0:3]\n\t"
    "v_mfma_f32_16x16x32_bf16 v[4:7], v[72:75], v[60:63], v[4:7]\n\t"
    "v_mfma_f32_16x16x32_bf16 v[12:15], v[76:79], v[60:63], v[12:15]\n\t"
    "v_mfma_f32_16x16x32_bf16 v[8:11], v[76:79], v[56:59], v[8:11]\n\t"
    "s_nop 7\n\t"
    "s_waitcnt vmcnt(4)\n\t"
    "s_barrier\n\t"
            "" : QOUT : QIN, "{s[48:49]}"(kv + wid * 4096), "{s50}"(lds0 + wid * 4096), "{s[52:53]}"(kv + wid * 1024) : QCLOB);
    }
    constexpr float QS = 0.17677669529663687f * 1.4426950408889634f;
    u32x4 q0, q1;
#pragma unroll
    for (int j = 0; j < 2; ++j) {
        auto r0 = __builtin_amdgcn_permlane16_swap(cvtpk(qa[2 * j] * QS, qa[2 * j + 1] * QS), cvtpk(qa[4 + 2 * j] * QS, qa[5 + 2 * j] * QS), false, false);
        auto r1 = __builtin_amdgcn_permlane16_swap(cvtpk(qa[8 + 2 * j] * QS, qa[9 + 2 * j] * QS), cvtpk(qa[12 + 2 * j] * QS, qa[13 + 2 * j] * QS), false, false);
        q0[j] = r0[0]; q0[2 + j] = r0[1]; q1[j] = r1[0]; q1[2 + j] = r1[1];
    }
    bf16x8 qf0 = asb(q0), qf1 = asb(q1);
    f32x16 negm, sa, sb;
    bf16x8 k0, k1, v0, v1;
    {
        const unsigned a = lds0 + lane * 16;
        const bf16x8 ka = lds128(a), kb = lds128(a + 1024);
        k0 = lds128(a + 4096); k1 = lds128(a + 5120); v0 = lds128(a + 6144);
        f32x16 s = {};
        s = MFMA32(ka, qf0, s); s = MFMA32(kb, qf1, s);
        float mx = s[0];
#pragma unroll
        for (int r = 1; r < 16; ++r) mx = fmaxf(mx, s[r]);
        mx = swapmax(mx);
#pragma unroll
        for (int r = 0; r < 16; ++r) { negm[r] = -mx; sa[r] = s[r] - mx; }
    }
    f32x16 o; f32x4 l, tt; u32x4 p0, p1;
    asm volatile("" : "=v"(sb), "=v"(p0), "=v"(o), "=v"(l), "=v"(tt), "=v"(p1), "=v"(v1));
    unsigned junk;
    const unsigned l16 = lane * 16, gl = lds0 + 81920, sl = lds0 + 24576;
    typedef unsigned u32x16 __attribute__((ext_vector_type(16)));
    u32x16 ra, rb;
    if (sj >= 232 && sj < 240) CODE_PF(k_main, CL_MAIN, wid, lane, sl + 1024);
    if (wid == 0) asm volatile("s_mov_b32 m0, %2\n\ts_nop 0\n\tglobal_load_lds_dword %0, %1 sc1" ::"v"(lane * 4), "s"(ws + WS_SILU + (sj & 7) * 256), "s"(sl) : "memory");
    if (sj == 0 && tid == 0) __hip_atomic_store((GAS unsigned long long*)(ws + WS_TAG), tag, __ATOMIC_RELAXED, __HIP_MEMORY_SCOPE_AGENT);
    const unsigned wvo = wv0 ^ ((((wid ^ (wid >> 1)) & 1) * 12u) * 2048u);
    auto osrc = [&](int j) { return wo + hg * 256 + (size_t)((2 * hg + j) & 15) * 131072; };
    auto ostage = [&](int j) { const unsigned lb = lds0 + WBUF(j < 7 ? j : j - 7) + wid * 1024; glds16(osrc(j), wvo, lb); glds16(osrc(j) + 65536, wvo, lb + 8192); };
    asm volatile("" : "=v"(ra), "=v"(rb), "=v"(junk));
#define AOUT "+{v[0:15]}"(o), "+{v[16:31]}"(sa), "+{v[32:47]}"(sb), "+{v[64:67]}"(l), "+{v[80:83]}"(k0), "+{v[84:87]}"(k1), "+{v[88:91]}"(v0), "+{v[92:95]}"(v1), \
             "+{v[96:99]}"(p0), "+{v[100:103]}"(p1), "+{v[104:107]}"(tt), "+{v[112:127]}"(ra), "+{v[128:143]}"(rb), "+{v111}"(junk), "+{v[172:173]}"(pfa)
#define AIN "{v[48:63]}"(negm), "{v[68:71]}"(qf0), "{v[72:75]}"(qf1), "{v[76:79]}"(sel), "{v109}"(l16), "{v110}"(l16 + 4096), "{v[160:163]}"(bop), "{v[174:175]}"((unsigned long long)pfs), \
            "{v176}"(ua), "{v180}"(lds0 + l16), "{s[34:35]}"(kv), "{s36}"(wid * 4096), "{s[40:43]}"(wrs), "{s44}"(lds0 + wid * 4096), "{s45}"(wid * 1024), "{s58}"(0x05040100u), "{s59}"(0x07060302u)
#define ACLOB "memory", "scc", "s37", "s38", "s39", "v108", "v144", "v145", "v146", "v147", "v148", "v149", "v150", "v151", "v152", "v153", "v154", "v155", "v156", "v157", "v158", "v159"
    asm volatile(
    "v_add_u32 v108, 98304, v180\n\t"
    "s_setprio 3\n\t"
    "v_mfma_f32_32x32x16_bf16 v[32:47], v[80:83], v[68:71], v[48:63]\n\t"
    "ds_read_b128 v[80:83], v108 offset:0\n\t"
    "v_exp_f32 v104, v16\n\t"
    "v_exp_f32 v105, v17\n\t"
    "v_exp_f32 v106, v18\n\t"
    "v_cvt_pk_bf16_f32 v96, v104, v105\n\t"
    "v_mfma_f32_32x32x16_bf16 v[32:47], v[84:87], v[72:75], v[32:47]\n\t"
    "ds_read_b128 v[84:87], v108 offset:1024\n\t"
    "v_exp_f32 v107, v19\n\t"
    "v_exp_f32 v104, v20\n\t"
    "v_cvt_pk_bf16_f32 v97, v106, v107\n\t"
    "v_exp_f32 v105, v21\n\t"
    "ds_read_b128 v[92:95], v108 offset:3072\n\t"
    "v_exp_f32 v106, v22\n\t"
    "v_cvt_pk_bf16_f32 v98, v104, v105\n\t"
    "v_exp_f32 v107, v23\n\t"
    "v_exp_f32 v104, v24\n\t"
    "s_waitcnt lgkmcnt(3)\n\t"
    "v_cvt_pk_bf16_f32 v99, v106, v107\n\t"
    "v_exp_f32 v105, v25\n\t"
    "v_exp_f32 v106, v26\n\t"
    "v_mfma_f32_32x32x16_bf16 v[0:15], v[88:91], v[96:99], 0\n\t"
    "ds_read_b128 v[88:91], v108 offset:2048\n\t"
    "v_cvt_pk_bf16_f32 v100, v104, v105\n\t"
    "v_exp_f32 v107, v27\n\t"
    "v_exp_f32 v104, v28\n\t"
    "v_cvt_pk_bf16_f32 v101, v106, v107\n\t"
    "v_exp_f32 v105, v29\n\t"
    "v_mfma_f32_16x16x32_bf16 v[64:67], v[76:79], v[96:99], 0\n\t"
    "s_waitcnt lgkmcnt(1)\n\t"
    "global_load_dwordx4 v[112:115], v[164:165], off offset:-4096 nt\n\t"
    "v_exp_f32 v106, v30\n\t"
    "v_cvt_pk_bf16_f32 v102, v104, v105\n\t"
    "v_exp_f32 v107, v31\n\t"
    "v_mfma_f32_32x32x16_bf16 v[16:31], v[80:83], v[68:71], v[48:63]\n\t"
    "ds_read_b128 v[80:83], v108 offset:4096\n\t"
    "v_cvt_pk_bf16_f32 v103, v106, v107\n\t"
    "v_exp_f32 v104, v32\n\t"
    "v_exp_f32 v105, v33\n\t"
    "v_exp_f32 v106, v34\n\t"
    "v_cvt_pk_bf16_f32 v96, v104, v105\n\t"
    "v_mfma_f32_32x32x16_bf16 v[16:31], v[84:87], v[72:75], v[16:31]\n\t"
    "ds_read_b128 v[84:87], v108 offset:5120\n\t"
    "v_exp_f32 v107, v35\n\t"
    "v_exp_f32 v104, v36\n\t"
    "v_cvt_pk_bf16_f32 v97, v106, v107\n\t"
    "v_exp_f32 v105, v37\n\t"
    "v_mfma_f32_32x32x16_bf16 v[0:15], v[92:95], v[100:103], v[0:15]\n\t"
    "ds_read_b128 v[92:95], v108 offset:7168\n\t"
    "v_exp_f32 v106, v38\n\t"
    "v_cvt_pk_bf16_f32 v98, v104, v105\n\t"
    "v_exp_f32 v107, v39\n\t"
    "v_exp_f32 v104, v40\n\t"
    "v_mfma_f32_16x16x32_bf16 v[64:67], v[76:79], v[100:103], v[64:67]\n\t"
    "s_waitcnt lgkmcnt(3)\n\t"
    "s_add_u32 s37, s36, 73728\n\t"
    "s_and_b32 s37, s37, 0x3ffff\n\t"
    "s_add_u32 s38, s34, s37\n\t"
    "s_addc_u32 s39, s35, 0\n\t"
    "s_add_u32 m0, s44, 32768\n\t"
    "v_cvt_pk_bf16_f32 v99, v106, v107\n\t"
    "v_exp_f32 v105, v41\n\t"
    "v_exp_f32 v106, v42\n\t"
    "v_mfma_f32_32x32x16_bf16 v[0:15], v[88:91], v[96:99], v[0:15]\n\t"
    "ds_read_b128 v[88:91], v108 offset:6144\n\t"
    "v_cvt_pk_bf16_f32 v100, v104, v105\n\t"
    "v_exp_f32 v107, v43\n\t"
    "v_exp_f32 v104, v44\n\t"
    "v_cvt_pk_bf16_f32 v101, v106, v107\n\t"
    "v_exp_f32 v105, v45\n\t"
    "v_mfma_f32_16x16x32_bf16 v[64:67], v[76:79], v[96:99], v[64:67]\n\t"
    "s_waitcnt lgkmcnt(1)\n\t"
    "global_load_dwordx4 v[116:119], v[164:165], off offset:3072 nt\n\t"
    "v_exp_f32 v106, v46\n\t"
    "v_cvt_pk_bf16_f32 v102, v104, v105\n\t"
    "v_exp_f32 v107, v47\n\t"
    "s_setprio 2\n\t"
    "v_mfma_f32_32x32x16_bf16 v[32:47], v[80:83], v[68:71], v[48:63]\n\t"
    "ds_read_b128 v[80:83], v108 offset:8192\n\t"
    "v_cvt_pk_bf16_f32 v103, v106, v107\n\t"
    "v_exp_f32 v104, v16\n\t"
    "v_exp_f32 v105, v17\n\t"
    "v_exp_f32 v106, v18\n\t"
    "v_cvt_pk_bf16_f32 v96, v104, v105\n\t"
    "v_mfma_f32_32x32x16_bf16 v[32:47], v[84:87], v[72:75], v[32:47]\n\t"
    "ds_read_b128 v[84:87], v108 offset:9216\n\t"
    "v_exp_f32 v107, v19\n\t"
    "v_exp_f32 v104, v20\n\t"
    "v_cvt_pk_bf16_f32 v97, v106, v107\n\t"
    "v_exp_f32 v105, v21\n\t"
    "v_mfma_f32_32x32x16_bf16 v[0:15], v[92:95], v[100:103], v[0:15]\n\t"
    "ds_read_b128 v[92:95], v108 offset:11264\n\t"
    "v_exp_f32 v106, v22\n\t"
    "v_cvt_pk_bf16_f32 v98, v104, v105\n\t"
    "v_exp_f32 v107, v23\n\t"
    "v_exp_f32 v104, v24\n\t"
    "v_mfma_f32_16x16x32_bf16 v[64:67], v[76:79], v[100:103], v[64:67]\n\t"
    "s_waitcnt lgkmcnt(3)\n\t"
    "global_load_lds_dwordx4 v109, s[38:39] offset:0 sc1\n\t"
    "v_cvt_pk_bf16_f32 v99, v106, v107\n\t"
    "v_exp_f32 v105, v25\n\t"
    "v_exp_f32 v106, v26\n\t"
    "v_mfma_f32_32x32x16_bf16 v[0:15], v[88:91], v[96:99], v[0:15]\n\t"
    "ds_read_b128 v[88:91], v108 offset:10240\n\t"
    "v_cvt_pk_bf16_f32 v100, v104, v105\n\t"
    "v_exp_f32 v107, v27\n\t"
    "v_exp_f32 v104, v28\n\t"
    "v_cvt_pk_bf16_f32 v101, v106, v107\n\t"
    "v_exp_f32 v105, v29\n\t"
    "v_mfma_f32_16x16x32_bf16 v[64:67], v[76:79], v[96:99], v[64:67]\n\t"
    "s_waitcnt lgkmcnt(1)\n\t"
    "global_load_dwordx4 v[120:123], v[166:167], off offset:-4096 nt\n\t"
    "v_exp_f32 v106, v30\n\t"
    "v_cvt_pk_bf16_f32 v102, v104, v105\n\t"
    "v_exp_f32 v107, v31\n\t"
    "v_mfma_f32_32x32x16_bf16 v[16:31], v[80:83], v[68:71], v[48:63]\n\t"
    "ds_read_b128 v[80:83], v108 offset:12288\n\t"
    "v_cvt_pk_bf16_f32 v103, v106, v107\n\t"
    "v_exp_f32 v104, v32\n\t"
    "v_exp_f32 v105, v33\n\t"
    "v_exp_f32 v106, v34\n\t"
    "v_cvt_pk_bf16_f32 v96, v104, v105\n\t"
    "v_mfma_f32_32x32x16_bf16 v[16:31], v[84:87], v[72:75], v[16:31]\n\t"
    "ds_read_b128 v[84:87], v108 offset:13312\n\t"
    "v_exp_f32 v107, v35\n\t"
    "v_exp_f32 v104, v36\n\t"
    "v_cvt_pk_bf16_f32 v97, v106, v107\n\t"
    "v_exp_f32 v105, v37\n\t"
    "v_mfma_f32_32x32x16_bf16 v[0:15], v[92:95], v[100:103], v[0:15]\n\t"
    "ds_read_b128 v[92:95], v108 offset:15360\n\t"
    "v_exp_f32 v106, v38\n\t"
    "v_cvt_pk_bf16_f32 v98, v104, v105\n\t"
    "v_exp_f32 v107, v39\n\t"
    "v_exp_f32 v104, v40\n\t"
    "v_mfma_f32_16x16x32_bf16 v[64:67], v[76:79], v[100:103], v[64:67]\n\t"
    "s_waitcnt lgkmcnt(3)\n\t"
    "global_load_lds_dwordx4 v109, s[38:39] offset:1024 sc1\n\t"
    "v_cvt_pk_bf16_f32 v99, v106, v107\n\t"
    "v_exp_f32 v105, v41\n\t"
    "v_exp_f32 v106, v42\n\t"
    "v_mfma_f32_32x32x16_bf16 v[0:15], v[88:91], v[96:99], v[0:15]\n\t"
    "ds_read_b128 v[88:91], v108 offset:14336\n\t"
    "v_cvt_pk_bf16_f32 v100, v104, v105\n\t"
    "v_exp_f32 v107, v43\n\t"
    "v_exp_f32 v104, v44\n\t"
    "v_cvt_pk_bf16_f32 v101, v106, v107\n\t"
    "v_exp_f32 v105, v45\n\t"
    "v_mfma_f32_16x16x32_bf16 v[64:67], v[76:79], v[96:99], v[64:67]\n\t"
    "s_waitcnt lgkmcnt(1)\n\t"
    "global_load_dwordx4 v[124:127], v[166:167], off offset:3072 nt\n\t"
    "v_exp_f32 v106, v46\n\t"
    "v_cvt_pk_bf16_f32 v102, v104, v105\n\t"
    "v_exp_f32 v107, v47\n\t"
    "s_setprio 1\n\t"
    "v_mfma_f32_32x32x16_bf16 v[32:47], v[80:83], v[68:71], v[48:63]\n\t"
    "ds_read_b128 v[80:83], v108 offset:16384\n\t"
    "v_cvt_pk_bf16_f32 v103, v106, v107\n\t"
    "v_exp_f32 v104, v16\n\t"
    "v_exp_f32 v105, v17\n\t"
    "v_exp_f32 v106, v18\n\t"
    "v_cvt_pk_bf16_f32 v96, v104, v105\n\t"
    "v_mfma_f32_32x32x16_bf16 v[32:47], v[84:87], v[72:75], v[32:47]\n\t"
    "ds_read_b128 v[84:87], v108 offset:17408\n\t"
    "v_exp_f32 v107, v19\n\t"
    "v_exp_f32 v104, v20\n\t"
    "v_cvt_pk_bf16_f32 v97, v106, v107\n\t"
    "v_exp_f32 v105, v21\n\t"
    "v_mfma_f32_32x32x16_bf16 v[0:15], v[92:95], v[100:103], v[0:15]\n\t"
    "ds_read_b128 v[92:95], v108 offset:19456\n\t"
    "v_exp_f32 v106, v22\n\t"
    "v_cvt_pk_bf16_f32 v98, v104, v105\n\t"
    "v_exp_f32 v107, v23\n\t"
    "v_exp_f32 v104, v24\n\t"
    "v_mfma_f32_16x16x32_bf16 v[64:67], v[76:79], v[100:103], v[64:67]\n\t"
    "s_waitcnt lgkmcnt(3)\n\t"
    "global_load_lds_dwordx4 v109, s[38:39] offset:2048 sc1\n\t"
    "v_cvt_pk_bf16_f32 v99, v106, v107\n\t"
    "v_exp_f32 v105, v25\n\t"
    "v_exp_f32 v106, v26\n\t"
    "v_mfma_f32_32x32x16_bf16 v[0:15], v[88:91], v[96:99], v[0:15]\n\t"
    "ds_read_b128 v[88:91], v108 offset:18432\n\t"
    "v_cvt_pk_bf16_f32 v100, v104, v105\n\t"
    "v_exp_f32 v107, v27\n\t"
    "v_exp_f32 v104, v28\n\t"
    "v_cvt_pk_bf16_f32 v101, v106, v107\n\t"
    "v_exp_f32 v105, v29\n\t"
    "v_mfma_f32_16x16x32_bf16 v[64:67], v[76:79], v[96:99], v[64:67]\n\t"
    "s_waitcnt lgkmcnt(1)\n\t"
    "v_exp_f32 v106, v30\n\t"
    "v_cvt_pk_bf16_f32 v102, v104, v105\n\t"
    "v_exp_f32 v107, v31\n\t"
    "v_mfma_f32_32x32x16_bf16 v[16:31], v[80:83], v[68:71], v[48:63]\n\t"
    "ds_read_b128 v[80:83], v108 offset:20480\n\t"
    "v_cvt_pk_bf16_f32 v103, v106, v107\n\t"
    "v_exp_f32 v104, v32\n\t"
    "v_exp_f32 v105, v33\n\t"
    "v_exp_f32 v106, v34\n\t"
    "v_cvt_pk_bf16_f32 v96, v104, v105\n\t"
    "v_mfma_f32_32x32x16_bf16 v[16:31], v[84:87], v[72:75], v[16:31]\n\t"
    "ds_read_b128 v[84:87], v108 offset:21504\n\t"
    "v_exp_f32 v107, v35\n\t"
    "v_exp_f32 v104, v36\n\t"
    "v_cvt_pk_bf16_f32 v97, v106, v107\n\t"
    "v_exp_f32 v105, v37\n\t"
    "v_mfma_f32_32x32x16_bf16 v[0:15], v[92:95], v[100:103], v[0:15]\n\t"
    "ds_read_b128 v[92:95], v108 offset:23552\n\t"
    "v_exp_f32 v106, v38\n\t"
    "v_cvt_pk_bf16_f32 v98, v104, v105\n\t"
    "v_exp_f32 v107, v39\n\t"
    "v_exp_f32 v104, v40\n\t"
    "v_mfma_f32_16x16x32_bf16 v[64:67], v[76:79], v[100:103], v[64:67]\n\t"
    "s_waitcnt lgkmcnt(3)\n\t"
    "global_load_lds_dwordx4 v109, s[38:39] offset:3072 sc1\n\t"
    "v_cvt_pk_bf16_f32 v99, v106, v107\n\t"
    "v_exp_f32 v105, v41\n\t"
    "v_exp_f32 v106, v42\n\t"
    "v_mfma_f32_32x32x16_bf16 v[0:15], v[88:91], v[96:99], v[0:15]\n\t"
    "ds_read_b128 v[88:91], v108 offset:22528\n\t"
    "v_cvt_pk_bf16_f32 v100, v104, v105\n\t"
    "v_exp_f32 v107, v43\n\t"
    "v_exp_f32 v104, v44\n\t"
    "v_cvt_pk_bf16_f32 v101, v106, v107\n\t"
    "v_exp_f32 v105, v45\n\t"
    "v_mfma_f32_16x16x32_bf16 v[64:67], v[76:79], v[96:99], v[64:67]\n\t"
    "s_waitcnt lgkmcnt(1)\n\t"
    "v_exp_f32 v106, v46\n\t"
    "v_cvt_pk_bf16_f32 v102, v104, v105\n\t"
    "v_exp_f32 v107, v47\n\t"
    "s_setprio 0\n\t"
    "v_mfma_f32_32x32x16_bf16 v[32:47], v[80:83], v[68:71], v[48:63]\n\t"
    "ds_read_b128 v[80:83], v108 offset:24576\n\t"
    "v_cvt_pk_bf16_f32 v103, v106, v107\n\t"
    "v_exp_f32 v104, v16\n\t"
    "v_exp_f32 v105, v17\n\t"
    "v_exp_f32 v106, v18\n\t"
    "v_cvt_pk_bf16_f32 v96, v104, v105\n\t"
    "v_mfma_f32_32x32x16_bf16 v[32:47], v[84:87], v[72:75], v[32:47]\n\t"
    "ds_read_b128 v[84:87], v108 offset:25600\n\t"
    "v_exp_f32 v107, v19\n\t"
    "v_exp_f32 v104, v20\n\t"
    "v_cvt_pk_bf16_f32 v97, v106, v107\n\t"
    "v_exp_f32 v105, v21\n\t"
    "v_mfma_f32_32x32x16_bf16 v[0:15], v[92:95], v[100:103], v[0:15]\n\t"
    "ds_read_b128 v[92:95], v108 offset:27648\n\t"
    "v_exp_f32 v106, v22\n\t"
    "v_cvt_pk_bf16_f32 v98, v104, v105\n\t"
    "v_exp_f32 v107, v23\n\t"
    "v_exp_f32 v104, v24\n\t"
    "v_mfma_f32_16x16x32_bf16 v[64:67], v[76:79], v[100:103], v[64:67]\n\t"
    "s_waitcnt lgkmcnt(3)\n\t"
    "v_cvt_pk_bf16_f32 v99, v106, v107\n\t"
    "v_exp_f32 v105, v25\n\t"
    "v_exp_f32 v106, v26\n\t"
    "v_mfma_f32_32x32x16_bf16 v[0:15], v[88:91], v[96:99], v[0:15]\n\t"
    "ds_read_b128 v[88:91], v108 offset:26624\n\t"
    "v_cvt_pk_bf16_f32 v100, v104, v105\n\t"
    "v_exp_f32 v107, v27\n\t"
    "v_exp_f32 v104, v28\n\t"
    "v_cvt_pk_bf16_f32 v101, v106, v107\n\t"
    "v_exp_f32 v105, v29\n\t"
    "v_mfma_f32_16x16x32_bf16 v[64:67], v[76:79], v[96:99], v[64:67]\n\t"
    "s_waitcnt lgkmcnt(1)\n\t"
    "global_load_dword v111, v[172:173], off\n\t"
    "v_exp_f32 v106, v30\n\t"
    "v_cvt_pk_bf16_f32 v102, v104, v105\n\t"
    "v_exp_f32 v107, v31\n\t"
    "v_mfma_f32_32x32x16_bf16 v[16:31], v[80:83], v[68:71], v[48:63]\n\t"
    "ds_read_b128 v[80:83], v108 offset:28672\n\t"
    "v_cvt_pk_bf16_f32 v103, v106, v107\n\t"
    "v_exp_f32 v104, v32\n\t"
    "v_exp_f32 v105, v33\n\t"
    "v_exp_f32 v106, v34\n\t"
    "v_cvt_pk_bf16_f32 v96, v104, v105\n\t"
    "v_mfma_f32_32x32x16_bf16 v[16:31], v[84:87], v[72:75], v[16:31]\n\t"
    "ds_read_b128 v[84:87], v108 offset:29696\n\t"
    "v_exp_f32 v107, v35\n\t"
    "v_exp_f32 v104, v36\n\t"
    "v_cvt_pk_bf16_f32 v97, v106, v107\n\t"
    "v_exp_f32 v105, v37\n\t"
    "v_mfma_f32_32x32x16_bf16 v[0:15], v[92:95], v[100:103], v[0:15]\n\t"
    "ds_read_b128 v[92:95], v108 offset:31744\n\t"
    "v_exp_f32 v106, v38\n\t"
    "v_cvt_pk_bf16_f32 v98, v104, v105\n\t"
    "v_exp_f32 v107, v39\n\t"
    "v_exp_f32 v104, v40\n\t"
    "v_mfma_f32_16x16x32_bf16 v[64:67], v[76:79], v[100:103], v[64:67]\n\t"
    "s_waitcnt lgkmcnt(3)\n\t"
    "v_cvt_pk_bf16_f32 v99, v106, v107\n\t"
    "v_exp_f32 v105, v41\n\t"
    "v_exp_f32 v106, v42\n\t"
    "v_mfma_f32_32x32x16_bf16 v[0:15], v[88:91], v[96:99], v[0:15]\n\t"
    "ds_read_b128 v[88:91], v108 offset:30720\n\t"
    "v_cvt_pk_bf16_f32 v100, v104, v105\n\t"
    "v_exp_f32 v107, v43\n\t"
    "v_exp_f32 v104, v44\n\t"
    "v_cvt_pk_bf16_f32 v101, v106, v107\n\t"
    "v_exp_f32 v105, v45\n\t"
    "v_mfma_f32_16x16x32_bf16 v[64:67], v[76:79], v[96:99], v[64:67]\n\t"
    "v_lshl_add_u64 v[172:173], v[172:173], 0, v[174:175]\n\t"
    "v_exp_f32 v106, v46\n\t"
    "v_cvt_pk_bf16_f32 v102, v104, v105\n\t"
    "v_exp_f32 v107, v47\n\t"
    "s_waitcnt vmcnt(9) lgkmcnt(0)\n\t"
    "s_barrier\n\t"
    "v_add_u32 v108, 131072, v180\n\t"
    "s_setprio 3\n\t"
    "v_mfma_f32_32x32x16_bf16 v[32:47], v[80:83], v[68:71], v[48:63]\n\t"
    "ds_read_b128 v[80:83], v108 offset:0\n\t"
    "v_cvt_pk_bf16_f32 v103, v106, v107\n\t"
    "v_exp_f32 v104, v16\n\t"
    "v_exp_f32 v105, v17\n\t"
    "v_exp_f32 v106, v18\n\t"
    "v_cvt_pk_bf16_f32 v96, v104, v105\n\t"
    "v_mfma_f32_32x32x16_bf16 v[32:47], v[84:87], v[72:75], v[32:47]\n\t"
    "ds_read_b128 v[84:87], v108 offset:1024\n\t"
    "v_exp_f32 v107, v19\n\t"
    "v_exp_f32 v104, v20\n\t"
    "v_cvt_pk_bf16_f32 v97, v106, v107\n\t"
    "v_exp_f32 v105, v21\n\t"
    "v_mfma_f32_32x32x16_bf16 v[0:15], v[92:95], v[100:103], v[0:15]\n\t"
    "ds_read_b128 v[92:95], v108 offset:3072\n\t"
    "v_exp_f32 v106, v22\n\t"
    "v_cvt_pk_bf16_f32 v98, v104, v105\n\t"
    "v_exp_f32 v107, v23\n\t"
    "v_exp_f32 v104, v24\n\t"
    "v_mfma_f32_16x16x32_bf16 v[64:67], v[76:79], v[100:103], v[64:67]\n\t"
    "s_waitcnt lgkmcnt(3)\n\t"
    "v_cvt_pk_bf16_f32 v99, v106, v107\n\t"
    "v_exp_f32 v105, v25\n\t"
    "v_exp_f32 v106, v26\n\t"
    "v_mfma_f32_32x32x16_bf16 v[0:15], v[88:91], v[96:99], v[0:15]\n\t"
    "ds_read_b128 v[88:91], v108 offset:2048\n\t"
    "v_cvt_pk_bf16_f32 v100, v104, v105\n\t"
    "v_exp_f32 v107, v27\n\t"
    "v_exp_f32 v104, v28\n\t"
    "v_cvt_pk_bf16_f32 v101, v106, v107\n\t"
    "v_exp_f32 v105, v29\n\t"
    "v_mfma_f32_16x16x32_bf16 v[64:67], v[76:79], v[96:99], v[64:67]\n\t"
    "s_waitcnt lgkmcnt(1)\n\t"
    "global_load_dwordx4 v[128:131], v[168:169], off offset:-4096 nt\n\t"
    "v_exp_f32 v106, v30\n\t"
    "v_cvt_pk_bf16_f32 v102, v104, v105\n\t"
    "v_exp_f32 v107, v31\n\t"
    "v_mfma_f32_32x32x16_bf16 v[16:31], v[80:83], v[68:71], v[48:63]\n\t"
    "ds_read_b128 v[80:83], v108 offset:4096\n\t"
    "v_cvt_pk_bf16_f32 v103, v106, v107\n\t"
    "v_exp_f32 v104, v32\n\t"
    "v_exp_f32 v105, v33\n\t"
    "v_exp_f32 v106, v34\n\t"
    "v_cvt_pk_bf16_f32 v96, v104, v105\n\t"
    "v_mfma_f32_32x32x16_bf16 v[16:31], v[84:87], v[72:75], v[16:31]\n\t"
    "ds_read_b128 v[84:87], v108 offset:5120\n\t"
    "v_exp_f32 v107, v35\n\t"
    "v_exp_f32 v104, v36\n\t"
    "v_cvt_pk_bf16_f32 v97, v106, v107\n\t"
    "v_exp_f32 v105, v37\n\t"
    "v_mfma_f32_32x32x16_bf16 v[0:15], v[92:95], v[100:103], v[0:15]\n\t"
    "ds_read_b128 v[92:95], v108 offset:7168\n\t"
    "v_exp_f32 v106, v38\n\t"
    "v_cvt_pk_bf16_f32 v98, v104, v105\n\t"
    "v_exp_f32 v107, v39\n\t"
    "v_exp_f32 v104, v40\n\t"
    "v_mfma_f32_16x16x32_bf16 v[64:67], v[76:79], v[100:103], v[64:67]\n\t"
    "s_waitcnt lgkmcnt(3)\n\t"
    "s_add_u32 s37, s36, 106496\n\t"
    "s_and_b32 s37, s37, 0x3ffff\n\t"
    "s_add_u32 s38, s34, s37\n\t"
    "s_addc_u32 s39, s35, 0\n\t"
    "s_add_u32 m0, s44, 98304\n\t"
    "v_cvt_pk_bf16_f32 v99, v106, v107\n\t"
    "v_exp_f32 v105, v41\n\t"
    "v_exp_f32 v106, v42\n\t"
    "v_mfma_f32_32x32x16_bf16 v[0:15], v[88:91], v[96:99], v[0:15]\n\t"
    "ds_read_b128 v[88:91], v108 offset:6144\n\t"
    "v_cvt_pk_bf16_f32 v100, v104, v105\n\t"
    "v_exp_f32 v107, v43\n\t"
    "v_exp_f32 v104, v44\n\t"
    "v_cvt_pk_bf16_f32 v101, v106, v107\n\t"
    "v_exp_f32 v105, v45\n\t"
    "v_mfma_f32_16x16x32_bf16 v[64:67], v[76:79], v[96:99], v[64:67]\n\t"
    "s_waitcnt lgkmcnt(1)\n\t"
    "global_load_dwordx4 v[132:135], v[168:169], off offset:3072 nt\n\t"
    "v_exp_f32 v106, v46\n\t"
    "v_cvt_pk_bf16_f32 v102, v104, v105\n\t"
    "v_exp_f32 v107, v47\n\t"
    "s_setprio 2\n\t"
    "v_mfma_f32_32x32x16_bf16 v[32:47], v[80:83], v[68:71], v[48:63]\n\t"
    "ds_read_b128 v[80:83], v108 offset:8192\n\t"
    "v_cvt_pk_bf16_f32 v103, v106, v107\n\t"
    "v_exp_f32 v104, v16\n\t"
    "v_exp_f32 v105, v17\n\t"
    "v_exp_f32 v106, v18\n\t"
    "v_cvt_pk_bf16_f32 v96, v104, v105\n\t"
    "v_mfma_f32_32x32x16_bf16 v[32:47], v[84:87], v[72:75], v[32:47]\n\t"
    "ds_read_b128 v[84:87], v108 offset:9216\n\t"
    "v_exp_f32 v107, v19\n\t"
    "v_exp_f32 v104, v20\n\t"
    "v_cvt_pk_bf16_f32 v97, v106, v107\n\t"
    "v_exp_f32 v105, v21\n\t"
    "v_mfma_f32_32x32x16_bf16 v[0:15], v[92:95], v[100:103], v[0:15]\n\t"
    "ds_read_b128 v[92:95], v108 offset:11264\n\t"
    "v_exp_f32 v106, v22\n\t"
    "v_cvt_pk_bf16_f32 v98, v104, v105\n\t"
    "v_exp_f32 v107, v23\n\t"
    "v_exp_f32 v104, v24\n\t"
    "v_mfma_f32_16x16x32_bf16 v[64:67], v[76:79], v[100:103], v[64:67]\n\t"
    "s_waitcnt lgkmcnt(3)\n\t"
    "global_load_lds_dwordx4 v109, s[38:39] offset:0 sc1\n\t"
    "v_cvt_pk_bf16_f32 v99, v106, v107\n\t"
    "v_exp_f32 v105, v25\n\t"
    "v_exp_f32 v106, v26\n\t"
    "v_mfma_f32_32x32x16_bf16 v[0:15], v[88:91], v[96:99], v[0:15]\n\t"
    "ds_read_b128 v[88:91], v108 offset:10240\n\t"
    "v_cvt_pk_bf16_f32 v100, v104, v105\n\t"
    "v_exp_f32 v107, v27\n\t"
    "v_exp_f32 v104, v28\n\t"
    "v_cvt_pk_bf16_f32 v101, v106, v107\n\t"
    "v_exp_f32 v105, v29\n\t"
    "v_mfma_f32_16x16x32_bf16 v[64:67], v[76:79], v[96:99], v[64:67]\n\t"
    "s_waitcnt lgkmcnt(1)\n\t"
    "global_load_dwordx4 v[136:139], v[170:171], off offset:-4096 nt\n\t"
    "v_exp_f32 v106, v30\n\t"
    "v_cvt_pk_bf16_f32 v102, v104, v105\n\t"
    "v_exp_f32 v107, v31\n\t"
    "v_mfma_f32_32x32x16_bf16 v[16:31], v[80:83], v[68:71], v[48:63]\n\t"
    "ds_read_b128 v[80:83], v108 offset:12288\n\t"
    "v_cvt_pk_bf16_f32 v103, v106, v107\n\t"
    "v_exp_f32 v104, v32\n\t"
    "v_exp_f32 v105, v33\n\t"
    "v_exp_f32 v106, v34\n\t"
    "v_cvt_pk_bf16_f32 v96, v104, v105\n\t"
    "v_mfma_f32_32x32x16_bf16 v[16:31], v[84:87], v[72:75], v[16:31]\n\t"
    "ds_read_b128 v[84:87], v108 offset:13312\n\t"
    "v_exp_f32 v107, v35\n\t"
    "v_exp_f32 v104, v36\n\t"
    "v_cvt_pk_bf16_f32 v97, v106, v107\n\t"
    "v_exp_f32 v105, v37\n\t"
    "v_mfma_f32_32x32x16_bf16 v[0:15], v[92:95], v[100:103], v[0:15]\n\t"
    "ds_read_b128 v[92:95], v108 offset:15360\n\t"
    "v_exp_f32 v106, v38\n\t"
    "v_cvt_pk_bf16_f32 v98, v104, v105\n\t"
    "v_exp_f32 v107, v39\n\t"
    "v_exp_f32 v104, v40\n\t"
    "v_mfma_f32_16x16x32_bf16 v[64:67], v[76:79], v[100:103], v[64:67]\n\t"
    "s_waitcnt lgkmcnt(3)\n\t"
    "global_load_lds_dwordx4 v109, s[38:39] offset:1024 sc1\n\t"
    "v_cvt_pk_bf16_f32 v99, v106, v107\n\t"
    "v_exp_f32 v105, v41\n\t"
    "v_exp_f32 v106, v42\n\t"
    "v_mfma_f32_32x32x16_bf16 v[0:15], v[88:91], v[96:99], v[0:15]\n\t"
    "ds_read_b128 v[88:91], v108 offset:14336\n\t"
    "v_cvt_pk_bf16_f32 v100, v104, v105\n\t"
    "v_exp_f32 v107, v43\n\t"
    "v_exp_f32 v104, v44\n\t"
    "v_cvt_pk_bf16_f32 v101, v106, v107\n\t"
    "v_exp_f32 v105, v45\n\t"
    "v_mfma_f32_16x16x32_bf16 v[64:67], v[76:79], v[96:99], v[64:67]\n\t"
    "s_waitcnt lgkmcnt(1)\n\t"
    "global_load_dwordx4 v[140:143], v[170:171], off offset:3072 nt\n\t"
    "v_exp_f32 v106, v46\n\t"
    "v_cvt_pk_bf16_f32 v102, v104, v105\n\t"
    "v_exp_f32 v107, v47\n\t"
    "s_setprio 1\n\t"
    "v_mfma_f32_32x32x16_bf16 v[32:47], v[80:83], v[68:71], v[48:63]\n\t"
    "ds_read_b128 v[80:83], v108 offset:16384\n\t"
    "v_cvt_pk_bf16_f32 v103, v106, v107\n\t"
    "v_exp_f32 v104, v16\n\t"
    "v_exp_f32 v105, v17\n\t"
    "v_exp_f32 v106, v18\n\t"
    "v_cvt_pk_bf16_f32 v96, v104, v105\n\t"
    "v_mfma_f32_32x32x16_bf16 v[32:47], v[84:87], v[72:75], v[32:47]\n\t"
    "ds_read_b128 v[84:87], v108 offset:17408\n\t"
    "v_exp_f32 v107, v19\n\t"
    "v_exp_f32 v104, v20\n\t"
    "v_cvt_pk_bf16_f32 v97, v106, v107\n\t"
    "v_exp_f32 v105, v21\n\t"
    "v_mfma_f32_32x32x16_bf16 v[0:15], v[92:95], v[100:103], v[0:15]\n\t"
    "ds_read_b128 v[92:95], v108 offset:19456\n\t"
    "v_exp_f32 v106, v22\n\t"
    "v_cvt_pk_bf16_f32 v98, v104, v105\n\t"
    "v_exp_f32 v107, v23\n\t"
    "v_exp_f32 v104, v24\n\t"
    "v_mfma_f32_16x16x32_bf16 v[64:67], v[76:79], v[100:103], v[64:67]\n\t"
    "s_waitcnt lgkmcnt(3)\n\t"
    "global_load_lds_dwordx4 v109, s[38:39] offset:2048 sc1\n\t"
    "v_cvt_pk_bf16_f32 v99, v106, v107\n\t"
    "v_exp_f32 v105, v25\n\t"
    "v_exp_f32 v106, v26\n\t"
    "v_mfma_f32_32x32x16_bf16 v[0:15], v[88:91], v[96:99], v[0:15]\n\t"
    "ds_read_b128 v[88:91], v108 offset:18432\n\t"
    "v_cvt_pk_bf16_f32 v100, v104, v105\n\t"
    "v_exp_f32 v107, v27\n\t"
    "v_exp_f32 v104, v28\n\t"
    "v_cvt_pk_bf16_f32 v101, v106, v107\n\t"
    "v_exp_f32 v105, v29\n\t"
    "v_mfma_f32_16x16x32_bf16 v[64:67], v[76:79], v[96:99], v[64:67]\n\t"
    "s_waitcnt lgkmcnt(1)\n\t"
    "v_exp_f32 v106, v30\n\t"
    "v_cvt_pk_bf16_f32 v102, v104, v105\n\t"
    "v_exp_f32 v107, v31\n\t"
    "v_mfma_f32_32x32x16_bf16 v[16:31], v[80:83], v[68:71], v[48:63]\n\t"
    "ds_read_b128 v[80:83], v108 offset:20480\n\t"
    "v_cvt_pk_bf16_f32 v103, v106, v107\n\t"
    "v_exp_f32 v104, v32\n\t"
    "v_exp_f32 v105, v33\n\t"
    "v_exp_f32 v106, v34\n\t"
    "v_cvt_pk_bf16_f32 v96, v104, v105\n\t"
    "v_mfma_f32_32x32x16_bf16 v[16:31], v[84:87], v[72:75], v[16:31]\n\t"
    "ds_read_b128 v[84:87], v108 offset:21504\n\t"
    "v_exp_f32 v107, v35\n\t"
    "v_exp_f32 v104, v36\n\t"
    "v_cvt_pk_bf16_f32 v97, v106, v107\n\t"
    "v_exp_f32 v105, v37\n\t"
    "v_mfma_f32_32x32x16_bf16 v[0:15], v[92:95], v[100:103], v[0:15]\n\t"
    "ds_read_b128 v[92:95], v108 offset:23552\n\t"
    "v_exp_f32 v106, v38\n\t"
    "v_cvt_pk_bf16_f32 v98, v104, v105\n\t"
    "v_exp_f32 v107, v39\n\t"
    "v_exp_f32 v104, v40\n\t"
    "v_mfma_f32_16x16x32_bf16 v[64:67], v[76:79], v[100:103], v[64:67]\n\t"
    "s_waitcnt lgkmcnt(3)\n\t"
    "global_load_lds_dwordx4 v109, s[38:39] offset:3072 sc1\n\t"
    "v_cvt_pk_bf16_f32 v99, v106, v107\n\t"
    "v_exp_f32 v105, v41\n\t"
    "v_exp_f32 v106, v42\n\t"
    "v_mfma_f32_32x32x16_bf16 v[0:15], v[88:91], v[96:99], v[0:15]\n\t"
    "ds_read_b128 v[88:91], v108 offset:22528\n\t"
    "v_cvt_pk_bf16_f32 v100, v104, v105\n\t"
    "v_exp_f32 v107, v43\n\t"
    "v_exp_f32 v104, v44\n\t"
    "v_cvt_pk_bf16_f32 v101, v106, v107\n\t"
    "v_exp_f32 v105, v45\n\t"
    "v_mfma_f32_16x16x32_bf16 v[64:67], v[76:79], v[96:99], v[64:67]\n\t"
    "s_waitcnt lgkmcnt(1)\n\t"
    "s_lshl_b32 s37, s45, 1\n\t"
    "s_add_u32 m0, s37, s46\n\t"
    "v_exp_f32 v106, v46\n\t"
    "v_cvt_pk_bf16_f32 v102, v104, v105\n\t"
    "v_exp_f32 v107, v47\n\t"
    "s_setprio 0\n\t"
    "v_mfma_f32_32x32x16_bf16 v[32:47], v[80:83], v[68:71], v[48:63]\n\t"
    "ds_read_b128 v[80:83], v108 offset:24576\n\t"
    "v_cvt_pk_bf16_f32 v103, v106, v107\n\t"
    "v_exp_f32 v104, v16\n\t"
    "v_exp_f32 v105, v17\n\t"
    "v_exp_f32 v106, v18\n\t"
    "v_cvt_pk_bf16_f32 v96, v104, v105\n\t"
    "v_mfma_f32_32x32x16_bf16 v[32:47], v[84:87], v[72:75], v[32:47]\n\t"
    "ds_read_b128 v[84:87], v108 offset:25600\n\t"
    "v_exp_f32 v107, v19\n\t"
    "v_exp_f32 v104, v20\n\t"
    "v_cvt_pk_bf16_f32 v97, v106, v107\n\t"
    "v_exp_f32 v105, v21\n\t"
    "v_mfma_f32_32x32x16_bf16 v[0:15], v[92:95], v[100:103], v[0:15]\n\t"
    "ds_read_b128 v[92:95], v108 offset:27648\n\t"
    "v_exp_f32 v106, v22\n\t"
    "v_cvt_pk_bf16_f32 v98, v104, v105\n\t"
    "v_exp_f32 v107, v23\n\t"
    "v_exp_f32 v104, v24\n\t"
    "v_mfma_f32_16x16x32_bf16 v[64:67], v[76:79], v[100:103], v[64:67]\n\t"
    "s_waitcnt lgkmcnt(3)\n\t"
    "global_load_lds_dwordx4 v178, s[52:53] nt\n\t"
    "s_add_u32 s52, s52, 98304\n\t"
    "s_addc_u32 s53, s53, 0\n\t"
    "s_add_u32 m0, m0, 0x400\n\t"
    "v_cvt_pk_bf16_f32 v99, v106, v107\n\t"
    "v_exp_f32 v105, v25\n\t"
    "v_exp_f32 v106, v26\n\t"
    "v_mfma_f32_32x32x16_bf16 v[0:15], v[88:91], v[96:99], v[0:15]\n\t"
    "ds_read_b128 v[88:91], v108 offset:26624\n\t"
    "v_cvt_pk_bf16_f32 v100, v104, v105\n\t"
    "v_exp_f32 v107, v27\n\t"
    "v_exp_f32 v104, v28\n\t"
    "v_cvt_pk_bf16_f32 v101, v106, v107\n\t"
    "v_exp_f32 v105, v29\n\t"
    "v_mfma_f32_16x16x32_bf16 v[64:67], v[76:79], v[96:99], v[64:67]\n\t"
    "s_waitcnt lgkmcnt(1)\n\t"
    "global_load_dword v111, v[172:173], off\n\t"
    "v_exp_f32 v106, v30\n\t"
    "v_cvt_pk_bf16_f32 v102, v104, v105\n\t"
    "v_exp_f32 v107, v31\n\t"
    "v_mfma_f32_32x32x16_bf16 v[16:31], v[80:83], v[68:71], v[48:63]\n\t"
    "ds_read_b128 v[80:83], v108 offset:28672\n\t"
    "v_cvt_pk_bf16_f32 v103, v106, v107\n\t"
    "v_exp_f32 v104, v32\n\t"
    "v_exp_f32 v105, v33\n\t"
    "v_exp_f32 v106, v34\n\t"
    "v_cvt_pk_bf16_f32 v96, v104, v105\n\t"
    "v_mfma_f32_32x32x16_bf16 v[16:31], v[84:87], v[72:75], v[16:31]\n\t"
    "ds_read_b128 v[84:87], v108 offset:29696\n\t"
    "v_exp_f32 v107, v35\n\t"
    "v_exp_f32 v104, v36\n\t"
    "v_cvt_pk_bf16_f32 v97, v106, v107\n\t"
    "v_exp_f32 v105, v37\n\t"
    "v_mfma_f32_32x32x16_bf16 v[0:15], v[92:95], v[100:103], v[0:15]\n\t"
    "ds_read_b128 v[92:95], v108 offset:31744\n\t"
    "v_exp_f32 v106, v38\n\t"
    "v_cvt_pk_bf16_f32 v98, v104, v105\n\t"
    "v_exp_f32 v107, v39\n\t"
    "v_exp_f32 v104, v40\n\t"
    "v_mfma_f32_16x16x32_bf16 v[64:67], v[76:79], v[100:103], v[64:67]\n\t"
    "s_waitcnt lgkmcnt(3)\n\t"
    "global_load_lds_dwordx4 v178, s[52:53] nt\n\t"
    "v_cvt_pk_bf16_f32 v99, v106, v107\n\t"
    "v_exp_f32 v105, v41\n\t"
    "v_exp_f32 v106, v42\n\t"
    "v_mfma_f32_32x32x16_bf16 v[0:15], v[88:91], v[96:99], v[0:15]\n\t"
    "ds_read_b128 v[88:91], v108 offset:30720\n\t"
    "v_cvt_pk_bf16_f32 v100, v104, v105\n\t"
    "v_exp_f32 v107, v43\n\t"
    "v_exp_f32 v104, v44\n\t"
    "v_cvt_pk_bf16_f32 v101, v106, v107\n\t"
    "v_exp_f32 v105, v45\n\t"
    "v_mfma_f32_16x16x32_bf16 v[64:67], v[76:79], v[96:99], v[64:67]\n\t"
    "v_lshl_add_u64 v[172:173], v[172:173], 0, v[174:175]\n\t"
    "v_exp_f32 v106, v46\n\t"
    "v_cvt_pk_bf16_f32 v102, v104, v105\n\t"
    "v_exp_f32 v107, v47\n\t"
    "s_waitcnt vmcnt(5) lgkmcnt(0)\n\t"
    "s_barrier\n\t"
    "v_add_u32 v108, 32768, v180\n\t"
    "s_setprio 3\n\t"
    "v_mfma_f32_32x32x16_bf16 v[32:47], v[80:83], v[68:71], v[48:63]\n\t"
    "ds_read_b128 v[80:83], v108 offset:0\n\t"
    "v_cvt_pk_bf16_f32 v103, v106, v107\n\t"
    "v_exp_f32 v104, v16\n\t"
    "v_exp_f32 v105, v17\n\t"
    "v_exp_f32 v106, v18\n\t"
    "v_cvt_pk_bf16_f32 v96, v104, v105\n\t"
    "v_mfma_f32_32x32x16_bf16 v[32:47], v[84:87], v[72:75], v[32:47]\n\t"
    "ds_read_b128 v[84:87], v108 offset:1024\n\t"
    "v_exp_f32 v107, v19\n\t"
    "v_exp_f32 v104, v20\n\t"
    "v_cvt_pk_bf16_f32 v97, v106, v107\n\t"
    "v_exp_f32 v105, v21\n\t"
    "v_mfma_f32_32x32x16_bf16 v[0:15], v[92:95], v[100:103], v[0:15]\n\t"
    "ds_read_b128 v[92:95], v108 offset:3072\n\t"
    "v_exp_f32 v106, v22\n\t"
    "v_cvt_pk_bf16_f32 v98, v104, v105\n\t"
    "v_exp_f32 v107, v23\n\t"
    "v_exp_f32 v104, v24\n\t"
    "v_mfma_f32_16x16x32_bf16 v[64:67], v[76:79], v[100:103], v[64:67]\n\t"
    "s_waitcnt lgkmcnt(3)\n\t"
    "v_cvt_pk_bf16_f32 v99, v106, v107\n\t"
    "v_exp_f32 v105, v25\n\t"
    "v_exp_f32 v106, v26\n\t"
    "v_mfma_f32_32x32x16_bf16 v[0:15], v[88:91], v[96:99], v[0:15]\n\t"
    "ds_read_b128 v[88:91], v108 offset:2048\n\t"
    "v_cvt_pk_bf16_f32 v100, v104, v105\n\t"
    "v_exp_f32 v107, v27\n\t"
    "v_exp_f32 v104, v28\n\t"
    "v_cvt_pk_bf16_f32 v101, v106, v107\n\t"
    "v_exp_f32 v105, v29\n\t"
    "v_mfma_f32_16x16x32_bf16 v[64:67], v[76:79], v[96:99], v[64:67]\n\t"
    "s_waitcnt lgkmcnt(1)\n\t"
    "v_perm_b32 v144, v116, v112, s58\n\t"
    "v_perm_b32 v145, v124, v120, s58\n\t"
    "v_perm_b32 v146, v132, v128, s58\n\t"
    "v_perm_b32 v147, v140, v136, s58\n\t"
    "v_exp_f32 v106, v30\n\t"
    "v_cvt_pk_bf16_f32 v102, v104, v105\n\t"
    "v_exp_f32 v107, v31\n\t"
    "v_mfma_f32_32x32x16_bf16 v[16:31], v[80:83], v[68:71], v[48:63]\n\t"
    "ds_read_b128 v[80:83], v108 offset:4096\n\t"
    "v_cvt_pk_bf16_f32 v103, v106, v107\n\t"
    "v_exp_f32 v104, v32\n\t"
    "v_exp_f32 v105, v33\n\t"
    "v_exp_f32 v106, v34\n\t"
    "v_cvt_pk_bf16_f32 v96, v104, v105\n\t"
    "v_mfma_f32_32x32x16_bf16 v[16:31], v[84:87], v[72:75], v[16:31]\n\t"
    "ds_read_b128 v[84:87], v108 offset:5120\n\t"
    "v_exp_f32 v107, v35\n\t"
    "v_exp_f32 v104, v36\n\t"
    "v_cvt_pk_bf16_f32 v97, v106, v107\n\t"
    "v_exp_f32 v105, v37\n\t"
    "v_mfma_f32_32x32x16_bf16 v[0:15], v[92:95], v[100:103], v[0:15]\n\t"
    "ds_read_b128 v[92:95], v108 offset:7168\n\t"
    "v_exp_f32 v106, v38\n\t"
    "v_cvt_pk_bf16_f32 v98, v104, v105\n\t"
    "v_exp_f32 v107, v39\n\t"
    "v_exp_f32 v104, v40\n\t"
    "v_mfma_f32_16x16x32_bf16 v[64:67], v[76:79], v[100:103], v[64:67]\n\t"
    "s_waitcnt lgkmcnt(3)\n\t"
    "s_add_u32 s37, s36, 139264\n\t"
    "s_and_b32 s37, s37, 0x3ffff\n\t"
    "s_add_u32 s38, s34, s37\n\t"
    "s_addc_u32 s39, s35, 0\n\t"
    "s_add_u32 m0, s44, 131072\n\t"
    "v_cvt_pk_bf16_f32 v99, v106, v107\n\t"
    "v_exp_f32 v105, v41\n\t"
    "v_exp_f32 v106, v42\n\t"
    "v_mfma_f32_32x32x16_bf16 v[0:15], v[88:91], v[96:99], v[0:15]\n\t"
    "ds_read_b128 v[88:91], v108 offset:6144\n\t"
    "v_cvt_pk_bf16_f32 v100, v104, v105\n\t"
    "v_exp_f32 v107, v43\n\t"
    "v_exp_f32 v104, v44\n\t"
    "v_cvt_pk_bf16_f32 v101, v106, v107\n\t"
    "v_exp_f32 v105, v45\n\t"
    "v_mfma_f32_16x16x32_bf16 v[64:67], v[76:79], v[96:99], v[64:67]\n\t"
    "s_waitcnt lgkmcnt(1)\n\t"
    "v_mfma_f32_16x16x32_bf16 v[152:155], v[144:147], v[160:163], 0\n\t"
    "buffer_store_dwordx4 v[144:147], v109, s[40:43], 0 offen offset:0 sc1\n\t"
    "v_exp_f32 v106, v46\n\t"
    "v_cvt_pk_bf16_f32 v102, v104, v105\n\t"
    "v_exp_f32 v107, v47\n\t"
    "s_setprio 2\n\t"
    "v_mfma_f32_32x32x16_bf16 v[32:47], v[80:83], v[68:71], v[48:63]\n\t"
    "ds_read_b128 v[80:83], v108 offset:8192\n\t"
    "v_cvt_pk_bf16_f32 v103, v106, v107\n\t"
    "v_exp_f32 v104, v16\n\t"
    "v_exp_f32 v105, v17\n\t"
    "v_exp_f32 v106, v18\n\t"
    "v_cvt_pk_bf16_f32 v96, v104, v105\n\t"
    "v_mfma_f32_32x32x16_bf16 v[32:47], v[84:87], v[72:75], v[32:47]\n\t"
    "ds_read_b128 v[84:87], v108 offset:9216\n\t"
    "v_exp_f32 v107, v19\n\t"
    "v_exp_f32 v104, v20\n\t"
    "v_cvt_pk_bf16_f32 v97, v106, v107\n\t"
    "v_exp_f32 v105, v21\n\t"
    "v_mfma_f32_32x32x16_bf16 v[0:15], v[92:95], v[100:103], v[0:15]\n\t"
    "ds_read_b128 v[92:95], v108 offset:11264\n\t"
    "v_exp_f32 v106, v22\n\t"
    "v_cvt_pk_bf16_f32 v98, v104, v105\n\t"
    "v_exp_f32 v107, v23\n\t"
    "v_exp_f32 v104, v24\n\t"
    "v_mfma_f32_16x16x32_bf16 v[64:67], v[76:79], v[100:103], v[64:67]\n\t"
    "s_waitcnt lgkmcnt(3)\n\t"
    "global_load_lds_dwordx4 v109, s[38:39] offset:0 sc1\n\t"
    "v_cvt_pk_bf16_f32 v99, v106, v107\n\t"
    "v_exp_f32 v105, v25\n\t"
    "v_exp_f32 v106, v26\n\t"
    "v_mfma_f32_32x32x16_bf16 v[0:15], v[88:91], v[96:99], v[0:15]\n\t"
    "ds_read_b128 v[88:91], v108 offset:10240\n\t"
    "v_cvt_pk_bf16_f32 v100, v104, v105\n\t"
    "v_exp_f32 v107, v27\n\t"
    "v_exp_f32 v104, v28\n\t"
    "v_cvt_pk_bf16_f32 v101, v106, v107\n\t"
    "v_exp_f32 v105, v29\n\t"
    "v_mfma_f32_16x16x32_bf16 v[64:67], v[76:79], v[96:99], v[64:67]\n\t"
    "s_waitcnt lgkmcnt(1)\n\t"
    "ds_write_b128 v176, v[152:155] offset:0\n\t"
    "v_perm_b32 v148, v116, v112, s59\n\t"
    "v_perm_b32 v149, v124, v120, s59\n\t"
    "v_perm_b32 v150, v132, v128, s59\n\t"
    "v_perm_b32 v151, v140, v136, s59\n\t"
    "v_exp_f32 v106, v30\n\t"
    "v_cvt_pk_bf16_f32 v102, v104, v105\n\t"
    "v_exp_f32 v107, v31\n\t"
    "v_mfma_f32_32x32x16_bf16 v[16:31], v[80:83], v[68:71], v[48:63]\n\t"
    "ds_read_b128 v[80:83], v108 offset:12288\n\t"
    "v_cvt_pk_bf16_f32 v103, v106, v107\n\t"
    "v_exp_f32 v104, v32\n\t"
    "v_exp_f32 v105, v33\n\t"
    "v_exp_f32 v106, v34\n\t"
    "v_cvt_pk_bf16_f32 v96, v104, v105\n\t"
    "v_mfma_f32_32x32x16_bf16 v[16:31], v[84:87], v[72:75], v[16:31]\n\t"
    "ds_read_b128 v[84:87], v108 offset:13312\n\t"
    "v_exp_f32 v107, v35\n\t"
    "v_exp_f32 v104, v36\n\t"
    "v_cvt_pk_bf16_f32 v97, v106, v107\n\t"
    "v_exp_f32 v105, v37\n\t"
    "v_mfma_f32_32x32x16_bf16 v[0:15], v[92:95], v[100:103], v[0:15]\n\t"
    "ds_read_b128 v[92:95], v108 offset:15360\n\t"
    "v_exp_f32 v106, v38\n\t"
    "v_cvt_pk_bf16_f32 v98, v104, v105\n\t"
    "v_exp_f32 v107, v39\n\t"
    "v_exp_f32 v104, v40\n\t"
    "v_mfma_f32_16x16x32_bf16 v[64:67], v[76:79], v[100:103], v[64:67]\n\t"
    "s_waitcnt lgkmcnt(3)\n\t"
    "global_load_lds_dwordx4 v109, s[38:39] offset:1024 sc1\n\t"
    "v_cvt_pk_bf16_f32 v99, v106, v107\n\t"
    "v_exp_f32 v105, v41\n\t"
    "v_exp_f32 v106, v42\n\t"
    "v_mfma_f32_32x32x16_bf16 v[0:15], v[88:91], v[96:99], v[0:15]\n\t"
    "ds_read_b128 v[88:91], v108 offset:14336\n\t"
    "v_cvt_pk_bf16_f32 v100, v104, v105\n\t"
    "v_exp_f32 v107, v43\n\t"
    "v_exp_f32 v104, v44\n\t"
    "v_cvt_pk_bf16_f32 v101, v106, v107\n\t"
    "v_exp_f32 v105, v45\n\t"
    "v_mfma_f32_16x16x32_bf16 v[64:67], v[76:79], v[96:99], v[64:67]\n\t"
    "s_waitcnt lgkmcnt(1)\n\t"
    "v_mfma_f32_16x16x32_bf16 v[156:159], v[148:151], v[160:163], 0\n\t"
    "buffer_store_dwordx4 v[148:151], v109, s[40:43], 0 offen offset:1024 sc1\n\t"
    "v_exp_f32 v106, v46\n\t"
    "v_cvt_pk_bf16_f32 v102, v104, v105\n\t"
    "v_exp_f32 v107, v47\n\t"
    "s_setprio 1\n\t"
    "v_mfma_f32_32x32x16_bf16 v[32:47], v[80:83], v[68:71], v[48:63]\n\t"
    "ds_read_b128 v[80:83], v108 offset:16384\n\t"
    "v_cvt_pk_bf16_f32 v103, v106, v107\n\t"
    "v_exp_f32 v104, v16\n\t"
    "v_exp_f32 v105, v17\n\t"
    "v_exp_f32 v106, v18\n\t"
    "v_cvt_pk_bf16_f32 v96, v104, v105\n\t"
    "v_mfma_f32_32x32x16_bf16 v[32:47], v[84:87], v[72:75], v[32:47]\n\t"
    "ds_read_b128 v[84:87], v108 offset:17408\n\t"
    "v_exp_f32 v107, v19\n\t"
    "v_exp_f32 v104, v20\n\t"
    "v_cvt_pk_bf16_f32 v97, v106, v107\n\t"
    "v_exp_f32 v105, v21\n\t"
    "v_mfma_f32_32x32x16_bf16 v[0:15], v[92:95], v[100:103], v[0:15]\n\t"
    "ds_read_b128 v[92:95], v108 offset:19456\n\t"
    "v_exp_f32 v106, v22\n\t"
    "v_cvt_pk_bf16_f32 v98, v104, v105\n\t"
    "v_exp_f32 v107, v23\n\t"
    "v_exp_f32 v104, v24\n\t"
    "v_mfma_f32_16x16x32_bf16 v[64:67], v[76:79], v[100:103], v[64:67]\n\t"
    "s_waitcnt lgkmcnt(3)\n\t"
    "global_load_lds_dwordx4 v109, s[38:39] offset:2048 sc1\n\t"
    "v_cvt_pk_bf16_f32 v99, v106, v107\n\t"
    "v_exp_f32 v105, v25\n\t"
    "v_exp_f32 v106, v26\n\t"
    "v_mfma_f32_32x32x16_bf16 v[0:15], v[88:91], v[96:99], v[0:15]\n\t"
    "ds_read_b128 v[88:91], v108 offset:18432\n\t"
    "v_cvt_pk_bf16_f32 v100, v104, v105\n\t"
    "v_exp_f32 v107, v27\n\t"
    "v_exp_f32 v104, v28\n\t"
    "v_cvt_pk_bf16_f32 v101, v106, v107\n\t"
    "v_exp_f32 v105, v29\n\t"
    "v_mfma_f32_16x16x32_bf16 v[64:67], v[76:79], v[96:99], v[64:67]\n\t"
    "s_waitcnt lgkmcnt(1)\n\t"
    "v_exp_f32 v106, v30\n\t"
    "v_cvt_pk_bf16_f32 v102, v104, v105\n\t"
    "v_exp_f32 v107, v31\n\t"
    "v_mfma_f32_32x32x16_bf16 v[16:31], v[80:83], v[68:71], v[48:63]\n\t"
    "ds_read_b128 v[80:83], v108 offset:20480\n\t"
    "v_cvt_pk_bf16_f32 v103, v106, v107\n\t"
    "v_exp_f32 v104, v32\n\t"
    "v_exp_f32 v105, v33\n\t"
    "v_exp_f32 v106, v34\n\t"
    "v_cvt_pk_bf16_f32 v96, v104, v105\n\t"
    "v_mfma_f32_32x32x16_bf16 v[16:31], v[84:87], v[72:75], v[16:31]\n\t"
    "ds_read_b128 v[84:87], v108 offset:21504\n\t"
    "v_exp_f32 v107, v35\n\t"
    "v_exp_f32 v104, v36\n\t"
    "v_cvt_pk_bf16_f32 v97, v106, v107\n\t"
    "v_exp_f32 v105, v37\n\t"
    "v_mfma_f32_32x32x16_bf16 v[0:15], v[92:95], v[100:103], v[0:15]\n\t"
    "ds_read_b128 v[92:95], v108 offset:23552\n\t"
    "v_exp_f32 v106, v38\n\t"
    "v_cvt_pk_bf16_f32 v98, v104, v105\n\t"
    "v_exp_f32 v107, v39\n\t"
    "v_exp_f32 v104, v40\n\t"
    "v_mfma_f32_16x16x32_bf16 v[64:67], v[76:79], v[100:103], v[64:67]\n\t"
    "s_waitcnt lgkmcnt(3)\n\t"
    "global_load_lds_dwordx4 v109, s[38:39] offset:3072 sc1\n\t"
    "v_cvt_pk_bf16_f32 v99, v106, v107\n\t"
    "v_exp_f32 v105, v41\n\t"
    "v_exp_f32 v106, v42\n\t"
    "v_mfma_f32_32x32x16_bf16 v[0:15], v[88:91], v[96:99], v[0:15]\n\t"
    "ds_read_b128 v[88:91], v108 offset:22528\n\t"
    "v_cvt_pk_bf16_f32 v100, v104, v105\n\t"
    "v_exp_f32 v107, v43\n\t"
    "v_exp_f32 v104, v44\n\t"
    "v_cvt_pk_bf16_f32 v101, v106, v107\n\t"
    "v_exp_f32 v105, v45\n\t"
    "v_mfma_f32_16x16x32_bf16 v[64:67], v[76:79], v[96:99], v[64:67]\n\t"
    "s_waitcnt lgkmcnt(1)\n\t"
    "ds_write_b128 v176, v[156:159] offset:128\n\t"
    "v_exp_f32 v106, v46\n\t"
    "v_cvt_pk_bf16_f32 v102, v104, v105\n\t"
    "v_exp_f32 v107, v47\n\t"
    "s_setprio 0\n\t"
    "v_mfma_f32_32x32x16_bf16 v[32:47], v[80:83], v[68:71], v[48:63]\n\t"
    "ds_read_b128 v[80:83], v108 offset:24576\n\t"
    "v_cvt_pk_bf16_f32 v103, v106, v107\n\t"
    "v_exp_f32 v104, v16\n\t"
    "v_exp_f32 v105, v17\n\t"
    "v_exp_f32 v106, v18\n\t"
    "v_cvt_pk_bf16_f32 v96, v104, v105\n\t"
    "v_mfma_f32_32x32x16_bf16 v[32:47], v[84:87], v[72:75], v[32:47]\n\t"
    "ds_read_b128 v[84:87], v108 offset:25600\n\t"
    "v_exp_f32 v107, v19\n\t"
    "v_exp_f32 v104, v20\n\t"
    "v_cvt_pk_bf16_f32 v97, v106, v107\n\t"
    "v_exp_f32 v105, v21\n\t"
    "v_mfma_f32_32x32x16_bf16 v[0:15], v[92:95], v[100:103], v[0:15]\n\t"
    "ds_read_b128 v[92:95], v108 offset:27648\n\t"
    "v_exp_f32 v106, v22\n\t"
    "v_cvt_pk_bf16_f32 v98, v104, v105\n\t"
    "v_exp_f32 v107, v23\n\t"
    "v_exp_f32 v104, v24\n\t"
    "v_mfma_f32_16x16x32_bf16 v[64:67], v[76:79], v[100:103], v[64:67]\n\t"
    "s_waitcnt lgkmcnt(3)\n\t"
    "v_cvt_pk_bf16_f32 v99, v106, v107\n\t"
    "v_exp_f32 v105, v25\n\t"
    "v_exp_f32 v106, v26\n\t"
    "v_mfma_f32_32x32x16_bf16 v[0:15], v[88:91], v[96:99], v[0:15]\n\t"
    "ds_read_b128 v[88:91], v108 offset:26624\n\t"
    "v_cvt_pk_bf16_f32 v100, v104, v105\n\t"
    "v_exp_f32 v107, v27\n\t"
    "v_exp_f32 v104, v28\n\t"
    "v_cvt_pk_bf16_f32 v101, v106, v107\n\t"
    "v_exp_f32 v105, v29\n\t"
    "v_mfma_f32_16x16x32_bf16 v[64:67], v[76:79], v[96:99], v[64:67]\n\t"
    "s_waitcnt lgkmcnt(1)\n\t"
    "global_load_dword v111, v[172:173], off\n\t"
    "v_exp_f32 v106, v30\n\t"
    "v_cvt_pk_bf16_f32 v102, v104, v105\n\t"
    "v_exp_f32 v107, v31\n\t"
    "v_mfma_f32_32x32x16_bf16 v[16:31], v[80:83], v[68:71], v[48:63]\n\t"
    "ds_read_b128 v[80:83], v108 offset:28672\n\t"
    "v_cvt_pk_bf16_f32 v103, v106, v107\n\t"
    "v_exp_f32 v104, v32\n\t"
    "v_exp_f32 v105, v33\n\t"
    "v_exp_f32 v106, v34\n\t"
    "v_cvt_pk_bf16_f32 v96, v104, v105\n\t"
    "v_mfma_f32_32x32x16_bf16 v[16:31], v[84:87], v[72:75], v[16:31]\n\t"
    "ds_read_b128 v[84:87], v108 offset:29696\n\t"
    "v_exp_f32 v107, v35\n\t"
    "v_exp_f32 v104, v36\n\t"
    "v_cvt_pk_bf16_f32 v97, v106, v107\n\t"
    "v_exp_f32 v105, v37\n\t"
    "v_mfma_f32_32x32x16_bf16 v[0:15], v[92:95], v[100:103], v[0:15]\n\t"
    "ds_read_b128 v[92:95], v108 offset:31744\n\t"
    "v_exp_f32 v106, v38\n\t"
    "v_cvt_pk_bf16_f32 v98, v104, v105\n\t"
    "v_exp_f32 v107, v39\n\t"
    "v_exp_f32 v104, v40\n\t"
    "v_mfma_f32_16x16x32_bf16 v[64:67], v[76:79], v[100:103], v[64:67]\n\t"
    "s_waitcnt lgkmcnt(3)\n\t"
    "v_cvt_pk_bf16_f32 v99, v106, v107\n\t"
    "v_exp_f32 v105, v41\n\t"
    "v_exp_f32 v106, v42\n\t"
    "v_mfma_f32_32x32x16_bf16 v[0:15], v[88:91], v[96:99], v[0:15]\n\t"
    "ds_read_b128 v[88:91], v108 offset:30720\n\t"
    "v_cvt_pk_bf16_f32 v100, v104, v105\n\t"
    "v_exp_f32 v107, v43\n\t"
    "v_exp_f32 v104, v44\n\t"
    "v_cvt_pk_bf16_f32 v101, v106, v107\n\t"
    "v_exp_f32 v105, v45\n\t"
    "v_mfma_f32_16x16x32_bf16 v[64:67], v[76:79], v[96:99], v[64:67]\n\t"
    "v_lshl_add_u64 v[172:173], v[172:173], 0, v[174:175]\n\t"
    "v_exp_f32 v106, v46\n\t"
    "v_cvt_pk_bf16_f32 v102, v104, v105\n\t"
    "v_exp_f32 v107, v47\n\t"
    "s_waitcnt vmcnt(7) lgkmcnt(0)\n\t"
    "s_barrier\n\t"
    "v_add_u32 v108, 98304, v180\n\t"
    "s_setprio 3\n\t"
    "v_mfma_f32_32x32x16_bf16 v[32:47], v[80:83], v[68:71], v[48:63]\n\t"
    "ds_read_b128 v[80:83], v108 offset:0\n\t"
    "v_cvt_pk_bf16_f32 v103, v106, v107\n\t"
    "v_exp_f32 v104, v16\n\t"
    "v_exp_f32 v105, v17\n\t"
    "v_exp_f32 v106, v18\n\t"
    "v_cvt_pk_bf16_f32 v96, v104, v105\n\t"
    "v_mfma_f32_32x32x16_bf16 v[32:47], v[84:87], v[72:75], v[32:47]\n\t"
    "ds_read_b128 v[84:87], v108 offset:1024\n\t"
    "v_exp_f32 v107, v19\n\t"
    "v_exp_f32 v104, v20\n\t"
    "v_cvt_pk_bf16_f32 v97, v106, v107\n\t"
    "v_exp_f32 v105, v21\n\t"
    "v_mfma_f32_32x32x16_bf16 v[0:15], v[92:95], v[100:103], v[0:15]\n\t"
    "ds_read_b128 v[92:95], v108 offset:3072\n\t"
    "v_exp_f32 v106, v22\n\t"
    "v_cvt_pk_bf16_f32 v98, v104, v105\n\t"
    "v_exp_f32 v107, v23\n\t"
    "v_exp_f32 v104, v24\n\t"
    "v_mfma_f32_16x16x32_bf16 v[64:67], v[76:79], v[100:103], v[64:67]\n\t"
    "s_waitcnt lgkmcnt(3)\n\t"
    "v_cvt_pk_bf16_f32 v99, v106, v107\n\t"
    "v_exp_f32 v105, v25\n\t"
    "v_exp_f32 v106, v26\n\t"
    "v_mfma_f32_32x32x16_bf16 v[0:15], v[88:91], v[96:99], v[0:15]\n\t"
    "ds_read_b128 v[88:91], v108 offset:2048\n\t"
    "v_cvt_pk_bf16_f32 v100, v104, v105\n\t"
    "v_exp_f32 v107, v27\n\t"
    "v_exp_f32 v104, v28\n\t"
    "v_cvt_pk_bf16_f32 v101, v106, v107\n\t"
    "v_exp_f32 v105, v29\n\t"
    "v_mfma_f32_16x16x32_bf16 v[64:67], v[76:79], v[96:99], v[64:67]\n\t"
    "s_waitcnt lgkmcnt(1)\n\t"
    "v_perm_b32 v144, v117, v113, s58\n\t"
    "v_perm_b32 v145, v125, v121, s58\n\t"
    "v_perm_b32 v146, v133, v129, s58\n\t"
    "v_perm_b32 v147, v141, v137, s58\n\t"
    "v_exp_f32 v106, v30\n\t"
    "v_cvt_pk_bf16_f32 v102, v104, v105\n\t"
    "v_exp_f32 v107, v31\n\t"
    "v_mfma_f32_32x32x16_bf16 v[16:31], v[80:83], v[68:71], v[48:63]\n\t"
    "ds_read_b128 v[80:83], v108 offset:4096\n\t"
    "v_cvt_pk_bf16_f32 v103, v106, v107\n\t"
    "v_exp_f32 v104, v32\n\t"
    "v_exp_f32 v105, v33\n\t"
    "v_exp_f32 v106, v34\n\t"
    "v_cvt_pk_bf16_f32 v96, v104, v105\n\t"
    "v_mfma_f32_32x32x16_bf16 v[16:31], v[84:87], v[72:75], v[16:31]\n\t"
    "ds_read_b128 v[84:87], v108 offset:5120\n\t"
    "v_exp_f32 v107, v35\n\t"
    "v_exp_f32 v104, v36\n\t"
    "v_cvt_pk_bf16_f32 v97, v106, v107\n\t"
    "v_exp_f32 v105, v37\n\t"
    "v_mfma_f32_32x32x16_bf16 v[0:15], v[92:95], v[100:103], v[0:15]\n\t"
    "ds_read_b128 v[92:95], v108 offset:7168\n\t"
    "v_exp_f32 v106, v38\n\t"
    "v_cvt_pk_bf16_f32 v98, v104, v105\n\t"
    "v_exp_f32 v107, v39\n\t"
    "v_exp_f32 v104, v40\n\t"
    "v_mfma_f32_16x16x32_bf16 v[64:67], v[76:79], v[100:103], v[64:67]\n\t"
    "s_waitcnt lgkmcnt(3)\n\t"
    "s_add_u32 s37, s36, 172032\n\t"
    "s_and_b32 s37, s37, 0x3ffff\n\t"
    "s_add_u32 s38, s34, s37\n\t"
    "s_addc_u32 s39, s35, 0\n\t"
    "s_add_u32 m0, s44, 32768\n\t"
    "v_cvt_pk_bf16_f32 v99, v106, v107\n\t"
    "v_exp_f32 v105, v41\n\t"
    "v_exp_f32 v106, v42\n\t"
    "v_mfma_f32_32x32x16_bf16 v[0:15], v[88:91], v[96:99], v[0:15]\n\t"
    "ds_read_b128 v[88:91], v108 offset:6144\n\t"
    "v_cvt_pk_bf16_f32 v100, v104, v105\n\t"
    "v_exp_f32 v107, v43\n\t"
    "v_exp_f32 v104, v44\n\t"
    "v_cvt_pk_bf16_f32 v101, v106, v107\n\t"
    "v_exp_f32 v105, v45\n\t"
    "v_mfma_f32_16x16x32_bf16 v[64:67], v[76:79], v[96:99], v[64:67]\n\t"
    "s_waitcnt lgkmcnt(1)\n\t"
    "v_mfma_f32_16x16x32_bf16 v[152:155], v[144:147], v[160:163], 0\n\t"
    "buffer_store_dwordx4 v[144:147], v109, s[40:43], 0 offen offset:2048 sc1\n\t"
    "v_exp_f32 v106, v46\n\t"
    "v_cvt_pk_bf16_f32 v102, v104, v105\n\t"
    "v_exp_f32 v107, v47\n\t"
    "s_setprio 2\n\t"
    "v_mfma_f32_32x32x16_bf16 v[32:47], v[80:83], v[68:71], v[48:63]\n\t"
    "ds_read_b128 v[80:83], v108 offset:8192\n\t"
    "v_cvt_pk_bf16_f32 v103, v106, v107\n\t"
    "v_exp_f32 v104, v16\n\t"
    "v_exp_f32 v105, v17\n\t"
    "v_exp_f32 v106, v18\n\t"
    "v_cvt_pk_bf16_f32 v96, v104, v105\n\t"
    "v_mfma_f32_32x32x16_bf16 v[32:47], v[84:87], v[72:75], v[32:47]\n\t"
    "ds_read_b128 v[84:87], v108 offset:9216\n\t"
    "v_exp_f32 v107, v19\n\t"
    "v_exp_f32 v104, v20\n\t"
    "v_cvt_pk_bf16_f32 v97, v106, v107\n\t"
    "v_exp_f32 v105, v21\n\t"
    "v_mfma_f32_32x32x16_bf16 v[0:15], v[92:95], v[100:103], v[0:15]\n\t"
    "ds_read_b128 v[92:95], v108 offset:11264\n\t"
    "v_exp_f32 v106, v22\n\t"
    "v_cvt_pk_bf16_f32 v98, v104, v105\n\t"
    "v_exp_f32 v107, v23\n\t"
    "v_exp_f32 v104, v24\n\t"
    "v_mfma_f32_16x16x32_bf16 v[64:67], v[76:79], v[100:103], v[64:67]\n\t"
    "s_waitcnt lgkmcnt(3)\n\t"
    "global_load_lds_dwordx4 v109, s[38:39] offset:0 sc1\n\t"
    "v_cvt_pk_bf16_f32 v99, v106, v107\n\t"
    "v_exp_f32 v105, v25\n\t"
    "v_exp_f32 v106, v26\n\t"
    "v_mfma_f32_32x32x16_bf16 v[0:15], v[88:91], v[96:99], v[0:15]\n\t"
    "ds_read_b128 v[88:91], v108 offset:10240\n\t"
    "v_cvt_pk_bf16_f32 v100, v104, v105\n\t"
    "v_exp_f32 v107, v27\n\t"
    "v_exp_f32 v104, v28\n\t"
    "v_cvt_pk_bf16_f32 v101, v106, v107\n\t"
    "v_exp_f32 v105, v29\n\t"
    "v_mfma_f32_16x16x32_bf16 v[64:67], v[76:79], v[96:99], v[64:67]\n\t"
    "s_waitcnt lgkmcnt(1)\n\t"
    "ds_write_b128 v176, v[152:155] offset:256\n\t"
    "v_perm_b32 v148, v117, v113, s59\n\t"
    "v_perm_b32 v149, v125, v121, s59\n\t"
    "v_perm_b32 v150, v133, v129, s59\n\t"
    "v_perm_b32 v151, v141, v137, s59\n\t"
    "v_exp_f32 v106, v30\n\t"
    "v_cvt_pk_bf16_f32 v102, v104, v105\n\t"
    "v_exp_f32 v107, v31\n\t"
    "v_mfma_f32_32x32x16_bf16 v[16:31], v[80:83], v[68:71], v[48:63]\n\t"
    "ds_read_b128 v[80:83], v108 offset:12288\n\t"
    "v_cvt_pk_bf16_f32 v103, v106, v107\n\t"
    "v_exp_f32 v104, v32\n\t"
    "v_exp_f32 v105, v33\n\t"
    "v_exp_f32 v106, v34\n\t"
    "v_cvt_pk_bf16_f32 v96, v104, v105\n\t"
    "v_mfma_f32_32x32x16_bf16 v[16:31], v[84:87], v[72:75], v[16:31]\n\t"
    "ds_read_b128 v[84:87], v108 offset:13312\n\t"
    "v_exp_f32 v107, v35\n\t"
    "v_exp_f32 v104, v36\n\t"
    "v_cvt_pk_bf16_f32 v97, v106, v107\n\t"
    "v_exp_f32 v105, v37\n\t"
    "v_mfma_f32_32x32x16_bf16 v[0:15], v[92:95], v[100:103], v[0:15]\n\t"
    "ds_read_b128 v[92:95], v108 offset:15360\n\t"
    "v_exp_f32 v106, v38\n\t"
    "v_cvt_pk_bf16_f32 v98, v104, v105\n\t"
    "v_exp_f32 v107, v39\n\t"
    "v_exp_f32 v104, v40\n\t"
    "v_mfma_f32_16x16x32_bf16 v[64:67], v[76:79], v[100:103], v[64:67]\n\t"
    "s_waitcnt lgkmcnt(3)\n\t"
    "global_load_lds_dwordx4 v109, s[38:39] offset:1024 sc1\n\t"
    "v_cvt_pk_bf16_f32 v99, v106, v107\n\t"
    "v_exp_f32 v105, v41\n\t"
    "v_exp_f32 v106, v42\n\t"
    "v_mfma_f32_32x32x16_bf16 v[0:15], v[88:91], v[96:99], v[0:15]\n\t"
    "ds_read_b128 v[88:91], v108 offset:14336\n\t"
    "v_cvt_pk_bf16_f32 v100, v104, v105\n\t"
    "v_exp_f32 v107, v43\n\t"
    "v_exp_f32 v104, v44\n\t"
    "v_cvt_pk_bf16_f32 v101, v106, v107\n\t"
    "v_exp_f32 v105, v45\n\t"
    "v_mfma_f32_16x16x32_bf16 v[64:67], v[76:79], v[96:99], v[64:67]\n\t"
    "s_waitcnt lgkmcnt(1)\n\t"
    "v_mfma_f32_16x16x32_bf16 v[156:159], v[148:151], v[160:163], 0\n\t"
    "buffer_store_dwordx4 v[148:151], v109, s[40:43], 0 offen offset:3072 sc1\n\t"
    "v_exp_f32 v106, v46\n\t"
    "v_cvt_pk_bf16_f32 v102, v104, v105\n\t"
    "v_exp_f32 v107, v47\n\t"
    "s_setprio 1\n\t"
    "v_mfma_f32_32x32x16_bf16 v[32:47], v[80:83], v[68:71], v[48:63]\n\t"
    "ds_read_b128 v[80:83], v108 offset:16384\n\t"
    "v_cvt_pk_bf16_f32 v103, v106, v107\n\t"
    "v_exp_f32 v104, v16\n\t"
    "v_exp_f32 v105, v17\n\t"
    "v_exp_f32 v106, v18\n\t"
    "v_cvt_pk_bf16_f32 v96, v104, v105\n\t"
    "v_mfma_f32_32x32x16_bf16 v[32:47], v[84:87], v[72:75], v[32:47]\n\t"
    "ds_read_b128 v[84:87], v108 offset:17408\n\t"
    "v_exp_f32 v107, v19\n\t"
    "v_exp_f32 v104, v20\n\t"
    "v_cvt_pk_bf16_f32 v97, v106, v107\n\t"
    "v_exp_f32 v105, v21\n\t"
    "v_mfma_f32_32x32x16_bf16 v[0:15], v[92:95], v[100:103], v[0:15]\n\t"
    "ds_read_b128 v[92:95], v108 offset:19456\n\t"
    "v_exp_f32 v106, v22\n\t"
    "v_cvt_pk_bf16_f32 v98, v104, v105\n\t"
    "v_exp_f32 v107, v23\n\t"
    "v_exp_f32 v104, v24\n\t"
    "v_mfma_f32_16x16x32_bf16 v[64:67], v[76:79], v[100:103], v[64:67]\n\t"
    "s_waitcnt lgkmcnt(3)\n\t"
    "global_load_lds_dwordx4 v109, s[38:39] offset:2048 sc1\n\t"
    "v_cvt_pk_bf16_f32 v99, v106, v107\n\t"
    "v_exp_f32 v105, v25\n\t"
    "v_exp_f32 v106, v26\n\t"
    "v_mfma_f32_32x32x16_bf16 v[0:15], v[88:91], v[96:99], v[0:15]\n\t"
    "ds_read_b128 v[88:91], v108 offset:18432\n\t"
    "v_cvt_pk_bf16_f32 v100, v104, v105\n\t"
    "v_exp_f32 v107, v27\n\t"
    "v_exp_f32 v104, v28\n\t"
    "v_cvt_pk_bf16_f32 v101, v106, v107\n\t"
    "v_exp_f32 v105, v29\n\t"
    "v_mfma_f32_16x16x32_bf16 v[64:67], v[76:79], v[96:99], v[64:67]\n\t"
    "s_waitcnt lgkmcnt(1)\n\t"
    "v_exp_f32 v106, v30\n\t"
    "v_cvt_pk_bf16_f32 v102, v104, v105\n\t"
    "v_exp_f32 v107, v31\n\t"
    "v_mfma_f32_32x32x16_bf16 v[16:31], v[80:83], v[68:71], v[48:63]\n\t"
    "ds_read_b128 v[80:83], v108 offset:20480\n\t"
    "v_cvt_pk_bf16_f32 v103, v106, v107\n\t"
    "v_exp_f32 v104, v32\n\t"
    "v_exp_f32 v105, v33\n\t"
    "v_exp_f32 v106, v34\n\t"
    "v_cvt_pk_bf16_f32 v96, v104, v105\n\t"
    "v_mfma_f32_32x32x16_bf16 v[16:31], v[84:87], v[72:75], v[16:31]\n\t"
    "ds_read_b128 v[84:87], v108 offset:21504\n\t"
    "v_exp_f32 v107, v35\n\t"
    "v_exp_f32 v104, v36\n\t"
    "v_cvt_pk_bf16_f32 v97, v106, v107\n\t"
    "v_exp_f32 v105, v37\n\t"
    "v_mfma_f32_32x32x16_bf16 v[0:15], v[92:95], v[100:103], v[0:15]\n\t"
    "ds_read_b128 v[92:95], v108 offset:23552\n\t"
    "v_exp_f32 v106, v38\n\t"
    "v_cvt_pk_bf16_f32 v98, v104, v105\n\t"
    "v_exp_f32 v107, v39\n\t"
    "v_exp_f32 v104, v40\n\t"
    "v_mfma_f32_16x16x32_bf16 v[64:67], v[76:79], v[100:103], v[64:67]\n\t"
    "s_waitcnt lgkmcnt(3)\n\t"
    "global_load_lds_dwordx4 v109, s[38:39] offset:3072 sc1\n\t"
    "v_cvt_pk_bf16_f32 v99, v106, v107\n\t"
    "v_exp_f32 v105, v41\n\t"
    "v_exp_f32 v106, v42\n\t"
    "v_mfma_f32_32x32x16_bf16 v[0:15], v[88:91], v[96:99], v[0:15]\n\t"
    "ds_read_b128 v[88:91], v108 offset:22528\n\t"
    "v_cvt_pk_bf16_f32 v100, v104, v105\n\t"
    "v_exp_f32 v107, v43\n\t"
    "v_exp_f32 v104, v44\n\t"
    "v_cvt_pk_bf16_f32 v101, v106, v107\n\t"
    "v_exp_f32 v105, v45\n\t"
    "v_mfma_f32_16x16x32_bf16 v[64:67], v[76:79], v[96:99], v[64:67]\n\t"
    "s_waitcnt lgkmcnt(1)\n\t"
    "ds_write_b128 v176, v[156:159] offset:384\n\t"
    "v_exp_f32 v106, v46\n\t"
    "v_cvt_pk_bf16_f32 v102, v104, v105\n\t"
    "v_exp_f32 v107, v47\n\t"
    "s_setprio 0\n\t"
    "v_mfma_f32_32x32x16_bf16 v[32:47], v[80:83], v[68:71], v[48:63]\n\t"
    "ds_read_b128 v[80:83], v108 offset:24576\n\t"
    "v_cvt_pk_bf16_f32 v103, v106, v107\n\t"
    "v_exp_f32 v104, v16\n\t"
    "v_exp_f32 v105, v17\n\t"
    "v_exp_f32 v106, v18\n\t"
    "v_cvt_pk_bf16_f32 v96, v104, v105\n\t"
    "v_mfma_f32_32x32x16_bf16 v[32:47], v[84:87], v[72:75], v[32:47]\n\t"
    "ds_read_b128 v[84:87], v108 offset:25600\n\t"
    "v_exp_f32 v107, v19\n\t"
    "v_exp_f32 v104, v20\n\t"
    "v_cvt_pk_bf16_f32 v97, v106, v107\n\t"
    "v_exp_f32 v105, v21\n\t"
    "v_mfma_f32_32x32x16_bf16 v[0:15], v[92:95], v[100:103], v[0:15]\n\t"
    "ds_read_b128 v[92:95], v108 offset:27648\n\t"
    "v_exp_f32 v106, v22\n\t"
    "v_cvt_pk_bf16_f32 v98, v104, v105\n\t"
    "v_exp_f32 v107, v23\n\t"
    "v_exp_f32 v104, v24\n\t"
    "v_mfma_f32_16x16x32_bf16 v[64:67], v[76:79], v[100:103], v[64:67]\n\t"
    "s_waitcnt lgkmcnt(3)\n\t"
    "v_cvt_pk_bf16_f32 v99, v106, v107\n\t"
    "v_exp_f32 v105, v25\n\t"
    "v_exp_f32 v106, v26\n\t"
    "v_mfma_f32_32x32x16_bf16 v[0:15], v[88:91], v[96:99], v[0:15]\n\t"
    "ds_read_b128 v[88:91], v108 offset:26624\n\t"
    "v_cvt_pk_bf16_f32 v100, v104, v105\n\t"
    "v_exp_f32 v107, v27\n\t"
    "v_exp_f32 v104, v28\n\t"
    "v_cvt_pk_bf16_f32 v101, v106, v107\n\t"
    "v_exp_f32 v105, v29\n\t"
    "v_mfma_f32_16x16x32_bf16 v[64:67], v[76:79], v[96:99], v[64:67]\n\t"
    "s_waitcnt lgkmcnt(1)\n\t"
    "global_load_dword v111, v[172:173], off\n\t"
    "v_exp_f32 v106, v30\n\t"
    "v_cvt_pk_bf16_f32 v102, v104, v105\n\t"
    "v_exp_f32 v107, v31\n\t"
    "v_mfma_f32_32x32x16_bf16 v[16:31], v[80:83], v[68:71], v[48:63]\n\t"
    "ds_read_b128 v[80:83], v108 offset:28672\n\t"
    "v_cvt_pk_bf16_f32 v103, v106, v107\n\t"
    "v_exp_f32 v104, v32\n\t"
    "v_exp_f32 v105, v33\n\t"
    "v_exp_f32 v106, v34\n\t"
    "v_cvt_pk_bf16_f32 v96, v104, v105\n\t"
    "v_mfma_f32_32x32x16_bf16 v[16:31], v[84:87], v[72:75], v[16:31]\n\t"
    "ds_read_b128 v[84:87], v108 offset:29696\n\t"
    "v_exp_f32 v107, v35\n\t"
    "v_exp_f32 v104, v36\n\t"
    "v_cvt_pk_bf16_f32 v97, v106, v107\n\t"
    "v_exp_f32 v105, v37\n\t"
    "v_mfma_f32_32x32x16_bf16 v[0:15], v[92:95], v[100:103], v[0:15]\n\t"
    "ds_read_b128 v[92:95], v108 offset:31744\n\t"
    "v_exp_f32 v106, v38\n\t"
    "v_cvt_pk_bf16_f32 v98, v104, v105\n\t"
    "v_exp_f32 v107, v39\n\t"
    "v_exp_f32 v104, v40\n\t"
    "v_mfma_f32_16x16x32_bf16 v[64:67], v[76:79], v[100:103], v[64:67]\n\t"
    "s_waitcnt lgkmcnt(3)\n\t"
    "v_cvt_pk_bf16_f32 v99, v106, v107\n\t"
    "v_exp_f32 v105, v41\n\t"
    "v_exp_f32 v106, v42\n\t"
    "v_mfma_f32_32x32x16_bf16 v[0:15], v[88:91], v[96:99], v[0:15]\n\t"
    "ds_read_b128 v[88:91], v108 offset:30720\n\t"
    "v_cvt_pk_bf16_f32 v100, v104, v105\n\t"
    "v_exp_f32 v107, v43\n\t"
    "v_exp_f32 v104, v44\n\t"
    "v_cvt_pk_bf16_f32 v101, v106, v107\n\t"
    "v_exp_f32 v105, v45\n\t"
    "v_mfma_f32_16x16x32_bf16 v[64:67], v[76:79], v[96:99], v[64:67]\n\t"
    "v_lshl_add_u64 v[172:173], v[172:173], 0, v[174:175]\n\t"
    "v_exp_f32 v106, v46\n\t"
    "v_cvt_pk_bf16_f32 v102, v104, v105\n\t"
    "v_exp_f32 v107, v47\n\t"
    "s_waitcnt vmcnt(7) lgkmcnt(0)\n\t"
        "" : AOUT, "+{s[52:53]}"(gsrc)
        : AIN, "{v[164:165]}"(src), "{v[166:167]}"(src + 2 * F_ * 2), "{v[168:169]}"(src + 16 * F_ * 2), "{v[170:171]}"(src + 18 * F_ * 2), "{v178}"((lane >> 3) * 12288 + (lane & 7) * 16), "{s46}"(gl)
        : ACLOB);
    if (wid < 4) {
        f32x4 g = {};
        const unsigned a = gl + (8 * lg + qq) * 128 + wid * 32 + (li & 3) * 8;
#pragma unroll
        for (int i = 0; i < 4; ++i) g = MFMA16(ldstr2(a + i * 4096, a + i * 4096 + 512), lds128(sl + i * 64 + lg * 16), g);
        if (li == 0) st16wt(mkrs(ws + WS_MODG + ((sj & 7) * 2048 + (sj >> 3) * 64 + wid * 16) * 4, 64), lg * 16, 0, __builtin_bit_cast(u32x4, g));
    }
    BAR();
    {
        const char* os0 = osrc(0); const char* os2 = osrc(2);
        asm volatile(
    "v_add_u32 v108, 131072, v180\n\t"
    "s_setprio 3\n\t"
    "v_mfma_f32_32x32x16_bf16 v[32:47], v[80:83], v[68:71], v[48:63]\n\t"
    "ds_read_b128 v[80:83], v108 offset:0\n\t"
    "v_cvt_pk_bf16_f32 v103, v106, v107\n\t"
    "v_exp_f32 v104, v16\n\t"
    "v_exp_f32 v105, v17\n\t"
    "v_exp_f32 v106, v18\n\t"
    "v_cvt_pk_bf16_f32 v96, v104, v105\n\t"
    "v_mfma_f32_32x32x16_bf16 v[32:47], v[84:87], v[72:75], v[32:47]\n\t"
    "ds_read_b128 v[84:87], v108 offset:1024\n\t"
    "v_exp_f32 v107, v19\n\t"
    "v_exp_f32 v104, v20\n\t"
    "v_cvt_pk_bf16_f32 v97, v106, v107\n\t"
    "v_exp_f32 v105, v21\n\t"
    "v_mfma_f32_32x32x16_bf16 v[0:15], v[92:95], v[100:103], v[0:15]\n\t"
    "ds_read_b128 v[92:95], v108 offset:3072\n\t"
    "v_exp_f32 v106, v22\n\t"
    "v_cvt_pk_bf16_f32 v98, v104, v105\n\t"
    "v_exp_f32 v107, v23\n\t"
    "v_exp_f32 v104, v24\n\t"
    "v_mfma_f32_16x16x32_bf16 v[64:67], v[76:79], v[100:103], v[64:67]\n\t"
    "s_waitcnt lgkmcnt(3)\n\t"
    "v_cvt_pk_bf16_f32 v99, v106, v107\n\t"
    "v_exp_f32 v105, v25\n\t"
    "v_exp_f32 v106, v26\n\t"
    "v_mfma_f32_32x32x16_bf16 v[0:15], v[88:91], v[96:99], v[0:15]\n\t"
    "ds_read_b128 v[88:91], v108 offset:2048\n\t"
    "v_cvt_pk_bf16_f32 v100, v104, v105\n\t"
    "v_exp_f32 v107, v27\n\t"
    "v_exp_f32 v104, v28\n\t"
    "v_cvt_pk_bf16_f32 v101, v106, v107\n\t"
    "v_exp_f32 v105, v29\n\t"
    "v_mfma_f32_16x16x32_bf16 v[64:67], v[76:79], v[96:99], v[64:67]\n\t"
    "s_waitcnt lgkmcnt(1)\n\t"
    "v_perm_b32 v144, v118, v114, s58\n\t"
    "v_perm_b32 v145, v126, v122, s58\n\t"
    "v_perm_b32 v146, v134, v130, s58\n\t"
    "v_perm_b32 v147, v142, v138, s58\n\t"
    "v_exp_f32 v106, v30\n\t"
    "v_cvt_pk_bf16_f32 v102, v104, v105\n\t"
    "v_exp_f32 v107, v31\n\t"
    "v_mfma_f32_32x32x16_bf16 v[16:31], v[80:83], v[68:71], v[48:63]\n\t"
    "ds_read_b128 v[80:83], v108 offset:4096\n\t"
    "v_cvt_pk_bf16_f32 v103, v106, v107\n\t"
    "v_exp_f32 v104, v32\n\t"
    "v_exp_f32 v105, v33\n\t"
    "v_exp_f32 v106, v34\n\t"
    "v_cvt_pk_bf16_f32 v96, v104, v105\n\t"
    "v_mfma_f32_32x32x16_bf16 v[16:31], v[84:87], v[72:75], v[16:31]\n\t"
    "ds_read_b128 v[84:87], v108 offset:5120\n\t"
    "v_exp_f32 v107, v35\n\t"
    "v_exp_f32 v104, v36\n\t"
    "v_cvt_pk_bf16_f32 v97, v106, v107\n\t"
    "v_exp_f32 v105, v37\n\t"
    "v_mfma_f32_32x32x16_bf16 v[0:15], v[92:95], v[100:103], v[0:15]\n\t"
    "ds_read_b128 v[92:95], v108 offset:7168\n\t"
    "v_exp_f32 v106, v38\n\t"
    "v_cvt_pk_bf16_f32 v98, v104, v105\n\t"
    "v_exp_f32 v107, v39\n\t"
    "v_exp_f32 v104, v40\n\t"
    "v_mfma_f32_16x16x32_bf16 v[64:67], v[76:79], v[100:103], v[64:67]\n\t"
    "s_waitcnt lgkmcnt(3)\n\t"
    "s_add_u32 s37, s36, 204800\n\t"
    "s_and_b32 s37, s37, 0x3ffff\n\t"
    "s_add_u32 s38, s34, s37\n\t"
    "s_addc_u32 s39, s35, 0\n\t"
    "s_add_u32 m0, s44, 98304\n\t"
    "v_cvt_pk_bf16_f32 v99, v106, v107\n\t"
    "v_exp_f32 v105, v41\n\t"
    "v_exp_f32 v106, v42\n\t"
    "v_mfma_f32_32x32x16_bf16 v[0:15], v[88:91], v[96:99], v[0:15]\n\t"
    "ds_read_b128 v[88:91], v108 offset:6144\n\t"
    "v_cvt_pk_bf16_f32 v100, v104, v105\n\t"
    "v_exp_f32 v107, v43\n\t"
    "v_exp_f32 v104, v44\n\t"
    "v_cvt_pk_bf16_f32 v101, v106, v107\n\t"
    "v_exp_f32 v105, v45\n\t"
    "v_mfma_f32_16x16x32_bf16 v[64:67], v[76:79], v[96:99], v[64:67]\n\t"
    "s_waitcnt lgkmcnt(1)\n\t"
    "v_mfma_f32_16x16x32_bf16 v[152:155], v[144:147], v[160:163], 0\n\t"
    "buffer_store_dwordx4 v[144:147], v110, s[40:43], 0 offen offset:0 sc1\n\t"
    "v_exp_f32 v106, v46\n\t"
    "v_cvt_pk_bf16_f32 v102, v104, v105\n\t"
    "v_exp_f32 v107, v47\n\t"
    "s_setprio 2\n\t"
    "v_mfma_f32_32x32x16_bf16 v[32:47], v[80:83], v[68:71], v[48:63]\n\t"
    "ds_read_b128 v[80:83], v108 offset:8192\n\t"
    "v_cvt_pk_bf16_f32 v103, v106, v107\n\t"
    "v_exp_f32 v104, v16\n\t"
    "v_exp_f32 v105, v17\n\t"
    "v_exp_f32 v106, v18\n\t"
    "v_cvt_pk_bf16_f32 v96, v104, v105\n\t"
    "v_mfma_f32_32x32x16_bf16 v[32:47], v[84:87], v[72:75], v[32:47]\n\t"
    "ds_read_b128 v[84:87], v108 offset:9216\n\t"
    "v_exp_f32 v107, v19\n\t"
    "v_exp_f32 v104, v20\n\t"
    "v_cvt_pk_bf16_f32 v97, v106, v107\n\t"
    "v_exp_f32 v105, v21\n\t"
    "v_mfma_f32_32x32x16_bf16 v[0:15], v[92:95], v[100:103], v[0:15]\n\t"
    "ds_read_b128 v[92:95], v108 offset:11264\n\t"
    "v_exp_f32 v106, v22\n\t"
    "v_cvt_pk_bf16_f32 v98, v104, v105\n\t"
    "v_exp_f32 v107, v23\n\t"
    "v_exp_f32 v104, v24\n\t"
    "v_mfma_f32_16x16x32_bf16 v[64:67], v[76:79], v[100:103], v[64:67]\n\t"
    "s_waitcnt lgkmcnt(3)\n\t"
    "global_load_lds_dwordx4 v109, s[38:39] offset:0 sc1\n\t"
    "v_cvt_pk_bf16_f32 v99, v106, v107\n\t"
    "v_exp_f32 v105, v25\n\t"
    "v_exp_f32 v106, v26\n\t"
    "v_mfma_f32_32x32x16_bf16 v[0:15], v[88:91], v[96:99], v[0:15]\n\t"
    "ds_read_b128 v[88:91], v108 offset:10240\n\t"
    "v_cvt_pk_bf16_f32 v100, v104, v105\n\t"
    "v_exp_f32 v107, v27\n\t"
    "v_exp_f32 v104, v28\n\t"
    "v_cvt_pk_bf16_f32 v101, v106, v107\n\t"
    "v_exp_f32 v105, v29\n\t"
    "v_mfma_f32_16x16x32_bf16 v[64:67], v[76:79], v[96:99], v[64:67]\n\t"
    "s_waitcnt lgkmcnt(1)\n\t"
    "ds_write_b128 v176, v[152:155] offset:512\n\t"
    "v_perm_b32 v148, v118, v114, s59\n\t"
    "v_perm_b32 v149, v126, v122, s59\n\t"
    "v_perm_b32 v150, v134, v130, s59\n\t"
    "v_perm_b32 v151, v142, v138, s59\n\t"
    "v_exp_f32 v106, v30\n\t"
    "v_cvt_pk_bf16_f32 v102, v104, v105\n\t"
    "v_exp_f32 v107, v31\n\t"
    "v_mfma_f32_32x32x16_bf16 v[16:31], v[80:83], v[68:71], v[48:63]\n\t"
    "ds_read_b128 v[80:83], v108 offset:12288\n\t"
    "v_cvt_pk_bf16_f32 v103, v106, v107\n\t"
    "v_exp_f32 v104, v32\n\t"
    "v_exp_f32 v105, v33\n\t"
    "v_exp_f32 v106, v34\n\t"
    "v_cvt_pk_bf16_f32 v96, v104, v105\n\t"
    "v_mfma_f32_32x32x16_bf16 v[16:31], v[84:87], v[72:75], v[16:31]\n\t"
    "ds_read_b128 v[84:87], v108 offset:13312\n\t"
    "v_exp_f32 v107, v35\n\t"
    "v_exp_f32 v104, v36\n\t"
    "v_cvt_pk_bf16_f32 v97, v106, v107\n\t"
    "v_exp_f32 v105, v37\n\t"
    "v_mfma_f32_32x32x16_bf16 v[0:15], v[92:95], v[100:103], v[0:15]\n\t"
    "ds_read_b128 v[92:95], v108 offset:15360\n\t"
    "v_exp_f32 v106, v38\n\t"
    "v_cvt_pk_bf16_f32 v98, v104, v105\n\t"
    "v_exp_f32 v107, v39\n\t"
    "v_exp_f32 v104, v40\n\t"
    "v_mfma_f32_16x16x32_bf16 v[64:67], v[76:79], v[100:103], v[64:67]\n\t"
    "s_waitcnt lgkmcnt(3)\n\t"
    "global_load_lds_dwordx4 v109, s[38:39] offset:1024 sc1\n\t"
    "v_cvt_pk_bf16_f32 v99, v106, v107\n\t"
    "v_exp_f32 v105, v41\n\t"
    "v_exp_f32 v106, v42\n\t"
    "v_mfma_f32_32x32x16_bf16 v[0:15], v[88:91], v[96:99], v[0:15]\n\t"
    "ds_read_b128 v[88:91], v108 offset:14336\n\t"
    "v_cvt_pk_bf16_f32 v100, v104, v105\n\t"
    "v_exp_f32 v107, v43\n\t"
    "v_exp_f32 v104, v44\n\t"
    "v_cvt_pk_bf16_f32 v101, v106, v107\n\t"
    "v_exp_f32 v105, v45\n\t"
    "v_mfma_f32_16x16x32_bf16 v[64:67], v[76:79], v[96:99], v[64:67]\n\t"
    "s_waitcnt lgkmcnt(1)\n\t"
    "v_mfma_f32_16x16x32_bf16 v[156:159], v[148:151], v[160:163], 0\n\t"
    "buffer_store_dwordx4 v[148:151], v110, s[40:43], 0 offen offset:1024 sc1\n\t"
    "v_exp_f32 v106, v46\n\t"
    "v_cvt_pk_bf16_f32 v102, v104, v105\n\t"
    "v_exp_f32 v107, v47\n\t"
    "s_setprio 1\n\t"
    "v_mfma_f32_32x32x16_bf16 v[32:47], v[80:83], v[68:71], v[48:63]\n\t"
    "ds_read_b128 v[80:83], v108 offset:16384\n\t"
    "v_cvt_pk_bf16_f32 v103, v106, v107\n\t"
    "v_exp_f32 v104, v16\n\t"
    "v_exp_f32 v105, v17\n\t"
    "v_exp_f32 v106, v18\n\t"
    "v_cvt_pk_bf16_f32 v96, v104, v105\n\t"
    "v_mfma_f32_32x32x16_bf16 v[32:47], v[84:87], v[72:75], v[32:47]\n\t"
    "ds_read_b128 v[84:87], v108 offset:17408\n\t"
    "v_exp_f32 v107, v19\n\t"
    "v_exp_f32 v104, v20\n\t"
    "v_cvt_pk_bf16_f32 v97, v106, v107\n\t"
    "v_exp_f32 v105, v21\n\t"
    "v_mfma_f32_32x32x16_bf16 v[0:15], v[92:95], v[100:103], v[0:15]\n\t"
    "ds_read_b128 v[92:95], v108 offset:19456\n\t"
    "v_exp_f32 v106, v22\n\t"
    "v_cvt_pk_bf16_f32 v98, v104, v105\n\t"
    "v_exp_f32 v107, v23\n\t"
    "v_exp_f32 v104, v24\n\t"
    "v_mfma_f32_16x16x32_bf16 v[64:67], v[76:79], v[100:103], v[64:67]\n\t"
    "s_waitcnt lgkmcnt(3)\n\t"
    "global_load_lds_dwordx4 v109, s[38:39] offset:2048 sc1\n\t"
    "v_cvt_pk_bf16_f32 v99, v106, v107\n\t"
    "v_exp_f32 v105, v25\n\t"
    "v_exp_f32 v106, v26\n\t"
    "v_mfma_f32_32x32x16_bf16 v[0:15], v[88:91], v[96:99], v[0:15]\n\t"
    "ds_read_b128 v[88:91], v108 offset:18432\n\t"
    "v_cvt_pk_bf16_f32 v100, v104, v105\n\t"
    "v_exp_f32 v107, v27\n\t"
    "v_exp_f32 v104, v28\n\t"
    "v_cvt_pk_bf16_f32 v101, v106, v107\n\t"
    "v_exp_f32 v105, v29\n\t"
    "v_mfma_f32_16x16x32_bf16 v[64:67], v[76:79], v[96:99], v[64:67]\n\t"
    "s_waitcnt lgkmcnt(1)\n\t"
    "v_exp_f32 v106, v30\n\t"
    "v_cvt_pk_bf16_f32 v102, v104, v105\n\t"
    "v_exp_f32 v107, v31\n\t"
    "v_mfma_f32_32x32x16_bf16 v[16:31], v[80:83], v[68:71], v[48:63]\n\t"
    "ds_read_b128 v[80:83], v108 offset:20480\n\t"
    "v_cvt_pk_bf16_f32 v103, v106, v107\n\t"
    "v_exp_f32 v104, v32\n\t"
    "v_exp_f32 v105, v33\n\t"
    "v_exp_f32 v106, v34\n\t"
    "v_cvt_pk_bf16_f32 v96, v104, v105\n\t"
    "v_mfma_f32_32x32x16_bf16 v[16:31], v[84:87], v[72:75], v[16:31]\n\t"
    "ds_read_b128 v[84:87], v108 offset:21504\n\t"
    "v_exp_f32 v107, v35\n\t"
    "v_exp_f32 v104, v36\n\t"
    "v_cvt_pk_bf16_f32 v97, v106, v107\n\t"
    "v_exp_f32 v105, v37\n\t"
    "v_mfma_f32_32x32x16_bf16 v[0:15], v[92:95], v[100:103], v[0:15]\n\t"
    "ds_read_b128 v[92:95], v108 offset:23552\n\t"
    "v_exp_f32 v106, v38\n\t"
    "v_cvt_pk_bf16_f32 v98, v104, v105\n\t"
    "v_exp_f32 v107, v39\n\t"
    "v_exp_f32 v104, v40\n\t"
    "v_mfma_f32_16x16x32_bf16 v[64:67], v[76:79], v[100:103], v[64:67]\n\t"
    "s_waitcnt lgkmcnt(3)\n\t"
    "global_load_lds_dwordx4 v109, s[38:39] offset:3072 sc1\n\t"
    "v_cvt_pk_bf16_f32 v99, v106, v107\n\t"
    "v_exp_f32 v105, v41\n\t"
    "v_exp_f32 v106, v42\n\t"
    "v_mfma_f32_32x32x16_bf16 v[0:15], v[88:91], v[96:99], v[0:15]\n\t"
    "ds_read_b128 v[88:91], v108 offset:22528\n\t"
    "v_cvt_pk_bf16_f32 v100, v104, v105\n\t"
    "v_exp_f32 v107, v43\n\t"
    "v_exp_f32 v104, v44\n\t"
    "v_cvt_pk_bf16_f32 v101, v106, v107\n\t"
    "v_exp_f32 v105, v45\n\t"
    "v_mfma_f32_16x16x32_bf16 v[64:67], v[76:79], v[96:99], v[64:67]\n\t"
    "s_waitcnt lgkmcnt(1)\n\t"
    "ds_write_b128 v176, v[156:159] offset:640\n\t"
    "v_exp_f32 v106, v46\n\t"
    "v_cvt_pk_bf16_f32 v102, v104, v105\n\t"
    "v_exp_f32 v107, v47\n\t"
    "s_setprio 0\n\t"
    "v_mfma_f32_32x32x16_bf16 v[32:47], v[80:83], v[68:71], v[48:63]\n\t"
    "ds_read_b128 v[80:83], v108 offset:24576\n\t"
    "v_cvt_pk_bf16_f32 v103, v106, v107\n\t"
    "v_exp_f32 v104, v16\n\t"
    "v_exp_f32 v105, v17\n\t"
    "v_exp_f32 v106, v18\n\t"
    "v_cvt_pk_bf16_f32 v96, v104, v105\n\t"
    "v_mfma_f32_32x32x16_bf16 v[32:47], v[84:87], v[72:75], v[32:47]\n\t"
    "ds_read_b128 v[84:87], v108 offset:25600\n\t"
    "v_exp_f32 v107, v19\n\t"
    "v_exp_f32 v104, v20\n\t"
    "v_cvt_pk_bf16_f32 v97, v106, v107\n\t"
    "v_exp_f32 v105, v21\n\t"
    "v_mfma_f32_32x32x16_bf16 v[0:15], v[92:95], v[100:103], v[0:15]\n\t"
    "ds_read_b128 v[92:95], v108 offset:27648\n\t"
    "v_exp_f32 v106, v22\n\t"
    "v_cvt_pk_bf16_f32 v98, v104, v105\n\t"
    "v_exp_f32 v107, v23\n\t"
    "v_exp_f32 v104, v24\n\t"
    "v_mfma_f32_16x16x32_bf16 v[64:67], v[76:79], v[100:103], v[64:67]\n\t"
    "s_waitcnt lgkmcnt(3)\n\t"
    "v_cvt_pk_bf16_f32 v99, v106, v107\n\t"
    "v_exp_f32 v105, v25\n\t"
    "v_exp_f32 v106, v26\n\t"
    "v_mfma_f32_32x32x16_bf16 v[0:15], v[88:91], v[96:99], v[0:15]\n\t"
    "ds_read_b128 v[88:91], v108 offset:26624\n\t"
    "v_cvt_pk_bf16_f32 v100, v104, v105\n\t"
    "v_exp_f32 v107, v27\n\t"
    "v_exp_f32 v104, v28\n\t"
    "v_cvt_pk_bf16_f32 v101, v106, v107\n\t"
    "v_exp_f32 v105, v29\n\t"
    "v_mfma_f32_16x16x32_bf16 v[64:67], v[76:79], v[96:99], v[64:67]\n\t"
    "s_waitcnt lgkmcnt(1)\n\t"
    "global_load_dword v111, v[172:173], off\n\t"
    "v_exp_f32 v106, v30\n\t"
    "v_cvt_pk_bf16_f32 v102, v104, v105\n\t"
    "v_exp_f32 v107, v31\n\t"
    "v_mfma_f32_32x32x16_bf16 v[16:31], v[80:83], v[68:71], v[48:63]\n\t"
    "ds_read_b128 v[80:83], v108 offset:28672\n\t"
    "v_cvt_pk_bf16_f32 v103, v106, v107\n\t"
    "v_exp_f32 v104, v32\n\t"
    "v_exp_f32 v105, v33\n\t"
    "v_exp_f32 v106, v34\n\t"
    "v_cvt_pk_bf16_f32 v96, v104, v105\n\t"
    "v_mfma_f32_32x32x16_bf16 v[16:31], v[84:87], v[72:75], v[16:31]\n\t"
    "ds_read_b128 v[84:87], v108 offset:29696\n\t"
    "v_exp_f32 v107, v35\n\t"
    "v_exp_f32 v104, v36\n\t"
    "v_cvt_pk_bf16_f32 v97, v106, v107\n\t"
    "v_exp_f32 v105, v37\n\t"
    "v_mfma_f32_32x32x16_bf16 v[0:15], v[92:95], v[100:103], v[0:15]\n\t"
    "ds_read_b128 v[92:95], v108 offset:31744\n\t"
    "v_exp_f32 v106, v38\n\t"
    "v_cvt_pk_bf16_f32 v98, v104, v105\n\t"
    "v_exp_f32 v107, v39\n\t"
    "v_exp_f32 v104, v40\n\t"
    "v_mfma_f32_16x16x32_bf16 v[64:67], v[76:79], v[100:103], v[64:67]\n\t"
    "s_waitcnt lgkmcnt(3)\n\t"
    "v_cvt_pk_bf16_f32 v99, v106, v107\n\t"
    "v_exp_f32 v105, v41\n\t"
    "v_exp_f32 v106, v42\n\t"
    "v_mfma_f32_32x32x16_bf16 v[0:15], v[88:91], v[96:99], v[0:15]\n\t"
    "ds_read_b128 v[88:91], v108 offset:30720\n\t"
    "v_cvt_pk_bf16_f32 v100, v104, v105\n\t"
    "v_exp_f32 v107, v43\n\t"
    "v_exp_f32 v104, v44\n\t"
    "v_cvt_pk_bf16_f32 v101, v106, v107\n\t"
    "v_exp_f32 v105, v45\n\t"
    "v_mfma_f32_16x16x32_bf16 v[64:67], v[76:79], v[96:99], v[64:67]\n\t"
    "v_lshl_add_u64 v[172:173], v[172:173], 0, v[174:175]\n\t"
    "v_exp_f32 v106, v46\n\t"
    "v_cvt_pk_bf16_f32 v102, v104, v105\n\t"
    "v_exp_f32 v107, v47\n\t"
    "s_waitcnt vmcnt(7) lgkmcnt(0)\n\t"
    "s_barrier\n\t"
    "v_add_u32 v108, 32768, v180\n\t"
    "s_setprio 3\n\t"
    "v_mfma_f32_32x32x16_bf16 v[32:47], v[80:83], v[68:71], v[48:63]\n\t"
    "ds_read_b128 v[80:83], v108 offset:0\n\t"
    "v_cvt_pk_bf16_f32 v103, v106, v107\n\t"
    "v_exp_f32 v104, v16\n\t"
    "v_exp_f32 v105, v17\n\t"
    "v_exp_f32 v106, v18\n\t"
    "v_cvt_pk_bf16_f32 v96, v104, v105\n\t"
    "v_mfma_f32_32x32x16_bf16 v[32:47], v[84:87], v[72:75], v[32:47]\n\t"
    "ds_read_b128 v[84:87], v108 offset:1024\n\t"
    "v_exp_f32 v107, v19\n\t"
    "v_exp_f32 v104, v20\n\t"
    "v_cvt_pk_bf16_f32 v97, v106, v107\n\t"
    "v_exp_f32 v105, v21\n\t"
    "v_mfma_f32_32x32x16_bf16 v[0:15], v[92:95], v[100:103], v[0:15]\n\t"
    "ds_read_b128 v[92:95], v108 offset:3072\n\t"
    "v_exp_f32 v106, v22\n\t"
    "v_cvt_pk_bf16_f32 v98, v104, v105\n\t"
    "v_exp_f32 v107, v23\n\t"
    "v_exp_f32 v104, v24\n\t"
    "v_mfma_f32_16x16x32_bf16 v[64:67], v[76:79], v[100:103], v[64:67]\n\t"
    "s_waitcnt lgkmcnt(3)\n\t"
    "s_cmp_lg_u32 s45, 0\n\t"
    "s_cbranch_scc1 .Lattn_na\n\t"
    "s_mov_b64 exec, 1\n\t"
    "global_atomic_add v109, v179, s[60:61]\n\t"
    "s_mov_b64 exec, -1\n\t"
    ".Lattn_na:\n\t"
    "v_cvt_pk_bf16_f32 v99, v106, v107\n\t"
    "v_exp_f32 v105, v25\n\t"
    "v_exp_f32 v106, v26\n\t"
    "v_mfma_f32_32x32x16_bf16 v[0:15], v[88:91], v[96:99], v[0:15]\n\t"
    "ds_read_b128 v[88:91], v108 offset:2048\n\t"
    "v_cvt_pk_bf16_f32 v100, v104, v105\n\t"
    "v_exp_f32 v107, v27\n\t"
    "v_exp_f32 v104, v28\n\t"
    "v_cvt_pk_bf16_f32 v101, v106, v107\n\t"
    "v_exp_f32 v105, v29\n\t"
    "v_mfma_f32_16x16x32_bf16 v[64:67], v[76:79], v[96:99], v[64:67]\n\t"
    "s_waitcnt lgkmcnt(1)\n\t"
    "v_perm_b32 v144, v119, v115, s58\n\t"
    "v_perm_b32 v145, v127, v123, s58\n\t"
    "v_perm_b32 v146, v135, v131, s58\n\t"
    "v_perm_b32 v147, v143, v139, s58\n\t"
    "v_exp_f32 v106, v30\n\t"
    "v_cvt_pk_bf16_f32 v102, v104, v105\n\t"
    "v_exp_f32 v107, v31\n\t"
    "v_mfma_f32_32x32x16_bf16 v[16:31], v[80:83], v[68:71], v[48:63]\n\t"
    "ds_read_b128 v[80:83], v108 offset:4096\n\t"
    "v_cvt_pk_bf16_f32 v103, v106, v107\n\t"
    "v_exp_f32 v104, v32\n\t"
    "v_exp_f32 v105, v33\n\t"
    "v_exp_f32 v106, v34\n\t"
    "v_cvt_pk_bf16_f32 v96, v104, v105\n\t"
    "v_mfma_f32_32x32x16_bf16 v[16:31], v[84:87], v[72:75], v[16:31]\n\t"
    "ds_read_b128 v[84:87], v108 offset:5120\n\t"
    "v_exp_f32 v107, v35\n\t"
    "v_exp_f32 v104, v36\n\t"
    "v_cvt_pk_bf16_f32 v97, v106, v107\n\t"
    "v_exp_f32 v105, v37\n\t"
    "v_mfma_f32_32x32x16_bf16 v[0:15], v[92:95], v[100:103], v[0:15]\n\t"
    "ds_read_b128 v[92:95], v108 offset:7168\n\t"
    "v_exp_f32 v106, v38\n\t"
    "v_cvt_pk_bf16_f32 v98, v104, v105\n\t"
    "v_exp_f32 v107, v39\n\t"
    "v_exp_f32 v104, v40\n\t"
    "v_mfma_f32_16x16x32_bf16 v[64:67], v[76:79], v[100:103], v[64:67]\n\t"
    "s_waitcnt lgkmcnt(3)\n\t"
    "s_add_u32 s37, s36, 237568\n\t"
    "s_and_b32 s37, s37, 0x3ffff\n\t"
    "s_add_u32 s38, s34, s37\n\t"
    "s_addc_u32 s39, s35, 0\n\t"
    "s_add_u32 m0, s44, 131072\n\t"
    "v_cvt_pk_bf16_f32 v99, v106, v107\n\t"
    "v_exp_f32 v105, v41\n\t"
    "v_exp_f32 v106, v42\n\t"
    "v_mfma_f32_32x32x16_bf16 v[0:15], v[88:91], v[96:99], v[0:15]\n\t"
    "ds_read_b128 v[88:91], v108 offset:6144\n\t"
    "v_cvt_pk_bf16_f32 v100, v104, v105\n\t"
    "v_exp_f32 v107, v43\n\t"
    "v_exp_f32 v104, v44\n\t"
    "v_cvt_pk_bf16_f32 v101, v106, v107\n\t"
    "v_exp_f32 v105, v45\n\t"
    "v_mfma_f32_16x16x32_bf16 v[64:67], v[76:79], v[96:99], v[64:67]\n\t"
    "s_waitcnt lgkmcnt(1)\n\t"
    "v_mfma_f32_16x16x32_bf16 v[152:155], v[144:147], v[160:163], 0\n\t"
    "buffer_store_dwordx4 v[144:147], v110, s[40:43], 0 offen offset:2048 sc1\n\t"
    "v_exp_f32 v106, v46\n\t"
    "v_cvt_pk_bf16_f32 v102, v104, v105\n\t"
    "v_exp_f32 v107, v47\n\t"
    "s_setprio 2\n\t"
    "v_mfma_f32_32x32x16_bf16 v[32:47], v[80:83], v[68:71], v[48:63]\n\t"
    "ds_read_b128 v[80:83], v108 offset:8192\n\t"
    "v_cvt_pk_bf16_f32 v103, v106, v107\n\t"
    "v_exp_f32 v104, v16\n\t"
    "v_exp_f32 v105, v17\n\t"
    "v_exp_f32 v106, v18\n\t"
    "v_cvt_pk_bf16_f32 v96, v104, v105\n\t"
    "v_mfma_f32_32x32x16_bf16 v[32:47], v[84:87], v[72:75], v[32:47]\n\t"
    "ds_read_b128 v[84:87], v108 offset:9216\n\t"
    "v_exp_f32 v107, v19\n\t"
    "v_exp_f32 v104, v20\n\t"
    "v_cvt_pk_bf16_f32 v97, v106, v107\n\t"
    "v_exp_f32 v105, v21\n\t"
    "v_mfma_f32_32x32x16_bf16 v[0:15], v[92:95], v[100:103], v[0:15]\n\t"
    "ds_read_b128 v[92:95], v108 offset:11264\n\t"
    "v_exp_f32 v106, v22\n\t"
    "v_cvt_pk_bf16_f32 v98, v104, v105\n\t"
    "v_exp_f32 v107, v23\n\t"
    "v_exp_f32 v104, v24\n\t"
    "v_mfma_f32_16x16x32_bf16 v[64:67], v[76:79], v[100:103], v[64:67]\n\t"
    "s_waitcnt lgkmcnt(3)\n\t"
    "global_load_lds_dwordx4 v109, s[38:39] offset:0 sc1\n\t"
    "v_cvt_pk_bf16_f32 v99, v106, v107\n\t"
    "v_exp_f32 v105, v25\n\t"
    "v_exp_f32 v106, v26\n\t"
    "v_mfma_f32_32x32x16_bf16 v[0:15], v[88:91], v[96:99], v[0:15]\n\t"
    "ds_read_b128 v[88:91], v108 offset:10240\n\t"
    "v_cvt_pk_bf16_f32 v100, v104, v105\n\t"
    "v_exp_f32 v107, v27\n\t"
    "v_exp_f32 v104, v28\n\t"
    "v_cvt_pk_bf16_f32 v101, v106, v107\n\t"
    "v_exp_f32 v105, v29\n\t"
    "v_mfma_f32_16x16x32_bf16 v[64:67], v[76:79], v[96:99], v[64:67]\n\t"
    "s_waitcnt lgkmcnt(1)\n\t"
    "ds_write_b128 v176, v[152:155] offset:768\n\t"
    "v_perm_b32 v148, v119, v115, s59\n\t"
    "v_perm_b32 v149, v127, v123, s59\n\t"
    "v_perm_b32 v150, v135, v131, s59\n\t"
    "v_perm_b32 v151, v143, v139, s59\n\t"
    "v_exp_f32 v106, v30\n\t"
    "v_cvt_pk_bf16_f32 v102, v104, v105\n\t"
    "v_exp_f32 v107, v31\n\t"
    "v_mfma_f32_32x32x16_bf16 v[16:31], v[80:83], v[68:71], v[48:63]\n\t"
    "ds_read_b128 v[80:83], v108 offset:12288\n\t"
    "v_cvt_pk_bf16_f32 v103, v106, v107\n\t"
    "v_exp_f32 v104, v32\n\t"
    "v_exp_f32 v105, v33\n\t"
    "v_exp_f32 v106, v34\n\t"
    "v_cvt_pk_bf16_f32 v96, v104, v105\n\t"
    "v_mfma_f32_32x32x16_bf16 v[16:31], v[84:87], v[72:75], v[16:31]\n\t"
    "ds_read_b128 v[84:87], v108 offset:13312\n\t"
    "v_exp_f32 v107, v35\n\t"
    "v_exp_f32 v104, v36\n\t"
    "v_cvt_pk_bf16_f32 v97, v106, v107\n\t"
    "v_exp_f32 v105, v37\n\t"
    "v_mfma_f32_32x32x16_bf16 v[0:15], v[92:95], v[100:103], v[0:15]\n\t"
    "ds_read_b128 v[92:95], v108 offset:15360\n\t"
    "v_exp_f32 v106, v38\n\t"
    "v_cvt_pk_bf16_f32 v98, v104, v105\n\t"
    "v_exp_f32 v107, v39\n\t"
    "v_exp_f32 v104, v40\n\t"
    "v_mfma_f32_16x16x32_bf16 v[64:67], v[76:79], v[100:103], v[64:67]\n\t"
    "s_waitcnt lgkmcnt(3)\n\t"
    "global_load_lds_dwordx4 v109, s[38:39] offset:1024 sc1\n\t"
    "v_cvt_pk_bf16_f32 v99, v106, v107\n\t"
    "v_exp_f32 v105, v41\n\t"
    "v_exp_f32 v106, v42\n\t"
    "v_mfma_f32_32x32x16_bf16 v[0:15], v[88:91], v[96:99], v[0:15]\n\t"
    "ds_read_b128 v[88:91], v108 offset:14336\n\t"
    "v_cvt_pk_bf16_f32 v100, v104, v105\n\t"
    "v_exp_f32 v107, v43\n\t"
    "v_exp_f32 v104, v44\n\t"
    "v_cvt_pk_bf16_f32 v101, v106, v107\n\t"
    "v_exp_f32 v105, v45\n\t"
    "v_mfma_f32_16x16x32_bf16 v[64:67], v[76:79], v[96:99], v[64:67]\n\t"
    "s_waitcnt lgkmcnt(1)\n\t"
    "v_mfma_f32_16x16x32_bf16 v[156:159], v[148:151], v[160:163], 0\n\t"
    "buffer_store_dwordx4 v[148:151], v110, s[40:43], 0 offen offset:3072 sc1\n\t"
    "v_exp_f32 v106, v46\n\t"
    "v_cvt_pk_bf16_f32 v102, v104, v105\n\t"
    "v_exp_f32 v107, v47\n\t"
    "s_setprio 1\n\t"
    "v_mfma_f32_32x32x16_bf16 v[32:47], v[80:83], v[68:71], v[48:63]\n\t"
    "ds_read_b128 v[80:83], v108 offset:16384\n\t"
    "v_cvt_pk_bf16_f32 v103, v106, v107\n\t"
    "v_exp_f32 v104, v16\n\t"
    "v_exp_f32 v105, v17\n\t"
    "v_exp_f32 v106, v18\n\t"
    "v_cvt_pk_bf16_f32 v96, v104, v105\n\t"
    "v_mfma_f32_32x32x16_bf16 v[32:47], v[84:87], v[72:75], v[32:47]\n\t"
    "ds_read_b128 v[84:87], v108 offset:17408\n\t"
    "v_exp_f32 v107, v19\n\t"
    "v_exp_f32 v104, v20\n\t"
    "v_cvt_pk_bf16_f32 v97, v106, v107\n\t"
    "v_exp_f32 v105, v21\n\t"
    "v_mfma_f32_32x32x16_bf16 v[0:15], v[92:95], v[100:103], v[0:15]\n\t"
    "ds_read_b128 v[92:95], v108 offset:19456\n\t"
    "v_exp_f32 v106, v22\n\t"
    "v_cvt_pk_bf16_f32 v98, v104, v105\n\t"
    "v_exp_f32 v107, v23\n\t"
    "v_exp_f32 v104, v24\n\t"
    "v_mfma_f32_16x16x32_bf16 v[64:67], v[76:79], v[100:103], v[64:67]\n\t"
    "s_waitcnt lgkmcnt(3)\n\t"
    "global_load_lds_dwordx4 v109, s[38:39] offset:2048 sc1\n\t"
    "v_cvt_pk_bf16_f32 v99, v106, v107\n\t"
    "v_exp_f32 v105, v25\n\t"
    "v_exp_f32 v106, v26\n\t"
    "v_mfma_f32_32x32x16_bf16 v[0:15], v[88:91], v[96:99], v[0:15]\n\t"
    "ds_read_b128 v[88:91], v108 offset:18432\n\t"
    "v_cvt_pk_bf16_f32 v100, v104, v105\n\t"
    "v_exp_f32 v107, v27\n\t"
    "v_exp_f32 v104, v28\n\t"
    "v_cvt_pk_bf16_f32 v101, v106, v107\n\t"
    "v_exp_f32 v105, v29\n\t"
    "v_mfma_f32_16x16x32_bf16 v[64:67], v[76:79], v[96:99], v[64:67]\n\t"
    "s_waitcnt lgkmcnt(1)\n\t"
    "v_exp_f32 v106, v30\n\t"
    "v_cvt_pk_bf16_f32 v102, v104, v105\n\t"
    "v_exp_f32 v107, v31\n\t"
    "v_mfma_f32_32x32x16_bf16 v[16:31], v[80:83], v[68:71], v[48:63]\n\t"
    "ds_read_b128 v[80:83], v108 offset:20480\n\t"
    "v_cvt_pk_bf16_f32 v103, v106, v107\n\t"
    "v_exp_f32 v104, v32\n\t"
    "v_exp_f32 v105, v33\n\t"
    "v_exp_f32 v106, v34\n\t"
    "v_cvt_pk_bf16_f32 v96, v104, v105\n\t"
    "v_mfma_f32_32x32x16_bf16 v[16:31], v[84:87], v[72:75], v[16:31]\n\t"
    "ds_read_b128 v[84:87], v108 offset:21504\n\t"
    "v_exp_f32 v107, v35\n\t"
    "v_exp_f32 v104, v36\n\t"
    "v_cvt_pk_bf16_f32 v97, v106, v107\n\t"
    "v_exp_f32 v105, v37\n\t"
    "v_mfma_f32_32x32x16_bf16 v[0:15], v[92:95], v[100:103], v[0:15]\n\t"
    "ds_read_b128 v[92:95], v108 offset:23552\n\t"
    "v_exp_f32 v106, v38\n\t"
    "v_cvt_pk_bf16_f32 v98, v104, v105\n\t"
    "v_exp_f32 v107, v39\n\t"
    "v_exp_f32 v104, v40\n\t"
    "v_mfma_f32_16x16x32_bf16 v[64:67], v[76:79], v[100:103], v[64:67]\n\t"
    "s_waitcnt lgkmcnt(3)\n\t"
    "global_load_lds_dwordx4 v109, s[38:39] offset:3072 sc1\n\t"
    "v_cvt_pk_bf16_f32 v99, v106, v107\n\t"
    "v_exp_f32 v105, v41\n\t"
    "v_exp_f32 v106, v42\n\t"
    "v_mfma_f32_32x32x16_bf16 v[0:15], v[88:91], v[96:99], v[0:15]\n\t"
    "ds_read_b128 v[88:91], v108 offset:22528\n\t"
    "v_cvt_pk_bf16_f32 v100, v104, v105\n\t"
    "v_exp_f32 v107, v43\n\t"
    "v_exp_f32 v104, v44\n\t"
    "v_cvt_pk_bf16_f32 v101, v106, v107\n\t"
    "v_exp_f32 v105, v45\n\t"
    "v_mfma_f32_16x16x32_bf16 v[64:67], v[76:79], v[96:99], v[64:67]\n\t"
    "s_waitcnt lgkmcnt(1)\n\t"
    "ds_write_b128 v176, v[156:159] offset:896\n\t"
    "v_exp_f32 v106, v46\n\t"
    "v_cvt_pk_bf16_f32 v102, v104, v105\n\t"
    "v_exp_f32 v107, v47\n\t"
    "s_setprio 0\n\t"
    "v_mfma_f32_32x32x16_bf16 v[32:47], v[80:83], v[68:71], v[48:63]\n\t"
    "ds_read_b128 v[80:83], v108 offset:24576\n\t"
    "v_cvt_pk_bf16_f32 v103, v106, v107\n\t"
    "v_exp_f32 v104, v16\n\t"
    "v_exp_f32 v105, v17\n\t"
    "v_exp_f32 v106, v18\n\t"
    "v_cvt_pk_bf16_f32 v96, v104, v105\n\t"
    "v_mfma_f32_32x32x16_bf16 v[32:47], v[84:87], v[72:75], v[32:47]\n\t"
    "ds_read_b128 v[84:87], v108 offset:25600\n\t"
    "v_exp_f32 v107, v19\n\t"
    "v_exp_f32 v104, v20\n\t"
    "v_cvt_pk_bf16_f32 v97, v106, v107\n\t"
    "v_exp_f32 v105, v21\n\t"
    "v_mfma_f32_32x32x16_bf16 v[0:15], v[92:95], v[100:103], v[0:15]\n\t"
    "ds_read_b128 v[92:95], v108 offset:27648\n\t"
    "v_exp_f32 v106, v22\n\t"
    "v_cvt_pk_bf16_f32 v98, v104, v105\n\t"
    "v_exp_f32 v107, v23\n\t"
    "v_exp_f32 v104, v24\n\t"
    "v_mfma_f32_16x16x32_bf16 v[64:67], v[76:79], v[100:103], v[64:67]\n\t"
    "s_waitcnt lgkmcnt(3)\n\t"
    "v_cvt_pk_bf16_f32 v99, v106, v107\n\t"
    "v_exp_f32 v105, v25\n\t"
    "v_exp_f32 v106, v26\n\t"
    "v_mfma_f32_32x32x16_bf16 v[0:15], v[88:91], v[96:99], v[0:15]\n\t"
    "ds_read_b128 v[88:91], v108 offset:26624\n\t"
    "v_cvt_pk_bf16_f32 v100, v104, v105\n\t"
    "v_exp_f32 v107, v27\n\t"
    "v_exp_f32 v104, v28\n\t"
    "v_cvt_pk_bf16_f32 v101, v106, v107\n\t"
    "v_exp_f32 v105, v29\n\t"
    "v_mfma_f32_16x16x32_bf16 v[64:67], v[76:79], v[96:99], v[64:67]\n\t"
    "s_waitcnt lgkmcnt(1)\n\t"
    "global_load_dword v111, v[172:173], off\n\t"
    "v_exp_f32 v106, v30\n\t"
    "v_cvt_pk_bf16_f32 v102, v104, v105\n\t"
    "v_exp_f32 v107, v31\n\t"
    "v_mfma_f32_32x32x16_bf16 v[16:31], v[80:83], v[68:71], v[48:63]\n\t"
    "ds_read_b128 v[80:83], v108 offset:28672\n\t"
    "v_cvt_pk_bf16_f32 v103, v106, v107\n\t"
    "v_exp_f32 v104, v32\n\t"
    "v_exp_f32 v105, v33\n\t"
    "v_exp_f32 v106, v34\n\t"
    "v_cvt_pk_bf16_f32 v96, v104, v105\n\t"
    "v_mfma_f32_32x32x16_bf16 v[16:31], v[84:87], v[72:75], v[16:31]\n\t"
    "ds_read_b128 v[84:87], v108 offset:29696\n\t"
    "v_exp_f32 v107, v35\n\t"
    "v_exp_f32 v104, v36\n\t"
    "v_cvt_pk_bf16_f32 v97, v106, v107\n\t"
    "v_exp_f32 v105, v37\n\t"
    "v_mfma_f32_32x32x16_bf16 v[0:15], v[92:95], v[100:103], v[0:15]\n\t"
    "ds_read_b128 v[92:95], v108 offset:31744\n\t"
    "v_exp_f32 v106, v38\n\t"
    "v_cvt_pk_bf16_f32 v98, v104, v105\n\t"
    "v_exp_f32 v107, v39\n\t"
    "v_exp_f32 v104, v40\n\t"
    "v_mfma_f32_16x16x32_bf16 v[64:67], v[76:79], v[100:103], v[64:67]\n\t"
    "s_waitcnt lgkmcnt(3)\n\t"
    "v_cvt_pk_bf16_f32 v99, v106, v107\n\t"
    "v_exp_f32 v105, v41\n\t"
    "v_exp_f32 v106, v42\n\t"
    "v_mfma_f32_32x32x16_bf16 v[0:15], v[88:91], v[96:99], v[0:15]\n\t"
    "ds_read_b128 v[88:91], v108 offset:30720\n\t"
    "v_cvt_pk_bf16_f32 v100, v104, v105\n\t"
    "v_exp_f32 v107, v43\n\t"
    "v_exp_f32 v104, v44\n\t"
    "v_cvt_pk_bf16_f32 v101, v106, v107\n\t"
    "v_exp_f32 v105, v45\n\t"
    "v_mfma_f32_16x16x32_bf16 v[64:67], v[76:79], v[96:99], v[64:67]\n\t"
    "v_lshl_add_u64 v[172:173], v[172:173], 0, v[174:175]\n\t"
    "v_exp_f32 v106, v46\n\t"
    "v_cvt_pk_bf16_f32 v102, v104, v105\n\t"
    "v_exp_f32 v107, v47\n\t"
    "s_waitcnt vmcnt(7) lgkmcnt(0)\n\t"
    "s_barrier\n\t"
    "v_add_u32 v108, 98304, v180\n\t"
    "s_setprio 3\n\t"
    "v_mfma_f32_32x32x16_bf16 v[32:47], v[80:83], v[68:71], v[48:63]\n\t"
    "ds_read_b128 v[80:83], v108 offset:0\n\t"
    "v_cvt_pk_bf16_f32 v103, v106, v107\n\t"
    "v_exp_f32 v104, v16\n\t"
    "v_exp_f32 v105, v17\n\t"
    "v_exp_f32 v106, v18\n\t"
    "v_cvt_pk_bf16_f32 v96, v104, v105\n\t"
    "v_mfma_f32_32x32x16_bf16 v[32:47], v[84:87], v[72:75], v[32:47]\n\t"
    "ds_read_b128 v[84:87], v108 offset:1024\n\t"
    "v_exp_f32 v107, v19\n\t"
    "v_exp_f32 v104, v20\n\t"
    "v_cvt_pk_bf16_f32 v97, v106, v107\n\t"
    "v_exp_f32 v105, v21\n\t"
    "v_mfma_f32_32x32x16_bf16 v[0:15], v[92:95], v[100:103], v[0:15]\n\t"
    "ds_read_b128 v[92:95], v108 offset:3072\n\t"
    "v_exp_f32 v106, v22\n\t"
    "v_cvt_pk_bf16_f32 v98, v104, v105\n\t"
    "v_exp_f32 v107, v23\n\t"
    "v_exp_f32 v104, v24\n\t"
    "v_mfma_f32_16x16x32_bf16 v[64:67], v[76:79], v[100:103], v[64:67]\n\t"
    "s_waitcnt lgkmcnt(3)\n\t"
    "v_cvt_pk_bf16_f32 v99, v106, v107\n\t"
    "v_exp_f32 v105, v25\n\t"
    "v_exp_f32 v106, v26\n\t"
    "v_mfma_f32_32x32x16_bf16 v[0:15], v[88:91], v[96:99], v[0:15]\n\t"
    "ds_read_b128 v[88:91], v108 offset:2048\n\t"
    "v_cvt_pk_bf16_f32 v100, v104, v105\n\t"
    "v_exp_f32 v107, v27\n\t"
    "v_exp_f32 v104, v28\n\t"
    "v_cvt_pk_bf16_f32 v101, v106, v107\n\t"
    "v_exp_f32 v105, v29\n\t"
    "v_mfma_f32_16x16x32_bf16 v[64:67], v[76:79], v[96:99], v[64:67]\n\t"
    "s_waitcnt lgkmcnt(1)\n\t"
    "v_mov_b32 v144, -1\n\t"
    "v_mov_b32 v145, -1\n\t"
    "v_mov_b32 v146, -1\n\t"
    "v_mov_b32 v147, -1\n\t"
    "s_add_u32 m0, s47, 8192\n\t"
    "v_exp_f32 v106, v30\n\t"
    "v_cvt_pk_bf16_f32 v102, v104, v105\n\t"
    "v_exp_f32 v107, v31\n\t"
    "v_mfma_f32_32x32x16_bf16 v[16:31], v[80:83], v[68:71], v[48:63]\n\t"
    "ds_read_b128 v[80:83], v108 offset:4096\n\t"
    "v_cvt_pk_bf16_f32 v103, v106, v107\n\t"
    "v_exp_f32 v104, v32\n\t"
    "v_exp_f32 v105, v33\n\t"
    "v_exp_f32 v106, v34\n\t"
    "v_cvt_pk_bf16_f32 v96, v104, v105\n\t"
    "v_mfma_f32_32x32x16_bf16 v[16:31], v[84:87], v[72:75], v[16:31]\n\t"
    "ds_read_b128 v[84:87], v108 offset:5120\n\t"
    "v_exp_f32 v107, v35\n\t"
    "v_exp_f32 v104, v36\n\t"
    "v_cvt_pk_bf16_f32 v97, v106, v107\n\t"
    "v_exp_f32 v105, v37\n\t"
    "v_mfma_f32_32x32x16_bf16 v[0:15], v[92:95], v[100:103], v[0:15]\n\t"
    "ds_read_b128 v[92:95], v108 offset:7168\n\t"
    "v_exp_f32 v106, v38\n\t"
    "v_cvt_pk_bf16_f32 v98, v104, v105\n\t"
    "v_exp_f32 v107, v39\n\t"
    "v_exp_f32 v104, v40\n\t"
    "v_mfma_f32_16x16x32_bf16 v[64:67], v[76:79], v[100:103], v[64:67]\n\t"
    "s_waitcnt lgkmcnt(3)\n\t"
    "global_load_lds_dwordx4 v177, s[54:55]\n\t"
    "s_add_u32 s54, s54, 0x10000\n\t"
    "s_addc_u32 s55, s55, 0\n\t"
    "v_cvt_pk_bf16_f32 v99, v106, v107\n\t"
    "v_exp_f32 v105, v41\n\t"
    "v_exp_f32 v106, v42\n\t"
    "v_mfma_f32_32x32x16_bf16 v[0:15], v[88:91], v[96:99], v[0:15]\n\t"
    "ds_read_b128 v[88:91], v108 offset:6144\n\t"
    "v_cvt_pk_bf16_f32 v100, v104, v105\n\t"
    "v_exp_f32 v107, v43\n\t"
    "v_exp_f32 v104, v44\n\t"
    "v_cvt_pk_bf16_f32 v101, v106, v107\n\t"
    "v_exp_f32 v105, v45\n\t"
    "v_mfma_f32_16x16x32_bf16 v[64:67], v[76:79], v[96:99], v[64:67]\n\t"
    "s_waitcnt lgkmcnt(1)\n\t"
    "buffer_store_dwordx4 v[144:147], v109, s[48:51], 0 offen sc1\n\t"
    "s_add_u32 m0, s47, 16384\n\t"
    "v_exp_f32 v106, v46\n\t"
    "v_cvt_pk_bf16_f32 v102, v104, v105\n\t"
    "v_exp_f32 v107, v47\n\t"
    "s_setprio 2\n\t"
    "v_mfma_f32_32x32x16_bf16 v[32:47], v[80:83], v[68:71], v[48:63]\n\t"
    "ds_read_b128 v[80:83], v108 offset:8192\n\t"
    "v_cvt_pk_bf16_f32 v103, v106, v107\n\t"
    "v_exp_f32 v104, v16\n\t"
    "v_exp_f32 v105, v17\n\t"
    "v_exp_f32 v106, v18\n\t"
    "v_cvt_pk_bf16_f32 v96, v104, v105\n\t"
    "v_mfma_f32_32x32x16_bf16 v[32:47], v[84:87], v[72:75], v[32:47]\n\t"
    "ds_read_b128 v[84:87], v108 offset:9216\n\t"
    "v_exp_f32 v107, v19\n\t"
    "v_exp_f32 v104, v20\n\t"
    "v_cvt_pk_bf16_f32 v97, v106, v107\n\t"
    "v_exp_f32 v105, v21\n\t"
    "v_mfma_f32_32x32x16_bf16 v[0:15], v[92:95], v[100:103], v[0:15]\n\t"
    "ds_read_b128 v[92:95], v108 offset:11264\n\t"
    "v_exp_f32 v106, v22\n\t"
    "v_cvt_pk_bf16_f32 v98, v104, v105\n\t"
    "v_exp_f32 v107, v23\n\t"
    "v_exp_f32 v104, v24\n\t"
    "v_mfma_f32_16x16x32_bf16 v[64:67], v[76:79], v[100:103], v[64:67]\n\t"
    "s_waitcnt lgkmcnt(3)\n\t"
    "global_load_lds_dwordx4 v177, s[54:55]\n\t"
    "s_add_u32 s54, s54, 0x10000\n\t"
    "s_addc_u32 s55, s55, 0\n\t"
    "v_cvt_pk_bf16_f32 v99, v106, v107\n\t"
    "v_exp_f32 v105, v25\n\t"
    "v_exp_f32 v106, v26\n\t"
    "v_mfma_f32_32x32x16_bf16 v[0:15], v[88:91], v[96:99], v[0:15]\n\t"
    "ds_read_b128 v[88:91], v108 offset:10240\n\t"
    "v_cvt_pk_bf16_f32 v100, v104, v105\n\t"
    "v_exp_f32 v107, v27\n\t"
    "v_exp_f32 v104, v28\n\t"
    "v_cvt_pk_bf16_f32 v101, v106, v107\n\t"
    "v_exp_f32 v105, v29\n\t"
    "v_mfma_f32_16x16x32_bf16 v[64:67], v[76:79], v[96:99], v[64:67]\n\t"
    "s_waitcnt lgkmcnt(1)\n\t"
    "buffer_store_dwordx4 v[144:147], v109, s[48:51], 0 offen offset:1024 sc1\n\t"
    "s_add_u32 m0, s47, 32768\n\t"
    "v_exp_f32 v106, v30\n\t"
    "v_cvt_pk_bf16_f32 v102, v104, v105\n\t"
    "v_exp_f32 v107, v31\n\t"
    "v_mfma_f32_32x32x16_bf16 v[16:31], v[80:83], v[68:71], v[48:63]\n\t"
    "ds_read_b128 v[80:83], v108 offset:12288\n\t"
    "v_cvt_pk_bf16_f32 v103, v106, v107\n\t"
    "v_exp_f32 v104, v32\n\t"
    "v_exp_f32 v105, v33\n\t"
    "v_exp_f32 v106, v34\n\t"
    "v_cvt_pk_bf16_f32 v96, v104, v105\n\t"
    "v_mfma_f32_32x32x16_bf16 v[16:31], v[84:87], v[72:75], v[16:31]\n\t"
    "ds_read_b128 v[84:87], v108 offset:13312\n\t"
    "v_exp_f32 v107, v35\n\t"
    "v_exp_f32 v104, v36\n\t"
    "v_cvt_pk_bf16_f32 v97, v106, v107\n\t"
    "v_exp_f32 v105, v37\n\t"
    "v_mfma_f32_32x32x16_bf16 v[0:15], v[92:95], v[100:103], v[0:15]\n\t"
    "ds_read_b128 v[92:95], v108 offset:15360\n\t"
    "v_exp_f32 v106, v38\n\t"
    "v_cvt_pk_bf16_f32 v98, v104, v105\n\t"
    "v_exp_f32 v107, v39\n\t"
    "v_exp_f32 v104, v40\n\t"
    "v_mfma_f32_16x16x32_bf16 v[64:67], v[76:79], v[100:103], v[64:67]\n\t"
    "s_waitcnt lgkmcnt(3)\n\t"
    "global_load_lds_dwordx4 v177, s[54:55]\n\t"
    "s_add_u32 s54, s54, 0x10000\n\t"
    "s_addc_u32 s55, s55, 0\n\t"
    "v_cvt_pk_bf16_f32 v99, v106, v107\n\t"
    "v_exp_f32 v105, v41\n\t"
    "v_exp_f32 v106, v42\n\t"
    "v_mfma_f32_32x32x16_bf16 v[0:15], v[88:91], v[96:99], v[0:15]\n\t"
    "ds_read_b128 v[88:91], v108 offset:14336\n\t"
    "v_cvt_pk_bf16_f32 v100, v104, v105\n\t"
    "v_exp_f32 v107, v43\n\t"
    "v_exp_f32 v104, v44\n\t"
    "v_cvt_pk_bf16_f32 v101, v106, v107\n\t"
    "v_exp_f32 v105, v45\n\t"
    "v_mfma_f32_16x16x32_bf16 v[64:67], v[76:79], v[96:99], v[64:67]\n\t"
    "s_waitcnt lgkmcnt(1)\n\t"
    "s_add_u32 m0, s47, 40960\n\t"
    "v_exp_f32 v106, v46\n\t"
    "v_cvt_pk_bf16_f32 v102, v104, v105\n\t"
    "v_exp_f32 v107, v47\n\t"
    "s_setprio 1\n\t"
    "v_mfma_f32_32x32x16_bf16 v[32:47], v[80:83], v[68:71], v[48:63]\n\t"
    "ds_read_b128 v[80:83], v108 offset:16384\n\t"
    "v_cvt_pk_bf16_f32 v103, v106, v107\n\t"
    "v_exp_f32 v104, v16\n\t"
    "v_exp_f32 v105, v17\n\t"
    "v_exp_f32 v106, v18\n\t"
    "v_cvt_pk_bf16_f32 v96, v104, v105\n\t"
    "v_mfma_f32_32x32x16_bf16 v[32:47], v[84:87], v[72:75], v[32:47]\n\t"
    "ds_read_b128 v[84:87], v108 offset:17408\n\t"
    "v_exp_f32 v107, v19\n\t"
    "v_exp_f32 v104, v20\n\t"
    "v_cvt_pk_bf16_f32 v97, v106, v107\n\t"
    "v_exp_f32 v105, v21\n\t"
    "v_mfma_f32_32x32x16_bf16 v[0:15], v[92:95], v[100:103], v[0:15]\n\t"
    "ds_read_b128 v[92:95], v108 offset:19456\n\t"
    "v_exp_f32 v106, v22\n\t"
    "v_cvt_pk_bf16_f32 v98, v104, v105\n\t"
    "v_exp_f32 v107, v23\n\t"
    "v_exp_f32 v104, v24\n\t"
    "v_mfma_f32_16x16x32_bf16 v[64:67], v[76:79], v[100:103], v[64:67]\n\t"
    "s_waitcnt lgkmcnt(3)\n\t"
    "global_load_lds_dwordx4 v177, s[54:55]\n\t"
    "s_add_u32 s54, s54, 0x10000\n\t"
    "s_addc_u32 s55, s55, 0\n\t"
    "v_cvt_pk_bf16_f32 v99, v106, v107\n\t"
    "v_exp_f32 v105, v25\n\t"
    "v_exp_f32 v106, v26\n\t"
    "v_mfma_f32_32x32x16_bf16 v[0:15], v[88:91], v[96:99], v[0:15]\n\t"
    "ds_read_b128 v[88:91], v108 offset:18432\n\t"
    "v_cvt_pk_bf16_f32 v100, v104, v105\n\t"
    "v_exp_f32 v107, v27\n\t"
    "v_exp_f32 v104, v28\n\t"
    "v_cvt_pk_bf16_f32 v101, v106, v107\n\t"
    "v_exp_f32 v105, v29\n\t"
    "v_mfma_f32_16x16x32_bf16 v[64:67], v[76:79], v[96:99], v[64:67]\n\t"
    "s_waitcnt lgkmcnt(1)\n\t"
    "v_exp_f32 v106, v30\n\t"
    "v_cvt_pk_bf16_f32 v102, v104, v105\n\t"
    "v_exp_f32 v107, v31\n\t"
    "v_mfma_f32_32x32x16_bf16 v[16:31], v[80:83], v[68:71], v[48:63]\n\t"
    "ds_read_b128 v[80:83], v108 offset:20480\n\t"
    "v_cvt_pk_bf16_f32 v103, v106, v107\n\t"
    "v_exp_f32 v104, v32\n\t"
    "v_exp_f32 v105, v33\n\t"
    "v_exp_f32 v106, v34\n\t"
    "v_cvt_pk_bf16_f32 v96, v104, v105\n\t"
    "v_mfma_f32_32x32x16_bf16 v[16:31], v[84:87], v[72:75], v[16:31]\n\t"
    "ds_read_b128 v[84:87], v108 offset:21504\n\t"
    "v_exp_f32 v107, v35\n\t"
    "v_exp_f32 v104, v36\n\t"
    "v_cvt_pk_bf16_f32 v97, v106, v107\n\t"
    "v_exp_f32 v105, v37\n\t"
    "v_mfma_f32_32x32x16_bf16 v[0:15], v[92:95], v[100:103], v[0:15]\n\t"
    "ds_read_b128 v[92:95], v108 offset:23552\n\t"
    "v_exp_f32 v106, v38\n\t"
    "v_cvt_pk_bf16_f32 v98, v104, v105\n\t"
    "v_exp_f32 v107, v39\n\t"
    "v_exp_f32 v104, v40\n\t"
    "v_mfma_f32_16x16x32_bf16 v[64:67], v[76:79], v[100:103], v[64:67]\n\t"
    "s_waitcnt lgkmcnt(3)\n\t"
    "v_cvt_pk_bf16_f32 v99, v106, v107\n\t"
    "v_exp_f32 v105, v41\n\t"
    "v_exp_f32 v106, v42\n\t"
    "v_mfma_f32_32x32x16_bf16 v[0:15], v[88:91], v[96:99], v[0:15]\n\t"
    "ds_read_b128 v[88:91], v108 offset:22528\n\t"
    "v_cvt_pk_bf16_f32 v100, v104, v105\n\t"
    "v_exp_f32 v107, v43\n\t"
    "v_exp_f32 v104, v44\n\t"
    "v_cvt_pk_bf16_f32 v101, v106, v107\n\t"
    "v_exp_f32 v105, v45\n\t"
    "v_mfma_f32_16x16x32_bf16 v[64:67], v[76:79], v[96:99], v[64:67]\n\t"
    "s_waitcnt lgkmcnt(1)\n\t"
    "v_exp_f32 v106, v46\n\t"
    "v_cvt_pk_bf16_f32 v102, v104, v105\n\t"
    "v_exp_f32 v107, v47\n\t"
    "s_setprio 0\n\t"
    "v_mfma_f32_32x32x16_bf16 v[32:47], v[80:83], v[68:71], v[48:63]\n\t"
    "ds_read_b128 v[80:83], v108 offset:24576\n\t"
    "v_cvt_pk_bf16_f32 v103, v106, v107\n\t"
    "v_exp_f32 v104, v16\n\t"
    "v_exp_f32 v105, v17\n\t"
    "v_exp_f32 v106, v18\n\t"
    "v_cvt_pk_bf16_f32 v96, v104, v105\n\t"
    "v_mfma_f32_32x32x16_bf16 v[32:47], v[84:87], v[72:75], v[32:47]\n\t"
    "ds_read_b128 v[84:87], v108 offset:25600\n\t"
    "v_exp_f32 v107, v19\n\t"
    "v_exp_f32 v104, v20\n\t"
    "v_cvt_pk_bf16_f32 v97, v106, v107\n\t"
    "v_exp_f32 v105, v21\n\t"
    "v_mfma_f32_32x32x16_bf16 v[0:15], v[92:95], v[100:103], v[0:15]\n\t"
    "ds_read_b128 v[92:95], v108 offset:27648\n\t"
    "v_exp_f32 v106, v22\n\t"
    "v_cvt_pk_bf16_f32 v98, v104, v105\n\t"
    "v_exp_f32 v107, v23\n\t"
    "v_exp_f32 v104, v24\n\t"
    "v_mfma_f32_16x16x32_bf16 v[64:67], v[76:79], v[100:103], v[64:67]\n\t"
    "s_waitcnt lgkmcnt(3)\n\t"
    "v_cvt_pk_bf16_f32 v99, v106, v107\n\t"
    "v_exp_f32 v105, v25\n\t"
    "v_exp_f32 v106, v26\n\t"
    "v_mfma_f32_32x32x16_bf16 v[0:15], v[88:91], v[96:99], v[0:15]\n\t"
    "ds_read_b128 v[88:91], v108 offset:26624\n\t"
    "v_cvt_pk_bf16_f32 v100, v104, v105\n\t"
    "v_exp_f32 v107, v27\n\t"
    "v_exp_f32 v104, v28\n\t"
    "v_cvt_pk_bf16_f32 v101, v106, v107\n\t"
    "v_exp_f32 v105, v29\n\t"
    "v_mfma_f32_16x16x32_bf16 v[64:67], v[76:79], v[96:99], v[64:67]\n\t"
    "s_waitcnt lgkmcnt(1)\n\t"
    "global_load_dword v111, v[172:173], off\n\t"
    "v_exp_f32 v106, v30\n\t"
    "v_cvt_pk_bf16_f32 v102, v104, v105\n\t"
    "v_exp_f32 v107, v31\n\t"
    "v_mfma_f32_32x32x16_bf16 v[16:31], v[80:83], v[68:71], v[48:63]\n\t"
    "ds_read_b128 v[80:83], v108 offset:28672\n\t"
    "v_cvt_pk_bf16_f32 v103, v106, v107\n\t"
    "v_exp_f32 v104, v32\n\t"
    "v_exp_f32 v105, v33\n\t"
    "v_exp_f32 v106, v34\n\t"
    "v_cvt_pk_bf16_f32 v96, v104, v105\n\t"
    "v_mfma_f32_32x32x16_bf16 v[16:31], v[84:87], v[72:75], v[16:31]\n\t"
    "ds_read_b128 v[84:87], v108 offset:29696\n\t"
    "v_exp_f32 v107, v35\n\t"
    "v_exp_f32 v104, v36\n\t"
    "v_cvt_pk_bf16_f32 v97, v106, v107\n\t"
    "v_exp_f32 v105, v37\n\t"
    "v_mfma_f32_32x32x16_bf16 v[0:15], v[92:95], v[100:103], v[0:15]\n\t"
    "ds_read_b128 v[92:95], v108 offset:31744\n\t"
    "v_exp_f32 v106, v38\n\t"
    "v_cvt_pk_bf16_f32 v98, v104, v105\n\t"
    "v_exp_f32 v107, v39\n\t"
    "v_exp_f32 v104, v40\n\t"
    "v_mfma_f32_16x16x32_bf16 v[64:67], v[76:79], v[100:103], v[64:67]\n\t"
    "s_waitcnt lgkmcnt(3)\n\t"
    "v_cvt_pk_bf16_f32 v99, v106, v107\n\t"
    "v_exp_f32 v105, v41\n\t"
    "v_exp_f32 v106, v42\n\t"
    "v_mfma_f32_32x32x16_bf16 v[0:15], v[88:91], v[96:99], v[0:15]\n\t"
    "ds_read_b128 v[88:91], v108 offset:30720\n\t"
    "v_cvt_pk_bf16_f32 v100, v104, v105\n\t"
    "v_exp_f32 v107, v43\n\t"
    "v_exp_f32 v104, v44\n\t"
    "v_cvt_pk_bf16_f32 v101, v106, v107\n\t"
    "v_exp_f32 v105, v45\n\t"
    "v_mfma_f32_16x16x32_bf16 v[64:67], v[76:79], v[96:99], v[64:67]\n\t"
    "v_exp_f32 v106, v46\n\t"
    "v_cvt_pk_bf16_f32 v102, v104, v105\n\t"
    "v_exp_f32 v107, v47\n\t"
    "s_waitcnt vmcnt(7) lgkmcnt(0)\n\t"
    "s_barrier\n\t"
    "v_add_u32 v108, 131072, v180\n\t"
    "s_setprio 3\n\t"
    "v_mfma_f32_32x32x16_bf16 v[32:47], v[80:83], v[68:71], v[48:63]\n\t"
    "ds_read_b128 v[80:83], v108 offset:0\n\t"
    "v_cvt_pk_bf16_f32 v103, v106, v107\n\t"
    "v_exp_f32 v104, v16\n\t"
    "v_exp_f32 v105, v17\n\t"
    "v_exp_f32 v106, v18\n\t"
    "v_cvt_pk_bf16_f32 v96, v104, v105\n\t"
    "v_mfma_f32_32x32x16_bf16 v[32:47], v[84:87], v[72:75], v[32:47]\n\t"
    "ds_read_b128 v[84:87], v108 offset:1024\n\t"
    "v_exp_f32 v107, v19\n\t"
    "v_exp_f32 v104, v20\n\t"
    "v_cvt_pk_bf16_f32 v97, v106, v107\n\t"
    "v_exp_f32 v105, v21\n\t"
    "v_mfma_f32_32x32x16_bf16 v[0:15], v[92:95], v[100:103], v[0:15]\n\t"
    "ds_read_b128 v[92:95], v108 offset:3072\n\t"
    "v_exp_f32 v106, v22\n\t"
    "v_cvt_pk_bf16_f32 v98, v104, v105\n\t"
    "v_exp_f32 v107, v23\n\t"
    "v_exp_f32 v104, v24\n\t"
    "v_mfma_f32_16x16x32_bf16 v[64:67], v[76:79], v[100:103], v[64:67]\n\t"
    "s_waitcnt lgkmcnt(3)\n\t"
    "v_cvt_pk_bf16_f32 v99, v106, v107\n\t"
    "v_exp_f32 v105, v25\n\t"
    "v_exp_f32 v106, v26\n\t"
    "v_mfma_f32_32x32x16_bf16 v[0:15], v[88:91], v[96:99], v[0:15]\n\t"
    "ds_read_b128 v[88:91], v108 offset:2048\n\t"
    "v_cvt_pk_bf16_f32 v100, v104, v105\n\t"
    "v_exp_f32 v107, v27\n\t"
    "v_exp_f32 v104, v28\n\t"
    "v_cvt_pk_bf16_f32 v101, v106, v107\n\t"
    "v_exp_f32 v105, v29\n\t"
    "v_mfma_f32_16x16x32_bf16 v[64:67], v[76:79], v[96:99], v[64:67]\n\t"
    "s_waitcnt lgkmcnt(1)\n\t"
    "s_add_u32 m0, s47, 57344\n\t"
    "v_exp_f32 v106, v30\n\t"
    "v_cvt_pk_bf16_f32 v102, v104, v105\n\t"
    "v_exp_f32 v107, v31\n\t"
    "v_mfma_f32_32x32x16_bf16 v[16:31], v[80:83], v[68:71], v[48:63]\n\t"
    "ds_read_b128 v[80:83], v108 offset:4096\n\t"
    "v_cvt_pk_bf16_f32 v103, v106, v107\n\t"
    "v_exp_f32 v104, v32\n\t"
    "v_exp_f32 v105, v33\n\t"
    "v_exp_f32 v106, v34\n\t"
    "v_cvt_pk_bf16_f32 v96, v104, v105\n\t"
    "v_mfma_f32_32x32x16_bf16 v[16:31], v[84:87], v[72:75], v[16:31]\n\t"
    "ds_read_b128 v[84:87], v108 offset:5120\n\t"
    "v_exp_f32 v107, v35\n\t"
    "v_exp_f32 v104, v36\n\t"
    "v_cvt_pk_bf16_f32 v97, v106, v107\n\t"
    "v_exp_f32 v105, v37\n\t"
    "v_mfma_f32_32x32x16_bf16 v[0:15], v[92:95], v[100:103], v[0:15]\n\t"
    "ds_read_b128 v[92:95], v108 offset:7168\n\t"
    "v_exp_f32 v106, v38\n\t"
    "v_cvt_pk_bf16_f32 v98, v104, v105\n\t"
    "v_exp_f32 v107, v39\n\t"
    "v_exp_f32 v104, v40\n\t"
    "v_mfma_f32_16x16x32_bf16 v[64:67], v[76:79], v[100:103], v[64:67]\n\t"
    "s_waitcnt lgkmcnt(3)\n\t"
    "global_load_lds_dwordx4 v177, s[56:57]\n\t"
    "s_add_u32 s56, s56, 0x10000\n\t"
    "s_addc_u32 s57, s57, 0\n\t"
    "v_cvt_pk_bf16_f32 v99, v106, v107\n\t"
    "v_exp_f32 v105, v41\n\t"
    "v_exp_f32 v106, v42\n\t"
    "v_mfma_f32_32x32x16_bf16 v[0:15], v[88:91], v[96:99], v[0:15]\n\t"
    "ds_read_b128 v[88:91], v108 offset:6144\n\t"
    "v_cvt_pk_bf16_f32 v100, v104, v105\n\t"
    "v_exp_f32 v107, v43\n\t"
    "v_exp_f32 v104, v44\n\t"
    "v_cvt_pk_bf16_f32 v101, v106, v107\n\t"
    "v_exp_f32 v105, v45\n\t"
    "v_mfma_f32_16x16x32_bf16 v[64:67], v[76:79], v[96:99], v[64:67]\n\t"
    "s_waitcnt lgkmcnt(1)\n\t"
    "s_add_u32 m0, s47, 65536\n\t"
    "v_exp_f32 v106, v46\n\t"
    "v_cvt_pk_bf16_f32 v102, v104, v105\n\t"
    "v_exp_f32 v107, v47\n\t"
    "s_setprio 2\n\t"
    "v_mfma_f32_32x32x16_bf16 v[32:47], v[80:83], v[68:71], v[48:63]\n\t"
    "ds_read_b128 v[80:83], v108 offset:8192\n\t"
    "v_cvt_pk_bf16_f32 v103, v106, v107\n\t"
    "v_exp_f32 v104, v16\n\t"
    "v_exp_f32 v105, v17\n\t"
    "v_exp_f32 v106, v18\n\t"
    "v_cvt_pk_bf16_f32 v96, v104, v105\n\t"
    "v_mfma_f32_32x32x16_bf16 v[32:47], v[84:87], v[72:75], v[32:47]\n\t"
    "ds_read_b128 v[84:87], v108 offset:9216\n\t"
    "v_exp_f32 v107, v19\n\t"
    "v_exp_f32 v104, v20\n\t"
    "v_cvt_pk_bf16_f32 v97, v106, v107\n\t"
    "v_exp_f32 v105, v21\n\t"
    "v_mfma_f32_32x32x16_bf16 v[0:15], v[92:95], v[100:103], v[0:15]\n\t"
    "ds_read_b128 v[92:95], v108 offset:11264\n\t"
    "v_exp_f32 v106, v22\n\t"
    "v_cvt_pk_bf16_f32 v98, v104, v105\n\t"
    "v_exp_f32 v107, v23\n\t"
    "v_exp_f32 v104, v24\n\t"
    "v_mfma_f32_16x16x32_bf16 v[64:67], v[76:79], v[100:103], v[64:67]\n\t"
    "s_waitcnt lgkmcnt(3)\n\t"
    "global_load_lds_dwordx4 v177, s[56:57]\n\t"
    "s_add_u32 s56, s56, 0x10000\n\t"
    "s_addc_u32 s57, s57, 0\n\t"
    "v_cvt_pk_bf16_f32 v99, v106, v107\n\t"
    "v_exp_f32 v105, v25\n\t"
    "v_exp_f32 v106, v26\n\t"
    "v_mfma_f32_32x32x16_bf16 v[0:15], v[88:91], v[96:99], v[0:15]\n\t"
    "ds_read_b128 v[88:91], v108 offset:10240\n\t"
    "v_cvt_pk_bf16_f32 v100, v104, v105\n\t"
    "v_exp_f32 v107, v27\n\t"
    "v_exp_f32 v104, v28\n\t"
    "v_cvt_pk_bf16_f32 v101, v106, v107\n\t"
    "v_exp_f32 v105, v29\n\t"
    "v_mfma_f32_16x16x32_bf16 v[64:67], v[76:79], v[96:99], v[64:67]\n\t"
    "s_waitcnt lgkmcnt(1)\n\t"
    "s_add_u32 m0, s47, 81920\n\t"
    "v_exp_f32 v106, v30\n\t"
    "v_cvt_pk_bf16_f32 v102, v104, v105\n\t"
    "v_exp_f32 v107, v31\n\t"
    "v_mfma_f32_32x32x16_bf16 v[16:31], v[80:83], v[68:71], v[48:63]\n\t"
    "ds_read_b128 v[80:83], v108 offset:12288\n\t"
    "v_cvt_pk_bf16_f32 v103, v106, v107\n\t"
    "v_exp_f32 v104, v32\n\t"
    "v_exp_f32 v105, v33\n\t"
    "v_exp_f32 v106, v34\n\t"
    "v_cvt_pk_bf16_f32 v96, v104, v105\n\t"
    "v_mfma_f32_32x32x16_bf16 v[16:31], v[84:87], v[72:75], v[16:31]\n\t"
    "ds_read_b128 v[84:87], v108 offset:13312\n\t"
    "v_exp_f32 v107, v35\n\t"
    "v_exp_f32 v104, v36\n\t"
    "v_cvt_pk_bf16_f32 v97, v106, v107\n\t"
    "v_exp_f32 v105, v37\n\t"
    "v_mfma_f32_32x32x16_bf16 v[0:15], v[92:95], v[100:103], v[0:15]\n\t"
    "ds_read_b128 v[92:95], v108 offset:15360\n\t"
    "v_exp_f32 v106, v38\n\t"
    "v_cvt_pk_bf16_f32 v98, v104, v105\n\t"
    "v_exp_f32 v107, v39\n\t"
    "v_exp_f32 v104, v40\n\t"
    "v_mfma_f32_16x16x32_bf16 v[64:67], v[76:79], v[100:103], v[64:67]\n\t"
    "s_waitcnt lgkmcnt(3)\n\t"
    "global_load_lds_dwordx4 v177, s[56:57]\n\t"
    "s_add_u32 s56, s56, 0x10000\n\t"
    "s_addc_u32 s57, s57, 0\n\t"
    "v_cvt_pk_bf16_f32 v99, v106, v107\n\t"
    "v_exp_f32 v105, v41\n\t"
    "v_exp_f32 v106, v42\n\t"
    "v_mfma_f32_32x32x16_bf16 v[0:15], v[88:91], v[96:99], v[0:15]\n\t"
    "ds_read_b128 v[88:91], v108 offset:14336\n\t"
    "v_cvt_pk_bf16_f32 v100, v104, v105\n\t"
    "v_exp_f32 v107, v43\n\t"
    "v_exp_f32 v104, v44\n\t"
    "v_cvt_pk_bf16_f32 v101, v106, v107\n\t"
    "v_exp_f32 v105, v45\n\t"
    "v_mfma_f32_16x16x32_bf16 v[64:67], v[76:79], v[96:99], v[64:67]\n\t"
    "s_waitcnt lgkmcnt(1)\n\t"
    "s_add_u32 m0, s47, 90112\n\t"
    "v_exp_f32 v106, v46\n\t"
    "v_cvt_pk_bf16_f32 v102, v104, v105\n\t"
    "v_exp_f32 v107, v47\n\t"
    "s_setprio 1\n\t"
    "v_mfma_f32_32x32x16_bf16 v[32:47], v[80:83], v[68:71], v[48:63]\n\t"
    "ds_read_b128 v[80:83], v108 offset:16384\n\t"
    "v_cvt_pk_bf16_f32 v103, v106, v107\n\t"
    "v_exp_f32 v104, v16\n\t"
    "v_exp_f32 v105, v17\n\t"
    "v_exp_f32 v106, v18\n\t"
    "v_cvt_pk_bf16_f32 v96, v104, v105\n\t"
    "v_mfma_f32_32x32x16_bf16 v[32:47], v[84:87], v[72:75], v[32:47]\n\t"
    "ds_read_b128 v[84:87], v108 offset:17408\n\t"
    "v_exp_f32 v107, v19\n\t"
    "v_exp_f32 v104, v20\n\t"
    "v_cvt_pk_bf16_f32 v97, v106, v107\n\t"
    "v_exp_f32 v105, v21\n\t"
    "v_mfma_f32_32x32x16_bf16 v[0:15], v[92:95], v[100:103], v[0:15]\n\t"
    "ds_read_b128 v[92:95], v108 offset:19456\n\t"
    "v_exp_f32 v106, v22\n\t"
    "v_cvt_pk_bf16_f32 v98, v104, v105\n\t"
    "v_exp_f32 v107, v23\n\t"
    "v_exp_f32 v104, v24\n\t"
    "v_mfma_f32_16x16x32_bf16 v[64:67], v[76:79], v[100:103], v[64:67]\n\t"
    "s_waitcnt lgkmcnt(3)\n\t"
    "global_load_lds_dwordx4 v177, s[56:57]\n\t"
    "s_add_u32 s56, s56, 0x10000\n\t"
    "s_addc_u32 s57, s57, 0\n\t"
    "v_cvt_pk_bf16_f32 v99, v106, v107\n\t"
    "v_exp_f32 v105, v25\n\t"
    "v_exp_f32 v106, v26\n\t"
    "v_mfma_f32_32x32x16_bf16 v[0:15], v[88:91], v[96:99], v[0:15]\n\t"
    "ds_read_b128 v[88:91], v108 offset:18432\n\t"
    "v_cvt_pk_bf16_f32 v100, v104, v105\n\t"
    "v_exp_f32 v107, v27\n\t"
    "v_exp_f32 v104, v28\n\t"
    "v_cvt_pk_bf16_f32 v101, v106, v107\n\t"
    "v_exp_f32 v105, v29\n\t"
    "v_mfma_f32_16x16x32_bf16 v[64:67], v[76:79], v[96:99], v[64:67]\n\t"
    "s_waitcnt lgkmcnt(1)\n\t"
    "v_exp_f32 v106, v30\n\t"
    "v_cvt_pk_bf16_f32 v102, v104, v105\n\t"
    "v_exp_f32 v107, v31\n\t"
    "v_mfma_f32_32x32x16_bf16 v[16:31], v[80:83], v[68:71], v[48:63]\n\t"
    "ds_read_b128 v[80:83], v108 offset:20480\n\t"
    "v_cvt_pk_bf16_f32 v103, v106, v107\n\t"
    "v_exp_f32 v104, v32\n\t"
    "v_exp_f32 v105, v33\n\t"
    "v_exp_f32 v106, v34\n\t"
    "v_cvt_pk_bf16_f32 v96, v104, v105\n\t"
    "v_mfma_f32_32x32x16_bf16 v[16:31], v[84:87], v[72:75], v[16:31]\n\t"
    "ds_read_b128 v[84:87], v108 offset:21504\n\t"
    "v_exp_f32 v107, v35\n\t"
    "v_exp_f32 v104, v36\n\t"
    "v_cvt_pk_bf16_f32 v97, v106, v107\n\t"
    "v_exp_f32 v105, v37\n\t"
    "v_mfma_f32_32x32x16_bf16 v[0:15], v[92:95], v[100:103], v[0:15]\n\t"
    "ds_read_b128 v[92:95], v108 offset:23552\n\t"
    "v_exp_f32 v106, v38\n\t"
    "v_cvt_pk_bf16_f32 v98, v104, v105\n\t"
    "v_exp_f32 v107, v39\n\t"
    "v_exp_f32 v104, v40\n\t"
    "v_mfma_f32_16x16x32_bf16 v[64:67], v[76:79], v[100:103], v[64:67]\n\t"
    "s_waitcnt lgkmcnt(3)\n\t"
    "v_cvt_pk_bf16_f32 v99, v106, v107\n\t"
    "v_exp_f32 v105, v41\n\t"
    "v_exp_f32 v106, v42\n\t"
    "v_mfma_f32_32x32x16_bf16 v[0:15], v[88:91], v[96:99], v[0:15]\n\t"
    "ds_read_b128 v[88:91], v108 offset:22528\n\t"
    "v_cvt_pk_bf16_f32 v100, v104, v105\n\t"
    "v_exp_f32 v107, v43\n\t"
    "v_exp_f32 v104, v44\n\t"
    "v_cvt_pk_bf16_f32 v101, v106, v107\n\t"
    "v_exp_f32 v105, v45\n\t"
    "v_mfma_f32_16x16x32_bf16 v[64:67], v[76:79], v[96:99], v[64:67]\n\t"
    "s_waitcnt lgkmcnt(1)\n\t"
    "v_exp_f32 v106, v46\n\t"
    "v_cvt_pk_bf16_f32 v102, v104, v105\n\t"
    "v_exp_f32 v107, v47\n\t"
    "s_setprio 0\n\t"
    "v_mfma_f32_32x32x16_bf16 v[32:47], v[80:83], v[68:71], v[48:63]\n\t"
    "ds_read_b128 v[80:83], v108 offset:24576\n\t"
    "v_cvt_pk_bf16_f32 v103, v106, v107\n\t"
    "v_exp_f32 v104, v16\n\t"
    "v_exp_f32 v105, v17\n\t"
    "v_exp_f32 v106, v18\n\t"
    "v_cvt_pk_bf16_f32 v96, v104, v105\n\t"
    "v_mfma_f32_32x32x16_bf16 v[32:47], v[84:87], v[72:75], v[32:47]\n\t"
    "ds_read_b128 v[84:87], v108 offset:25600\n\t"
    "v_exp_f32 v107, v19\n\t"
    "v_exp_f32 v104, v20\n\t"
    "v_cvt_pk_bf16_f32 v97, v106, v107\n\t"
    "v_exp_f32 v105, v21\n\t"
    "v_mfma_f32_32x32x16_bf16 v[0:15], v[92:95], v[100:103], v[0:15]\n\t"
    "ds_read_b128 v[92:95], v108 offset:27648\n\t"
    "v_exp_f32 v106, v22\n\t"
    "v_cvt_pk_bf16_f32 v98, v104, v105\n\t"
    "v_exp_f32 v107, v23\n\t"
    "v_exp_f32 v104, v24\n\t"
    "v_mfma_f32_16x16x32_bf16 v[64:67], v[76:79], v[100:103], v[64:67]\n\t"
    "s_waitcnt lgkmcnt(3)\n\t"
    "v_cvt_pk_bf16_f32 v99, v106, v107\n\t"
    "v_exp_f32 v105, v25\n\t"
    "v_exp_f32 v106, v26\n\t"
    "v_mfma_f32_32x32x16_bf16 v[0:15], v[88:91], v[96:99], v[0:15]\n\t"
    "ds_read_b128 v[88:91], v108 offset:26624\n\t"
    "v_cvt_pk_bf16_f32 v100, v104, v105\n\t"
    "v_exp_f32 v107, v27\n\t"
    "v_exp_f32 v104, v28\n\t"
    "v_cvt_pk_bf16_f32 v101, v106, v107\n\t"
    "v_exp_f32 v105, v29\n\t"
    "v_mfma_f32_16x16x32_bf16 v[64:67], v[76:79], v[96:99], v[64:67]\n\t"
    "s_waitcnt lgkmcnt(1)\n\t"
    "v_exp_f32 v106, v30\n\t"
    "v_cvt_pk_bf16_f32 v102, v104, v105\n\t"
    "v_exp_f32 v107, v31\n\t"
    "v_mfma_f32_32x32x16_bf16 v[16:31], v[80:83], v[68:71], v[48:63]\n\t"
    "ds_read_b128 v[80:83], v108 offset:28672\n\t"
    "v_cvt_pk_bf16_f32 v103, v106, v107\n\t"
    "v_exp_f32 v104, v32\n\t"
    "v_exp_f32 v105, v33\n\t"
    "v_exp_f32 v106, v34\n\t"
    "v_cvt_pk_bf16_f32 v96, v104, v105\n\t"
    "v_mfma_f32_32x32x16_bf16 v[16:31], v[84:87], v[72:75], v[16:31]\n\t"
    "ds_read_b128 v[84:87], v108 offset:29696\n\t"
    "v_exp_f32 v107, v35\n\t"
    "v_exp_f32 v104, v36\n\t"
    "v_cvt_pk_bf16_f32 v97, v106, v107\n\t"
    "v_exp_f32 v105, v37\n\t"
    "v_mfma_f32_32x32x16_bf16 v[0:15], v[92:95], v[100:103], v[0:15]\n\t"
    "ds_read_b128 v[92:95], v108 offset:31744\n\t"
    "v_exp_f32 v106, v38\n\t"
    "v_cvt_pk_bf16_f32 v98, v104, v105\n\t"
    "v_exp_f32 v107, v39\n\t"
    "v_exp_f32 v104, v40\n\t"
    "v_mfma_f32_16x16x32_bf16 v[64:67], v[76:79], v[100:103], v[64:67]\n\t"
    "s_waitcnt lgkmcnt(3)\n\t"
    "v_cvt_pk_bf16_f32 v99, v106, v107\n\t"
    "v_exp_f32 v105, v41\n\t"
    "v_exp_f32 v106, v42\n\t"
    "v_mfma_f32_32x32x16_bf16 v[0:15], v[88:91], v[96:99], v[0:15]\n\t"
    "ds_read_b128 v[88:91], v108 offset:30720\n\t"
    "v_cvt_pk_bf16_f32 v100, v104, v105\n\t"
    "v_exp_f32 v107, v43\n\t"
    "v_exp_f32 v104, v44\n\t"
    "v_cvt_pk_bf16_f32 v101, v106, v107\n\t"
    "v_exp_f32 v105, v45\n\t"
    "v_mfma_f32_16x16x32_bf16 v[64:67], v[76:79], v[96:99], v[64:67]\n\t"
    "v_exp_f32 v106, v46\n\t"
    "v_cvt_pk_bf16_f32 v102, v104, v105\n\t"
    "v_exp_f32 v107, v47\n\t"
    "s_waitcnt lgkmcnt(0)\n\t"
    "s_nop 11\n\t"
            "" : AOUT, "+{s[54:55]}"(os0), "+{s[56:57]}"(os2)
            : AIN, "{v177}"(wvo), "{v179}"(1u), "{s47}"(lds0 + wid * 1024), "{s[48:51]}"(xrs), "{s[60:61]}"(cnt + (72 + xcd) * 32)
            : ACLOB);
    }
    if (wid == 6) asm volatile("s_mov_b32 m0, %2\n\ts_nop 0\n\tglobal_load_lds_dword %0, %1 sc1" ::"v"((lane & 7) * 128), "s"(cnt + 72 * 32), "s"(lds0 + 49152) : "memory");
    p1[3] = cvtpk(tt[2], tt[3]);
    o = MFMA32(v1, asb(p1), o); l = MFMA16(sel, asb(p1), l);
    {
        float chk = l[0] * 1.0e8f;
#pragma unroll
        for (int r = 0; r < 16; ++r) chk += fabsf(o[r]) * 0.05f;
        if (__builtin_expect(__any(!(chk < 3.0e38f)), 0)) {
            __builtin_amdgcn_fence(__ATOMIC_ACQUIRE, "agent");
            const char* kb = kv + lane * 16;
            float mx = -3.0e38f;
            for (int t = 0; t < 64; ++t) {
                f32x16 s = {};
                s = MFMA32(*(const bf16x8*)(kb + t * 4096), qf0, s); s = MFMA32(*(const bf16x8*)(kb + t * 4096 + 1024), qf1, s);
#pragma unroll
                for (int r = 0; r < 16; ++r) mx = fmaxf(mx, s[r]);
            }
            mx = swapmax(mx);
#pragma unroll
            for (int r = 0; r < 16; ++r) { negm[r] = -mx; o[r] = 0.f; }
            l = f32x4{};
            for (int t = 0; t < 64; ++t) {
                f32x16 s = MFMA32(*(const bf16x8*)(kb + t * 4096), qf0, negm); s = MFMA32(*(const bf16x8*)(kb + t * 4096 + 1024), qf1, s);
#pragma unroll
                for (int j = 0; j < 4; ++j) {
                    p0[j] = cvtpk(__builtin_amdgcn_exp2f(s[2 * j]), __builtin_amdgcn_exp2f(s[2 * j + 1]));
                    p1[j] = cvtpk(__builtin_amdgcn_exp2f(s[8 + 2 * j]), __builtin_amdgcn_exp2f(s[9 + 2 * j]));
                }
                o = MFMA32(*(const bf16x8*)(kb + ((t + 1) & 63) * 4096 + 2048), asb(p0), o); o = MFMA32(*(const bf16x8*)(kb + ((t + 2) & 63) * 4096 + 3072), asb(p1), o);
                l = MFMA16(sel, asb(p0), l); l = MFMA16(sel, asb(p1), l);
            }
        }
    }
    const float rl = __builtin_amdgcn_rcpf(l[0]);
    u32x4 o0, o1;
#pragma unroll
    for (int j = 0; j < 4; ++j) { o0[j] = cvtpk(o[2 * j] * rl, o[2 * j + 1] * rl); o1[j] = cvtpk(o[8 + 2 * j] * rl, o[9 + 2 * j] * rl); }
    const __amdgpu_buffer_rsrc_t ors = mkrs(ws + WS_O + ((size_t)(tg * 2 + (wid & 1)) * 64 + (hg * 4 + (wid >> 1)) * 2) * 1024, 2048);
    st16wt(ors, l16, 0, o0); st16wt(ors, l16, 1024, o1);
    const int ng = hg;
    const unsigned xt = lds0 + 114688, sta = lds0 + 147456;
    const int nt = xcd * 4 + (idx & 3), mt = idx >> 2;
    const int wm = wid >> 1, wc = wid & 1;
    const unsigned wmask = (unsigned)((wid - 7) >> 31);
    const int G = nt >> 1, par = nt & 1;
    const int lwc = wid >= 3, lp = wid - 3 * lwc;
    const int phi = 28 * G + (wid >= 6 ? 24 : lp < 2 ? 8 * par + 4 * lwc + 2 * lp : 16 + 4 * par + 2 * lwc);
    const char* xs0 = ws + WS_Y + (size_t)(mt * 16 + wid * 2) * 32768;
    const char* ws0 = ws + WS_WGU + (size_t)(phi >> 3) * 262144 + (phi & 7) * 1024;
    const unsigned xvo = lane * 16;
    unsigned gwvo = (wid == 6 ? ((lane >> 3) & 1) * 1024 + (8 * par + (lane & 7) + (lane & 48)) * 16 : lane * 16) & wmask;
    const unsigned dx = lds0 + wid * 2048, wstep = 8192 & wmask, w2 = (wid == 6 ? 7168 : 1024) & wmask;
    const unsigned dw = lds0 + (((16384 + (wid == 6 ? 6144 : lwc * 7168 + lp * 2048)) & wmask) | ((5 * K5_SLOT) & ~wmask));
    const char* es0 = ws + WS_STAT + (size_t)wid * 16384 + mt * 2048;
    const char* es2 = ws + WS_UT + (size_t)((wid >> 1) * 448 + 28 * G) * 128;
    const int uj = (wid & 1) * 8 + (lane >> 3);
    unsigned evo = (uj < 8 ? 8 * par + uj : uj < 12 ? 8 + 4 * par + uj : 12 + uj) * 128 + (lane & 7) * 16;
    const unsigned de = lds0 + 16384 + wid * 1024;
    unsigned gxa = lds0 + wm * 4096 + lane * 16, gwa = lds0 + 16384 + wc * 7168 + lane * 16;
    f32x16 acc[7];
    f32x16 oa; f32x4 ot[4] = {}; float utv = 0.f;
    {
        const unsigned ow = lds0 + (wid >> 2) * 24576 + (wid & 1) * 4096 + ((wid >> 1) & 1) * 2048 + l16;
        *(LAS u32x4*)(size_t)ow = o0; *(LAS u32x4*)(size_t)(ow + 1024) = o1;
    asm volatile("" : "+v"(evo), "+v"(gwvo), "+v"(gxa), "+v"(gwa));
    asm volatile("" : "+s"(xs0), "+s"(ws0), "+s"(es0), "+s"(es2));
        if (wid == 6) {
            WAITV(2);
            unsigned gav = *(const LAS unsigned*)(size_t)(lds0 + 49152 + lane * 4);
#pragma nounroll
            for (unsigned spins = 0; !__all(gav == 32u) && spins < SPIN_MAX; ++spins) {
                __builtin_amdgcn_s_sleep(POLL_SLEEP);
                gav = __hip_atomic_load(cnt + (72 + (lane & 7)) * 32, __ATOMIC_RELAXED, __HIP_MEMORY_SCOPE_AGENT);
            }
        }
        asm volatile("s_waitcnt vmcnt(0) lgkmcnt(0)" : "+{v111}"(junk) :: "memory");
        BAR();
        if (tid == 448) __hip_atomic_store(cnt + (40 + tg) * 32 + hg, 1u, __ATOMIC_RELAXED, __HIP_MEMORY_SCOPE_AGENT);
        f32x4 gp, cp; u32x4 c0, c1;
        asm volatile("global_load_dwordx4 %0, %3, %5 sc1\n\tglobal_load_dwordx4 %1, %4, %6 sc1\n\tglobal_load_dwordx4 %2, %4, %6 offset:16 sc1" : "=&v"(gp), "=&v"(c0), "=&v"(c1)
                     : "v"((lane >> 3) * 8192 + (lane & 7) * 16), "v"(((lane >> 3) & 3) * 32768 + (lane & 7) * 32),
                       "s"(ws + WS_MODG + (ng * 4 + (wid >> 1)) * 128), "s"(ws + WS_MODP + 24576 + (ng * 4 + (wid >> 1)) * 256) : "memory");
        ostage(4); ostage(5);
#pragma unroll
        for (int s = 0; s < 4; ++s) {
            const unsigned bw = (s >> 1) * 24576 + (s & 1) * 8192, bx = xa + (s >> 1) * 24576 + (s & 1) * 2048;
            const bf16x8 wA = ldstr2(wa + bw, wa + bw + 1024), wB = ldstr2((wa ^ 32) + bw, (wa ^ 32) + bw + 1024), xA = lds128(bx), xB = lds128(bx + 256);
            ot[0] = MFMA16(wA, xA, ot[0]); ot[1] = MFMA16(wA, xB, ot[1]); ot[2] = MFMA16(wB, xA, ot[2]); ot[3] = MFMA16(wB, xB, ot[3]);
            if (s == 0 && wid == 7) asm volatile("s_mov_b32 m0, %2\n\ts_nop 0\n\tglobal_load_lds_dword %0, %1 sc1" ::"v"((lane & 7) * 4), "s"(cnt + (40 + tg) * 32), "s"(pollw) : "memory");
        }
#pragma unroll
        for (int i = 0; i < 16; ++i) oa[i] = ot[i >> 2][i & 3];
        if (rep && wid < 4) {
            float v = 0.f;
#pragma unroll
            for (int w = 0; w < 8; ++w) v += *(const LAS float*)(size_t)(lds0 + 73728 + w * 1024 + tid * 4);
            utv = v;
        }
        if (rep && wid < 4) __hip_atomic_store((GAS float*)(ws + WS_UT) + (kq * 448 + cu * 8) * 32 + tid, utv, __ATOMIC_RELAXED, __HIP_MEMORY_SCOPE_AGENT);
        ostage(6);
        if (wid == 7) {
            WAITV(2);
            if (!__all(*(const LAS unsigned*)(size_t)(pollw + lane * 4) == 1u)) {
#pragma nounroll
                for (unsigned spins = 0; spins < SPIN_MAX; ++spins) {
                    if (__all(__hip_atomic_load(cnt + (40 + tg) * 32 + (lane & 7), __ATOMIC_RELAXED, __HIP_MEMORY_SCOPE_AGENT) == 1u)) break;
                    __builtin_amdgcn_s_sleep(POLL_SLEEP);
                }
            }
        }
        WAITL0(); BAR();
        const char* xo_ = ws + WS_O + (size_t)(tg * 2 + (wid >> 2)) * 65536 + (wid & 3) * 1024;
#pragma unroll
        for (int j = 2; j < 6; ++j) glds16c(xo_ + ((2 * hg + j) & 15) * 4096, l16, lds0 + (j & 3) * 24576 + wid * 1024);
        asm volatile("s_waitcnt vmcnt(10)" : "+v"(gp), "+v"(c0), "+v"(c1) :: "memory");
        cp = f32x4{__uint_as_float(c0[0] & ~15u), __uint_as_float(c0[2] & ~15u), __uint_as_float(c1[0] & ~15u), __uint_as_float(c1[2] & ~15u)};
#pragma unroll
        for (int j = 0; j < 4; ++j) { gp[j] = bfr(sum_hi3(gp[j])); cp[j] = bfr(bfr(swap16add(ror8add(cp[j]))) + 1.f); }
        if (lane < 16) *(LAS f32x4*)(size_t)(sta + 2048 + wid * 256 + lane * 16) = lane < 8 ? gp : cp;
        ostage(7); ostage(8);
        f32x16 fa, fb, fc, fd; f32x16& qa = oa;
        asm volatile("" : "=v"(fa), "=v"(fb), "=v"(fc), "=v"(fd));
        const int xr_ = wid * 4 + lg;
#define OIN "{v94}"(xa), "{v95}"(wa), "{v98}"(l16q), "{v99}"(wvo), "{s[40:41]}"(xo_), "{s[42:43]}"(wo + hg * 256), "{s46}"(dstb), "{s47}"(2 * hg)
        asm volatile(
    "v_add_u32 v100, 49152, v94\n\t"
    "v_add_u32 v101, 49152, v95\n\t"
    "v_xor_b32 v102, 32, v101\n\t"
    "s_waitcnt vmcnt(7)\n\t"
    "s_barrier\n\t"
    "ds_read_b128 v[16:19], v100 offset:0\n\t"
    "ds_read_b64_tr_b16 v[32:33], v101 offset:0\n\t"
    "ds_read_b64_tr_b16 v[34:35], v101 offset:1024\n\t"
    "ds_read_b128 v[20:23], v100 offset:256\n\t"
    "ds_read_b64_tr_b16 v[36:37], v102 offset:0\n\t"
    "ds_read_b64_tr_b16 v[38:39], v102 offset:1024\n\t"
    "ds_read_b128 v[24:27], v100 offset:2048\n\t"
    "ds_read_b64_tr_b16 v[40:41], v101 offset:8192\n\t"
    "ds_read_b64_tr_b16 v[42:43], v101 offset:9216\n\t"
    "ds_read_b128 v[28:31], v100 offset:2304\n\t"
    "ds_read_b64_tr_b16 v[44:45], v102 offset:8192\n\t"
    "ds_read_b64_tr_b16 v[46:47], v102 offset:9216\n\t"
    "v_add_u32 v100, 73728, v94\n\t"
    "v_add_u32 v101, 73728, v95\n\t"
    "v_xor_b32 v102, 32, v101\n\t"
    "s_waitcnt vmcnt(6) lgkmcnt(0)\n\t"
    "s_barrier\n\t"
    "s_add_u32 s44, s47, 6\n\t"
    "s_and_b32 s44, s44, 15\n\t"
    "s_lshl_b32 s44, s44, 12\n\t"
    "s_add_u32 s44, s40, s44\n\t"
    "s_addc_u32 s45, s41, 0\n\t"
    "s_add_u32 m0, s46, 49152\n\t"
    "v_mfma_f32_16x16x32_bf16 v[0:3], v[32:35], v[16:19], v[0:3]\n\t"
    "global_load_lds_dwordx4 v98, s[44:45] sc1\n\t"
    "ds_read_b128 v[48:51], v100 offset:0\n\t"
    "ds_read_b64_tr_b16 v[64:65], v101 offset:0\n\t"
    "v_mfma_f32_16x16x32_bf16 v[4:7], v[32:35], v[20:23], v[4:7]\n\t"
    "ds_read_b64_tr_b16 v[66:67], v101 offset:1024\n\t"
    "ds_read_b128 v[52:55], v100 offset:256\n\t"
    "s_add_u32 s44, s47, 9\n\t"
    "s_and_b32 s44, s44, 15\n\t"
    "s_lshl_b32 s44, s44, 17\n\t"
    "s_add_u32 s44, s42, s44\n\t"
    "s_addc_u32 s45, s43, 0\n\t"
    "s_add_u32 m0, s46, 57344\n\t"
    "v_mfma_f32_16x16x32_bf16 v[12:15], v[36:39], v[20:23], v[12:15]\n\t"
    "global_load_lds_dwordx4 v99, s[44:45]\n\t"
    "ds_read_b64_tr_b16 v[68:69], v102 offset:0\n\t"
    "ds_read_b64_tr_b16 v[70:71], v102 offset:1024\n\t"
    "v_mfma_f32_16x16x32_bf16 v[8:11], v[36:39], v[16:19], v[8:11]\n\t"
    "ds_read_b128 v[56:59], v100 offset:2048\n\t"
    "ds_read_b64_tr_b16 v[72:73], v101 offset:8192\n\t"
    "s_add_u32 s44, s44, 0x10000\n\t"
    "s_addc_u32 s45, s45, 0\n\t"
    "s_add_u32 m0, m0, 0x2000\n\t"
    "v_mfma_f32_16x16x32_bf16 v[0:3], v[40:43], v[24:27], v[0:3]\n\t"
    "global_load_lds_dwordx4 v99, s[44:45]\n\t"
    "ds_read_b64_tr_b16 v[74:75], v101 offset:9216\n\t"
    "ds_read_b128 v[60:63], v100 offset:2304\n\t"
    "v_mfma_f32_16x16x32_bf16 v[4:7], v[40:43], v[28:31], v[4:7]\n\t"
    "ds_read_b64_tr_b16 v[76:77], v102 offset:8192\n\t"
    "ds_read_b64_tr_b16 v[78:79], v102 offset:9216\n\t"
    "v_mfma_f32_16x16x32_bf16 v[12:15], v[44:47], v[28:31], v[12:15]\n\t"
    "v_mfma_f32_16x16x32_bf16 v[8:11], v[44:47], v[24:27], v[8:11]\n\t"
    "s_waitcnt lgkmcnt(0)\n\t"
            "" : QOUT : OIN : QCLOB);
        if (tid == 384) __hip_atomic_fetch_add(cnt + (88 + xcd) * 32, 1u, __ATOMIC_RELAXED, __HIP_MEMORY_SCOPE_AGENT);
        asm volatile(
    "v_add_u32 v100, 0, v94\n\t"
    "v_add_u32 v101, 90112, v95\n\t"
    "v_xor_b32 v102, 32, v101\n\t"
    "s_waitcnt vmcnt(8) lgkmcnt(0)\n\t"
    "s_barrier\n\t"
    "s_add_u32 s44, s47, 7\n\t"
    "s_and_b32 s44, s44, 15\n\t"
    "s_lshl_b32 s44, s44, 12\n\t"
    "s_add_u32 s44, s40, s44\n\t"
    "s_addc_u32 s45, s41, 0\n\t"
    "s_add_u32 m0, s46, 73728\n\t"
    "v_mfma_f32_16x16x32_bf16 v[0:3], v[64:67], v[48:51], v[0:3]\n\t"
    "global_load_lds_dwordx4 v98, s[44:45] sc1\n\t"
    "ds_read_b128 v[16:19], v100 offset:0\n\t"
    "ds_read_b64_tr_b16 v[32:33], v101 offset:0\n\t"
    "v_mfma_f32_16x16x32_bf16 v[4:7], v[64:67], v[52:55], v[4:7]\n\t"
    "ds_read_b64_tr_b16 v[34:35], v101 offset:1024\n\t"
    "ds_read_b128 v[20:23], v100 offset:256\n\t"
    "s_add_u32 s44, s47, 10\n\t"
    "s_and_b32 s44, s44, 15\n\t"
    "s_lshl_b32 s44, s44, 17\n\t"
    "s_add_u32 s44, s42, s44\n\t"
    "s_addc_u32 s45, s43, 0\n\t"
    "s_add_u32 m0, s46, 81920\n\t"
    "v_mfma_f32_16x16x32_bf16 v[12:15], v[68:71], v[52:55], v[12:15]\n\t"
    "global_load_lds_dwordx4 v99, s[44:45]\n\t"
    "ds_read_b64_tr_b16 v[36:37], v102 offset:0\n\t"
    "ds_read_b64_tr_b16 v[38:39], v102 offset:1024\n\t"
    "v_mfma_f32_16x16x32_bf16 v[8:11], v[68:71], v[48:51], v[8:11]\n\t"
    "ds_read_b128 v[24:27], v100 offset:2048\n\t"
    "ds_read_b64_tr_b16 v[40:41], v101 offset:8192\n\t"
    "s_add_u32 s44, s44, 0x10000\n\t"
    "s_addc_u32 s45, s45, 0\n\t"
    "s_add_u32 m0, m0, 0x2000\n\t"
    "v_mfma_f32_16x16x32_bf16 v[0:3], v[72:75], v[56:59], v[0:3]\n\t"
    "global_load_lds_dwordx4 v99, s[44:45]\n\t"
    "ds_read_b64_tr_b16 v[42:43], v101 offset:9216\n\t"
    "ds_read_b128 v[28:31], v100 offset:2304\n\t"
    "v_mfma_f32_16x16x32_bf16 v[4:7], v[72:75], v[60:63], v[4:7]\n\t"
    "ds_read_b64_tr_b16 v[44:45], v102 offset:8192\n\t"
    "ds_read_b64_tr_b16 v[46:47], v102 offset:9216\n\t"
    "v_mfma_f32_16x16x32_bf16 v[12:15], v[76:79], v[60:63], v[12:15]\n\t"
    "v_mfma_f32_16x16x32_bf16 v[8:11], v[76:79], v[56:59], v[8:11]\n\t"
    "v_add_u32 v100, 24576, v94\n\t"
    "v_add_u32 v101, 106496, v95\n\t"
    "v_xor_b32 v102, 32, v101\n\t"
    "s_waitcnt vmcnt(10) lgkmcnt(0)\n\t"
    "s_barrier\n\t"
    "s_add_u32 s44, s47, 8\n\t"
    "s_and_b32 s44, s44, 15\n\t"
    "s_lshl_b32 s44, s44, 12\n\t"
    "s_add_u32 s44, s40, s44\n\t"
    "s_addc_u32 s45, s41, 0\n\t"
    "s_add_u32 m0, s46, 0\n\t"
    "v_mfma_f32_16x16x32_bf16 v[0:3], v[32:35], v[16:19], v[0:3]\n\t"
    "global_load_lds_dwordx4 v98, s[44:45] sc1\n\t"
    "ds_read_b128 v[48:51], v100 offset:0\n\t"
    "ds_read_b64_tr_b16 v[64:65], v101 offset:0\n\t"
    "v_mfma_f32_16x16x32_bf16 v[4:7], v[32:35], v[20:23], v[4:7]\n\t"
    "ds_read_b64_tr_b16 v[66:67], v101 offset:1024\n\t"
    "ds_read_b128 v[52:55], v100 offset:256\n\t"
    "s_add_u32 s44, s47, 11\n\t"
    "s_and_b32 s44, s44, 15\n\t"
    "s_lshl_b32 s44, s44, 17\n\t"
    "s_add_u32 s44, s42, s44\n\t"
    "s_addc_u32 s45, s43, 0\n\t"
    "s_add_u32 m0, s46, 98304\n\t"
    "v_mfma_f32_16x16x32_bf16 v[12:15], v[36:39], v[20:23], v[12:15]\n\t"
    "global_load_lds_dwordx4 v99, s[44:45]\n\t"
    "ds_read_b64_tr_b16 v[68:69], v102 offset:0\n\t"
    "ds_read_b64_tr_b16 v[70:71], v102 offset:1024\n\t"
    "v_mfma_f32_16x16x32_bf16 v[8:11], v[36:39], v[16:19], v[8:11]\n\t"
    "ds_read_b128 v[56:59], v100 offset:2048\n\t"
    "ds_read_b64_tr_b16 v[72:73], v101 offset:8192\n\t"
    "s_add_u32 s44, s44, 0x10000\n\t"
    "s_addc_u32 s45, s45, 0\n\t"
    "s_add_u32 m0, m0, 0x2000\n\t"
    "v_mfma_f32_16x16x32_bf16 v[0:3], v[40:43], v[24:27], v[0:3]\n\t"
    "global_load_lds_dwordx4 v99, s[44:45]\n\t"
    "ds_read_b64_tr_b16 v[74:75], v101 offset:9216\n\t"
    "ds_read_b128 v[60:63], v100 offset:2304\n\t"
    "v_mfma_f32_16x16x32_bf16 v[4:7], v[40:43], v[28:31], v[4:7]\n\t"
    "ds_read_b64_tr_b16 v[76:77], v102 offset:8192\n\t"
    "ds_read_b64_tr_b16 v[78:79], v102 offset:9216\n\t"
    "v_mfma_f32_16x16x32_bf16 v[12:15], v[44:47], v[28:31], v[12:15]\n\t"
    "v_mfma_f32_16x16x32_bf16 v[8:11], v[44:47], v[24:27], v[8:11]\n\t"
    "v_add_u32 v100, 49152, v94\n\t"
    "v_add_u32 v101, 122880, v95\n\t"
    "v_xor_b32 v102, 32, v101\n\t"
    "s_waitcnt vmcnt(8) lgkmcnt(0)\n\t"
    "s_barrier\n\t"
    "s_add_u32 s44, s47, 9\n\t"
    "s_and_b32 s44, s44, 15\n\t"
    "s_lshl_b32 s44, s44, 12\n\t"
    "s_add_u32 s44, s40, s44\n\t"
    "s_addc_u32 s45, s41, 0\n\t"
    "s_add_u32 m0, s46, 24576\n\t"
    "v_mfma_f32_16x16x32_bf16 v[0:3], v[64:67], v[48:51], v[0:3]\n\t"
    "global_load_lds_dwordx4 v98, s[44:45] sc1\n\t"
    "ds_read_b128 v[16:19], v100 offset:0\n\t"
    "ds_read_b64_tr_b16 v[32:33], v101 offset:0\n\t"
    "v_mfma_f32_16x16x32_bf16 v[4:7], v[64:67], v[52:55], v[4:7]\n\t"
    "ds_read_b64_tr_b16 v[34:35], v101 offset:1024\n\t"
    "ds_read_b128 v[20:23], v100 offset:256\n\t"
    "s_add_u32 m0, s46, 114688\n\t"
    "v_mfma_f32_16x16x32_bf16 v[12:15], v[68:71], v[52:55], v[12:15]\n\t"
    "global_load_lds_dwordx4 v103, s[48:49]\n\t"
    "ds_read_b64_tr_b16 v[36:37], v102 offset:0\n\t"
    "ds_read_b64_tr_b16 v[38:39], v102 offset:1024\n\t"
    "v_mfma_f32_16x16x32_bf16 v[8:11], v[68:71], v[48:51], v[8:11]\n\t"
    "ds_read_b128 v[24:27], v100 offset:2048\n\t"
    "ds_read_b64_tr_b16 v[40:41], v101 offset:8192\n\t"
    "s_add_u32 s44, s48, 0x10000\n\t"
    "s_addc_u32 s45, s49, 0\n\t"
    "s_add_u32 m0, s46, 122880\n\t"
    "v_mfma_f32_16x16x32_bf16 v[0:3], v[72:75], v[56:59], v[0:3]\n\t"
    "global_load_lds_dwordx4 v103, s[44:45]\n\t"
    "ds_read_b64_tr_b16 v[42:43], v101 offset:9216\n\t"
    "ds_read_b128 v[28:31], v100 offset:2304\n\t"
    "v_mfma_f32_16x16x32_bf16 v[4:7], v[72:75], v[60:63], v[4:7]\n\t"
    "ds_read_b64_tr_b16 v[44:45], v102 offset:8192\n\t"
    "ds_read_b64_tr_b16 v[46:47], v102 offset:9216\n\t"
    "v_mfma_f32_16x16x32_bf16 v[12:15], v[76:79], v[60:63], v[12:15]\n\t"
    "v_mfma_f32_16x16x32_bf16 v[8:11], v[76:79], v[56:59], v[8:11]\n\t"
    "v_add_u32 v100, 73728, v94\n\t"
    "v_add_u32 v101, 0, v95\n\t"
    "v_xor_b32 v102, 32, v101\n\t"
    "s_waitcnt vmcnt(8) lgkmcnt(0)\n\t"
    "s_barrier\n\t"
    "s_add_u32 s44, s47, 10\n\t"
    "s_and_b32 s44, s44, 15\n\t"
    "s_lshl_b32 s44, s44, 12\n\t"
    "s_add_u32 s44, s40, s44\n\t"
    "s_addc_u32 s45, s41, 0\n\t"
    "s_add_u32 m0, s46, 49152\n\t"
    "v_mfma_f32_16x16x32_bf16 v[0:3], v[32:35], v[16:19], v[0:3]\n\t"
    "global_load_lds_dwordx4 v98, s[44:45] sc1\n\t"
    "ds_read_b128 v[48:51], v100 offset:0\n\t"
    "ds_read_b64_tr_b16 v[64:65], v101 offset:0\n\t"
    "v_mfma_f32_16x16x32_bf16 v[4:7], v[32:35], v[20:23], v[4:7]\n\t"
    "ds_read_b64_tr_b16 v[66:67], v101 offset:1024\n\t"
    "ds_read_b128 v[52:55], v100 offset:256\n\t"
    "s_add_u32 s44, s47, 12\n\t"
    "s_and_b32 s44, s44, 15\n\t"
    "s_lshl_b32 s44, s44, 17\n\t"
    "s_add_u32 s44, s42, s44\n\t"
    "s_addc_u32 s45, s43, 0\n\t"
    "s_add_u32 m0, s46, 131072\n\t"
    "v_mfma_f32_16x16x32_bf16 v[12:15], v[36:39], v[20:23], v[12:15]\n\t"
    "global_load_lds_dwordx4 v99, s[44:45]\n\t"
    "ds_read_b64_tr_b16 v[68:69], v102 offset:0\n\t"
    "ds_read_b64_tr_b16 v[70:71], v102 offset:1024\n\t"
    "v_mfma_f32_16x16x32_bf16 v[8:11], v[36:39], v[16:19], v[8:11]\n\t"
    "ds_read_b128 v[56:59], v100 offset:2048\n\t"
    "ds_read_b64_tr_b16 v[72:73], v101 offset:8192\n\t"
    "s_add_u32 s44, s44, 0x10000\n\t"
    "s_addc_u32 s45, s45, 0\n\t"
    "s_add_u32 m0, m0, 0x2000\n\t"
    "v_mfma_f32_16x16x32_bf16 v[0:3], v[40:43], v[24:27], v[0:3]\n\t"
    "global_load_lds_dwordx4 v99, s[44:45]\n\t"
    "ds_read_b64_tr_b16 v[74:75], v101 offset:9216\n\t"
    "ds_read_b128 v[60:63], v100 offset:2304\n\t"
    "v_mfma_f32_16x16x32_bf16 v[4:7], v[40:43], v[28:31], v[4:7]\n\t"
    "ds_read_b64_tr_b16 v[76:77], v102 offset:8192\n\t"
    "ds_read_b64_tr_b16 v[78:79], v102 offset:9216\n\t"
    "v_mfma_f32_16x16x32_bf16 v[12:15], v[44:47], v[28:31], v[12:15]\n\t"
    "v_mfma_f32_16x16x32_bf16 v[8:11], v[44:47], v[24:27], v[8:11]\n\t"
    "v_add_u32 v100, 0, v94\n\t"
    "v_add_u32 v101, 24576, v95\n\t"
    "v_xor_b32 v102, 32, v101\n\t"
    "s_waitcnt vmcnt(8) lgkmcnt(0)\n\t"
    "s_barrier\n\t"
    "s_add_u32 s44, s47, 11\n\t"
    "s_and_b32 s44, s44, 15\n\t"
    "s_lshl_b32 s44, s44, 12\n\t"
    "s_add_u32 s44, s40, s44\n\t"
    "s_addc_u32 s45, s41, 0\n\t"
    "s_add_u32 m0, s46, 73728\n\t"
    "v_mfma_f32_16x16x32_bf16 v[0:3], v[64:67], v[48:51], v[0:3]\n\t"
    "global_load_lds_dwordx4 v98, s[44:45] sc1\n\t"
    "ds_read_b128 v[16:19], v100 offset:0\n\t"
    "ds_read_b64_tr_b16 v[32:33], v101 offset:0\n\t"
    "v_mfma_f32_16x16x32_bf16 v[4:7], v[64:67], v[52:55], v[4:7]\n\t"
    "ds_read_b64_tr_b16 v[34:35], v101 offset:1024\n\t"
    "ds_read_b128 v[20:23], v100 offset:256\n\t"
    "s_add_u32 s44, s47, 13\n\t"
    "s_and_b32 s44, s44, 15\n\t"
    "s_lshl_b32 s44, s44, 17\n\t"
    "s_add_u32 s44, s42, s44\n\t"
    "s_addc_u32 s45, s43, 0\n\t"
    "s_add_u32 m0, s46, 8192\n\t"
    "v_mfma_f32_16x16x32_bf16 v[12:15], v[68:71], v[52:55], v[12:15]\n\t"
    "global_load_lds_dwordx4 v99, s[44:45]\n\t"
    "ds_read_b64_tr_b16 v[36:37], v102 offset:0\n\t"
    "ds_read_b64_tr_b16 v[38:39], v102 offset:1024\n\t"
    "v_mfma_f32_16x16x32_bf16 v[8:11], v[68:71], v[48:51], v[8:11]\n\t"
    "ds_read_b128 v[24:27], v100 offset:2048\n\t"
    "ds_read_b64_tr_b16 v[40:41], v101 offset:8192\n\t"
    "s_add_u32 s44, s44, 0x10000\n\t"
    "s_addc_u32 s45, s45, 0\n\t"
    "s_add_u32 m0, m0, 0x2000\n\t"
    "v_mfma_f32_16x16x32_bf16 v[0:3], v[72:75], v[56:59], v[0:3]\n\t"
    "global_load_lds_dwordx4 v99, s[44:45]\n\t"
    "ds_read_b64_tr_b16 v[42:43], v101 offset:9216\n\t"
    "ds_read_b128 v[28:31], v100 offset:2304\n\t"
    "v_mfma_f32_16x16x32_bf16 v[4:7], v[72:75], v[60:63], v[4:7]\n\t"
    "ds_read_b64_tr_b16 v[44:45], v102 offset:8192\n\t"
    "ds_read_b64_tr_b16 v[46:47], v102 offset:9216\n\t"
    "v_mfma_f32_16x16x32_bf16 v[12:15], v[76:79], v[60:63], v[12:15]\n\t"
    "v_mfma_f32_16x16x32_bf16 v[8:11], v[76:79], v[56:59], v[8:11]\n\t"
    "v_add_u32 v100, 24576, v94\n\t"
    "v_add_u32 v101, 49152, v95\n\t"
    "v_xor_b32 v102, 32, v101\n\t"
    "s_waitcnt vmcnt(8) lgkmcnt(0)\n\t"
    "s_barrier\n\t"
    "s_add_u32 s44, s47, 12\n\t"
    "s_and_b32 s44, s44, 15\n\t"
    "s_lshl_b32 s44, s44, 12\n\t"
    "s_add_u32 s44, s40, s44\n\t"
    "s_addc_u32 s45, s41, 0\n\t"
    "s_add_u32 m0, s46, 0\n\t"
    "v_mfma_f32_16x16x32_bf16 v[0:3], v[32:35], v[16:19], v[0:3]\n\t"
    "global_load_lds_dwordx4 v98, s[44:45] sc1\n\t"
    "ds_read_b128 v[48:51], v100 offset:0\n\t"
    "ds_read_b64_tr_b16 v[64:65], v101 offset:0\n\t"
    "v_mfma_f32_16x16x32_bf16 v[4:7], v[32:35], v[20:23], v[4:7]\n\t"
    "ds_read_b64_tr_b16 v[66:67], v101 offset:1024\n\t"
    "ds_read_b128 v[52:55], v100 offset:256\n\t"
    "s_add_u32 s44, s47, 14\n\t"
    "s_and_b32 s44, s44, 15\n\t"
    "s_lshl_b32 s44, s44, 17\n\t"
    "s_add_u32 s44, s42, s44\n\t"
    "s_addc_u32 s45, s43, 0\n\t"
    "s_add_u32 m0, s46, 32768\n\t"
    "v_mfma_f32_16x16x32_bf16 v[12:15], v[36:39], v[20:23], v[12:15]\n\t"
    "global_load_lds_dwordx4 v99, s[44:45]\n\t"
    "ds_read_b64_tr_b16 v[68:69], v102 offset:0\n\t"
    "ds_read_b64_tr_b16 v[70:71], v102 offset:1024\n\t"
    "v_mfma_f32_16x16x32_bf16 v[8:11], v[36:39], v[16:19], v[8:11]\n\t"
    "ds_read_b128 v[56:59], v100 offset:2048\n\t"
    "ds_read_b64_tr_b16 v[72:73], v101 offset:8192\n\t"
    "s_add_u32 s44, s44, 0x10000\n\t"
    "s_addc_u32 s45, s45, 0\n\t"
    "s_add_u32 m0, m0, 0x2000\n\t"
    "v_mfma_f32_16x16x32_bf16 v[0:3], v[40:43], v[24:27], v[0:3]\n\t"
    "global_load_lds_dwordx4 v99, s[44:45]\n\t"
    "ds_read_b64_tr_b16 v[74:75], v101 offset:9216\n\t"
    "ds_read_b128 v[60:63], v100 offset:2304\n\t"
    "v_mfma_f32_16x16x32_bf16 v[4:7], v[40:43], v[28:31], v[4:7]\n\t"
    "ds_read_b64_tr_b16 v[76:77], v102 offset:8192\n\t"
    "ds_read_b64_tr_b16 v[78:79], v102 offset:9216\n\t"
    "v_mfma_f32_16x16x32_bf16 v[12:15], v[44:47], v[28:31], v[12:15]\n\t"
    "v_mfma_f32_16x16x32_bf16 v[8:11], v[44:47], v[24:27], v[8:11]\n\t"
    "v_add_u32 v100, 49152, v94\n\t"
    "v_add_u32 v101, 73728, v95\n\t"
    "v_xor_b32 v102, 32, v101\n\t"
    "s_waitcnt vmcnt(8) lgkmcnt(0)\n\t"
    "s_barrier\n\t"
    "s_add_u32 s44, s47, 13\n\t"
    "s_and_b32 s44, s44, 15\n\t"
    "s_lshl_b32 s44, s44, 12\n\t"
    "s_add_u32 s44, s40, s44\n\t"
    "s_addc_u32 s45, s41, 0\n\t"
    "s_add_u32 m0, s46, 24576\n\t"
    "v_mfma_f32_16x16x32_bf16 v[0:3], v[64:67], v[48:51], v[0:3]\n\t"
    "global_load_lds_dwordx4 v98, s[44:45] sc1\n\t"
    "ds_read_b128 v[16:19], v100 offset:0\n\t"
    "ds_read_b64_tr_b16 v[32:33], v101 offset:0\n\t"
    "v_mfma_f32_16x16x32_bf16 v[4:7], v[64:67], v[52:55], v[4:7]\n\t"
    "ds_read_b64_tr_b16 v[34:35], v101 offset:1024\n\t"
    "ds_read_b128 v[20:23], v100 offset:256\n\t"
    "s_add_u32 s44, s47, 15\n\t"
    "s_and_b32 s44, s44, 15\n\t"
    "s_lshl_b32 s44, s44, 17\n\t"
    "s_add_u32 s44, s42, s44\n\t"
    "s_addc_u32 s45, s43, 0\n\t"
    "s_add_u32 m0, s46, 57344\n\t"
    "v_mfma_f32_16x16x32_bf16 v[12:15], v[68:71], v[52:55], v[12:15]\n\t"
    "global_load_lds_dwordx4 v99, s[44:45]\n\t"
    "ds_read_b64_tr_b16 v[36:37], v102 offset:0\n\t"
    "ds_read_b64_tr_b16 v[38:39], v102 offset:1024\n\t"
    "v_mfma_f32_16x16x32_bf16 v[8:11], v[68:71], v[48:51], v[8:11]\n\t"
    "ds_read_b128 v[24:27], v100 offset:2048\n\t"
    "ds_read_b64_tr_b16 v[40:41], v101 offset:8192\n\t"
    "s_add_u32 s44, s44, 0x10000\n\t"
    "s_addc_u32 s45, s45, 0\n\t"
    "s_add_u32 m0, m0, 0x2000\n\t"
    "v_mfma_f32_16x16x32_bf16 v[0:3], v[72:75], v[56:59], v[0:3]\n\t"
    "global_load_lds_dwordx4 v99, s[44:45]\n\t"
    "ds_read_b64_tr_b16 v[42:43], v101 offset:9216\n\t"
    "ds_read_b128 v[28:31], v100 offset:2304\n\t"
    "v_mfma_f32_16x16x32_bf16 v[4:7], v[72:75], v[60:63], v[4:7]\n\t"
    "ds_read_b64_tr_b16 v[44:45], v102 offset:8192\n\t"
    "ds_read_b64_tr_b16 v[46:47], v102 offset:9216\n\t"
    "v_mfma_f32_16x16x32_bf16 v[12:15], v[76:79], v[60:63], v[12:15]\n\t"
    "v_mfma_f32_16x16x32_bf16 v[8:11], v[76:79], v[56:59], v[8:11]\n\t"
    "v_add_u32 v100, 73728, v94\n\t"
    "v_add_u32 v101, 90112, v95\n\t"
    "v_xor_b32 v102, 32, v101\n\t"
    "s_waitcnt vmcnt(8) lgkmcnt(0)\n\t"
    "s_barrier\n\t"
    "s_add_u32 s44, s47, 14\n\t"
    "s_and_b32 s44, s44, 15\n\t"
    "s_lshl_b32 s44, s44, 12\n\t"
    "s_add_u32 s44, s40, s44\n\t"
    "s_addc_u32 s45, s41, 0\n\t"
    "s_add_u32 m0, s46, 49152\n\t"
    "v_mfma_f32_16x16x32_bf16 v[0:3], v[32:35], v[16:19], v[0:3]\n\t"
    "global_load_lds_dwordx4 v98, s[44:45] sc1\n\t"
    "ds_read_b128 v[48:51], v100 offset:0\n\t"
    "ds_read_b64_tr_b16 v[64:65], v101 offset:0\n\t"
    "v_mfma_f32_16x16x32_bf16 v[4:7], v[32:35], v[20:23], v[4:7]\n\t"
    "ds_read_b64_tr_b16 v[66:67], v101 offset:1024\n\t"
    "ds_read_b128 v[52:55], v100 offset:256\n\t"
    "v_mfma_f32_16x16x32_bf16 v[12:15], v[36:39], v[20:23], v[12:15]\n\t"
    "ds_read_b64_tr_b16 v[68:69], v102 offset:0\n\t"
    "ds_read_b64_tr_b16 v[70:71], v102 offset:1024\n\t"
    "v_mfma_f32_16x16x32_bf16 v[8:11], v[36:39], v[16:19], v[8:11]\n\t"
    "ds_read_b128 v[56:59], v100 offset:2048\n\t"
    "ds_read_b64_tr_b16 v[72:73], v101 offset:8192\n\t"
    "v_mfma_f32_16x16x32_bf16 v[0:3], v[40:43], v[24:27], v[0:3]\n\t"
    "ds_read_b64_tr_b16 v[74:75], v101 offset:9216\n\t"
    "ds_read_b128 v[60:63], v100 offset:2304\n\t"
    "v_mfma_f32_16x16x32_bf16 v[4:7], v[40:43], v[28:31], v[4:7]\n\t"
    "ds_read_b64_tr_b16 v[76:77], v102 offset:8192\n\t"
    "ds_read_b64_tr_b16 v[78:79], v102 offset:9216\n\t"
    "v_mfma_f32_16x16x32_bf16 v[12:15], v[44:47], v[28:31], v[12:15]\n\t"
    "v_mfma_f32_16x16x32_bf16 v[8:11], v[44:47], v[24:27], v[8:11]\n\t"
    "v_add_u32 v100, 0, v94\n\t"
    "v_add_u32 v101, 122880, v95\n\t"
    "v_xor_b32 v102, 32, v101\n\t"
    "s_waitcnt vmcnt(6) lgkmcnt(0)\n\t"
    "s_barrier\n\t"
    "s_add_u32 s44, s47, 15\n\t"
    "s_and_b32 s44, s44, 15\n\t"
    "s_lshl_b32 s44, s44, 12\n\t"
    "s_add_u32 s44, s40, s44\n\t"
    "s_addc_u32 s45, s41, 0\n\t"
    "s_add_u32 m0, s46, 73728\n\t"
    "v_mfma_f32_16x16x32_bf16 v[0:3], v[64:67], v[48:51], v[0:3]\n\t"
    "global_load_lds_dwordx4 v98, s[44:45] sc1\n\t"
    "ds_read_b128 v[16:19], v100 offset:0\n\t"
    "ds_read_b64_tr_b16 v[32:33], v101 offset:0\n\t"
    "v_mfma_f32_16x16x32_bf16 v[4:7], v[64:67], v[52:55], v[4:7]\n\t"
    "ds_read_b64_tr_b16 v[34:35], v101 offset:1024\n\t"
    "ds_read_b128 v[20:23], v100 offset:256\n\t"
    "v_mfma_f32_16x16x32_bf16 v[12:15], v[68:71], v[52:55], v[12:15]\n\t"
    "ds_read_b64_tr_b16 v[36:37], v102 offset:0\n\t"
    "ds_read_b64_tr_b16 v[38:39], v102 offset:1024\n\t"
    "v_mfma_f32_16x16x32_bf16 v[8:11], v[68:71], v[48:51], v[8:11]\n\t"
    "ds_read_b128 v[24:27], v100 offset:2048\n\t"
    "ds_read_b64_tr_b16 v[40:41], v101 offset:8192\n\t"
    "v_mfma_f32_16x16x32_bf16 v[0:3], v[72:75], v[56:59], v[0:3]\n\t"
    "ds_read_b64_tr_b16 v[42:43], v101 offset:9216\n\t"
    "ds_read_b128 v[28:31], v100 offset:2304\n\t"
    "v_mfma_f32_16x16x32_bf16 v[4:7], v[72:75], v[60:63], v[4:7]\n\t"
    "ds_read_b64_tr_b16 v[44:45], v102 offset:8192\n\t"
    "ds_read_b64_tr_b16 v[46:47], v102 offset:9216\n\t"
    "v_mfma_f32_16x16x32_bf16 v[12:15], v[76:79], v[60:63], v[12:15]\n\t"
    "v_mfma_f32_16x16x32_bf16 v[8:11], v[76:79], v[56:59], v[8:11]\n\t"
    "v_add_u32 v100, 24576, v94\n\t"
    "v_add_u32 v101, 0, v95\n\t"
    "v_xor_b32 v102, 32, v101\n\t"
    "s_waitcnt vmcnt(4) lgkmcnt(0)\n\t"
    "s_barrier\n\t"
    "v_mfma_f32_16x16x32_bf16 v[0:3], v[32:35], v[16:19], v[0:3]\n\t"
    "ds_read_b128 v[48:51], v100 offset:0\n\t"
    "ds_read_b64_tr_b16 v[64:65], v101 offset:0\n\t"
    "v_mfma_f32_16x16x32_bf16 v[4:7], v[32:35], v[20:23], v[4:7]\n\t"
    "ds_read_b64_tr_b16 v[66:67], v101 offset:1024\n\t"
    "ds_read_b128 v[52:55], v100 offset:256\n\t"
    "v_mfma_f32_16x16x32_bf16 v[12:15], v[36:39], v[20:23], v[12:15]\n\t"
    "ds_read_b64_tr_b16 v[68:69], v102 offset:0\n\t"
    "ds_read_b64_tr_b16 v[70:71], v102 offset:1024\n\t"
    "v_mfma_f32_16x16x32_bf16 v[8:11], v[36:39], v[16:19], v[8:11]\n\t"
    "ds_read_b128 v[56:59], v100 offset:2048\n\t"
    "ds_read_b64_tr_b16 v[72:73], v101 offset:8192\n\t"
    "v_mfma_f32_16x16x32_bf16 v[0:3], v[40:43], v[24:27], v[0:3]\n\t"
    "ds_read_b64_tr_b16 v[74:75], v101 offset:9216\n\t"
    "ds_read_b128 v[60:63], v100 offset:2304\n\t"
    "v_mfma_f32_16x16x32_bf16 v[4:7], v[40:43], v[28:31], v[4:7]\n\t"
    "ds_read_b64_tr_b16 v[76:77], v102 offset:8192\n\t"
    "ds_read_b64_tr_b16 v[78:79], v102 offset:9216\n\t"
    "v_mfma_f32_16x16x32_bf16 v[12:15], v[44:47], v[28:31], v[12:15]\n\t"
    "v_mfma_f32_16x16x32_bf16 v[8:11], v[44:47], v[24:27], v[8:11]\n\t"
    "v_add_u32 v100, 49152, v94\n\t"
    "v_add_u32 v101, 24576, v95\n\t"
    "v_xor_b32 v102, 32, v101\n\t"
    "s_waitcnt vmcnt(1) lgkmcnt(0)\n\t"
    "s_barrier\n\t"
    "v_mfma_f32_16x16x32_bf16 v[0:3], v[64:67], v[48:51], v[0:3]\n\t"
    "ds_read_b128 v[16:19], v100 offset:0\n\t"
    "ds_read_b64_tr_b16 v[32:33], v101 offset:0\n\t"
    "v_mfma_f32_16x16x32_bf16 v[4:7], v[64:67], v[52:55], v[4:7]\n\t"
    "ds_read_b64_tr_b16 v[34:35], v101 offset:1024\n\t"
    "ds_read_b128 v[20:23], v100 offset:256\n\t"
    "v_mfma_f32_16x16x32_bf16 v[12:15], v[68:71], v[52:55], v[12:15]\n\t"
    "ds_read_b64_tr_b16 v[36:37], v102 offset:0\n\t"
    "ds_read_b64_tr_b16 v[38:39], v102 offset:1024\n\t"
    "v_mfma_f32_16x16x32_bf16 v[8:11], v[68:71], v[48:51], v[8:11]\n\t"
    "ds_read_b128 v[24:27], v100 offset:2048\n\t"
    "ds_read_b64_tr_b16 v[40:41], v101 offset:8192\n\t"
    "v_mfma_f32_16x16x32_bf16 v[0:3], v[72:75], v[56:59], v[0:3]\n\t"
    "ds_read_b64_tr_b16 v[42:43], v101 offset:9216\n\t"
    "ds_read_b128 v[28:31], v100 offset:2304\n\t"
    "v_mfma_f32_16x16x32_bf16 v[4:7], v[72:75], v[60:63], v[4:7]\n\t"
    "ds_read_b64_tr_b16 v[44:45], v102 offset:8192\n\t"
    "ds_read_b64_tr_b16 v[46:47], v102 offset:9216\n\t"
    "v_mfma_f32_16x16x32_bf16 v[12:15], v[76:79], v[60:63], v[12:15]\n\t"
    "v_mfma_f32_16x16x32_bf16 v[8:11], v[76:79], v[56:59], v[8:11]\n\t"
    "v_add_u32 v100, 73728, v94\n\t"
    "v_add_u32 v101, 49152, v95\n\t"
    "v_xor_b32 v102, 32, v101\n\t"
    "s_waitcnt vmcnt(0) lgkmcnt(0)\n\t"
    "s_barrier\n\t"
    "v_mfma_f32_16x16x32_bf16 v[0:3], v[32:35], v[16:19], v[0:3]\n\t"
    "ds_read_b128 v[48:51], v100 offset:0\n\t"
    "ds_read_b64_tr_b16 v[64:65], v101 offset:0\n\t"
    "v_mfma_f32_16x16x32_bf16 v[4:7], v[32:35], v[20:23], v[4:7]\n\t"
    "ds_read_b64_tr_b16 v[66:67], v101 offset:1024\n\t"
    "ds_read_b128 v[52:55], v100 offset:256\n\t"
    "v_mfma_f32_16x16x32_bf16 v[12:15], v[36:39], v[20:23], v[12:15]\n\t"
    "ds_read_b64_tr_b16 v[68:69], v102 offset:0\n\t"
    "ds_read_b64_tr_b16 v[70:71], v102 offset:1024\n\t"
    "v_mfma_f32_16x16x32_bf16 v[8:11], v[36:39], v[16:19], v[8:11]\n\t"
    "ds_read_b128 v[56:59], v100 offset:2048\n\t"
    "ds_read_b64_tr_b16 v[72:73], v101 offset:8192\n\t"
    "v_mfma_f32_16x16x32_bf16 v[0:3], v[40:43], v[24:27], v[0:3]\n\t"
    "ds_read_b64_tr_b16 v[74:75], v101 offset:9216\n\t"
    "ds_read_b128 v[60:63], v100 offset:2304\n\t"
    "v_mfma_f32_16x16x32_bf16 v[4:7], v[40:43], v[28:31], v[4:7]\n\t"
    "ds_read_b64_tr_b16 v[76:77], v102 offset:8192\n\t"
    "ds_read_b64_tr_b16 v[78:79], v102 offset:9216\n\t"
    "v_mfma_f32_16x16x32_bf16 v[12:15], v[44:47], v[28:31], v[12:15]\n\t"
    "v_mfma_f32_16x16x32_bf16 v[8:11], v[44:47], v[24:27], v[8:11]\n\t"
    "s_waitcnt lgkmcnt(0)\n\t"
    "v_mfma_f32_16x16x32_bf16 v[0:3], v[64:67], v[48:51], v[0:3]\n\t"
    "v_mfma_f32_16x16x32_bf16 v[4:7], v[64:67], v[52:55], v[4:7]\n\t"
    "v_mfma_f32_16x16x32_bf16 v[12:15], v[68:71], v[52:55], v[12:15]\n\t"
    "v_mfma_f32_16x16x32_bf16 v[8:11], v[68:71], v[48:51], v[8:11]\n\t"
    "v_mfma_f32_16x16x32_bf16 v[0:3], v[72:75], v[56:59], v[0:3]\n\t"
    "v_mfma_f32_16x16x32_bf16 v[4:7], v[72:75], v[60:63], v[4:7]\n\t"
    "v_mfma_f32_16x16x32_bf16 v[12:15], v[76:79], v[60:63], v[12:15]\n\t"
    "v_mfma_f32_16x16x32_bf16 v[8:11], v[76:79], v[56:59], v[8:11]\n\t"
    "s_nop 7\n\t"
            "" : QOUT : OIN, "{v103}"((unsigned)xr_ * 2048 + (unsigned)((li ^ (xr_ & 15)) * 16)), "{s[48:49]}"(x + (size_t)tg * 64 * 2048 + ng * 256) : QCLOB);
    }
    if (wid == 6) asm volatile("s_mov_b32 m0, %2\n\ts_nop 0\n\tglobal_load_lds_dword %0, %1 sc1" ::"v"((lane & 7) * 128), "s"(cnt + 88 * 32), "s"(pollw) : "memory");
    const int rbk = wid & 1, cb = wid >> 1;
    f32x4 xo[4];
    u32x4 x1v[2];
#pragma unroll
    for (int a = 0; a < 2; ++a) {
        const f32x4 ga = ldsf4(sta + 2048 + wid * 256 + a * 64 + lg * 16);
#pragma unroll
        for (int t = 0; t < 2; ++t) {
            const u32x2 xv = *(const LAS u32x2*)(size_t)(xt + (rbk * 32 + t * 16 + li) * 256 + (((cb * 4 + a * 2 + (lg >> 1)) ^ li) * 16) + (lg & 1) * 8);
            const int r = 8 * a + 4 * t;
            const unsigned p = cvtpk(bflo(xv[0]) + ga[0] * oa[r], bfhi(xv[0]) + ga[1] * oa[r + 1]), q = cvtpk(bflo(xv[1]) + ga[2] * oa[r + 2], bfhi(xv[1]) + ga[3] * oa[r + 3]);
            x1v[a][2 * t] = p; x1v[a][2 * t + 1] = q;
            xo[2 * a + t] = f32x4{bflo(p), bfhi(p), bflo(q), bfhi(q)};
        }
    }
    LAS f32x2* st = (LAS f32x2*)(size_t)sta;
#pragma unroll
    for (int t = 0; t < 2; ++t) {
        const f32x4 s4 = xo[t] + xo[2 + t];
        const float mw = swapadd(swap16add((s4[0] + s4[1]) + (s4[2] + s4[3]))) * (1.f / 32.f);
        const f32x4 d0 = xo[t] - mw, d1 = xo[2 + t] - mw, q4 = d0 * d0 + d1 * d1;
        const float q = swapadd(swap16add((q4[0] + q4[1]) + (q4[2] + q4[3])));
        if (lg == 0) st[(rbk * 32 + t * 16 + li) * 4 + cb] = f32x2{mw, q};
    }
    const int grb = tg * 2 + rbk, gcb = ng * 4 + cb;
    const __amdgpu_buffer_rsrc_t x1rs = mkrs(ws + WS_X1 + ((size_t)grb * 64 + gcb * 2) * 1024, 2048), yrs = mkrs(ws + WS_Y + (size_t)grb * 65536 + gcb * 1024, 33792);
    {
        const f32x4 c0 = ldsf4(sta + 2048 + wid * 256 + 128 + lg * 16), c1 = ldsf4(sta + 2048 + wid * 256 + 192 + lg * 16);
#pragma unroll
        for (int t = 0; t < 2; ++t) { const f32x4 y0 = xo[t] * c0, y1 = xo[2 + t] * c1; st16wt(yrs, l16, t * 32768, u32x4{cvtpk(y0[0], y0[1]), cvtpk(y0[2], y0[3]), cvtpk(y1[0], y1[1]), cvtpk(y1[2], y1[3])}); }
    }
    st16wt(x1rs, l16, 0, x1v[0]); st16wt(x1rs, l16, 1024, x1v[1]);
    WAITL0(); BAR();
    if (tid < 64) {
        const f32x4 a = *(LAS f32x4*)(st + tid * 4), b = *(LAS f32x4*)(st + tid * 4 + 2);
        const float mean = 0.25f * ((a[0] + a[2]) + (b[0] + b[2]));
        const float d0 = a[0] - mean, d1 = a[2] - mean, d2 = b[0] - mean, d3 = b[2] - mean;
        const f32x2 sv = {mean, (a[1] + a[3]) + (b[1] + b[3]) + 32.f * ((d0 * d0 + d1 * d1) + (d2 * d2 + d3 * d3))};
        __hip_atomic_store((GAS unsigned long long*)(ws + WS_STAT) + ng * 2048 + tg * 64 + tid, __builtin_bit_cast(unsigned long long, sv), __ATOMIC_RELAXED, __HIP_MEMORY_SCOPE_AGENT);
    }
    auto side_ok = [&]() { return __all(__hip_atomic_load(cnt + (88 + (lane & 7)) * 32, __ATOMIC_RELAXED, __HIP_MEMORY_SCOPE_AGENT) == 32u); };
    WAITL0(); WAITV(0); BAR();
    if (tid >= 448 && tid < 456) __hip_atomic_store(cnt + (192 + (tg >> 2) * 8 + lane) * 32 + idx, 1u, __ATOMIC_RELAXED, __HIP_MEMORY_SCOPE_AGENT);
    const char* xs1 = xs0 + 32768; const char* ws1 = ws0 + (wid == 6 ? 2048 : 1024); const char* es1 = es0 + 1024;
    unsigned w2v = w2, wvov = gwvo, dwv = dw, wmv = wmask;
    auto wfirst = [&]() {
#pragma unroll
        for (int i = 0; i < 2; ++i) { glds16c(ws0 + i * wstep, gwvo, dw + ((i * K5_SLOT) & wmask)); glds16c(ws1 + i * wstep, gwvo, dw + ((i * K5_SLOT) & wmask) + w2); }
    };
    const bool early = __all(*(const LAS unsigned*)(size_t)(pollw + lane * 4) == 32u);
    if (early) wfirst();
    else if (wid == 6) {
#pragma nounroll
        for (unsigned spins = 0; spins < SPIN_MAX && !side_ok(); ++spins) __builtin_amdgcn_s_sleep(POLL_SLEEP);
    }
    if (wid == 7) {
#pragma nounroll
        for (unsigned spins = 0; spins < SPIN_MAX; ++spins) {
            if (__all(__hip_atomic_load(cnt + (192 + mt * 8 + xcd) * 32 + (lane & 31), __ATOMIC_RELAXED, __HIP_MEMORY_SCOPE_AGENT) == 1u)) break;
            __builtin_amdgcn_s_sleep(POLL_SLEEP);
        }
    }
    BAR();
    if (!early) wfirst();
    ws0 += 2 * wstep; ws1 += 2 * wstep;
#pragma unroll
    for (int i = 0; i < 2; ++i) { glds16c(xs0, xvo, dx + i * K5_SLOT); glds16c(xs1, xvo, dx + i * K5_SLOT + 1024); xs0 += 1024; xs1 += 1024; }
    asm volatile(
    "s_mov_b32 s51, 61440\n\t"
    "s_waitcnt vmcnt(2)\n\t"
    "s_barrier\n\t"
    "ds_read_b128 v[112:115], v201 offset:0\n\t"
    "ds_read_b128 v[116:119], v201 offset:1024\n\t"
    "ds_read_b128 v[120:123], v201 offset:2048\n\t"
    "ds_read_b128 v[124:127], v201 offset:3072\n\t"
    "ds_read_b128 v[128:131], v202 offset:0\n\t"
    "ds_read_b128 v[132:135], v202 offset:1024\n\t"
    "ds_read_b128 v[136:139], v202 offset:2048\n\t"
    "ds_read_b128 v[140:143], v202 offset:3072\n\t"
    "ds_read_b128 v[144:147], v202 offset:4096\n\t"
    "ds_read_b128 v[148:151], v202 offset:5120\n\t"
    "ds_read_b128 v[152:155], v202 offset:6144\n\t"
    "s_add_u32 m0, s51, s48\n\t"
    "s_nop 0\n\t"
    "global_load_lds_dwordx4 v200, s[40:41] sc1\n\t"
    "s_add_u32 s40, s40, 0x400\n\t"
    "s_addc_u32 s41, s41, 0\n\t"
    "s_nop 0\n\t"
    "s_add_u32 m0, m0, 0x400\n\t"
    "s_nop 0\n\t"
    "global_load_lds_dwordx4 v200, s[42:43] sc1\n\t"
    "s_add_u32 s42, s42, 0x400\n\t"
    "s_addc_u32 s43, s43, 0\n\t"
    "s_nop 0\n\t"
    "s_and_b32 s62, s51, s55\n\t"
    "s_add_u32 m0, s62, s49\n\t"
    "s_nop 0\n\t"
    "global_load_lds_dwordx4 v205, s[44:45] sc1\n\t"
    "s_add_u32 s44, s44, s53\n\t"
    "s_addc_u32 s45, s45, 0\n\t"
    "s_nop 0\n\t"
    "s_add_u32 m0, m0, s54\n\t"
    "s_nop 0\n\t"
    "global_load_lds_dwordx4 v205, s[46:47] sc1\n\t"
    "s_add_u32 s46, s46, s53\n\t"
    "s_addc_u32 s47, s47, 0\n\t"
    "s_nop 0\n\t"
    "s_mov_b32 s50, 30720\n\t"
    "s_mov_b32 s51, 92160\n\t"
    "s_mov_b32 s52, 13\n\t"
    "v_add_u32 v203, s50, v201\n\t"
    "v_add_u32 v204, s50, v202\n\t"
    "s_waitcnt lgkmcnt(0)\n\t"
    "v_mfma_f32_16x16x32_bf16 v[0:3], v[128:131], v[112:115], 0\n\t"
    "v_mfma_f32_16x16x32_bf16 v[16:19], v[132:135], v[112:115], 0\n\t"
    "v_mfma_f32_16x16x32_bf16 v[32:35], v[136:139], v[112:115], 0\n\t"
    "v_mfma_f32_16x16x32_bf16 v[48:51], v[140:143], v[112:115], 0\n\t"
    "v_mfma_f32_16x16x32_bf16 v[64:67], v[144:147], v[112:115], 0\n\t"
    "v_mfma_f32_16x16x32_bf16 v[80:83], v[148:151], v[112:115], 0\n\t"
    "v_mfma_f32_16x16x32_bf16 v[96:99], v[152:155], v[112:115], 0\n\t"
    "v_mfma_f32_16x16x32_bf16 v[100:103], v[152:155], v[116:119], 0\n\t"
    "v_mfma_f32_16x16x32_bf16 v[84:87], v[148:151], v[116:119], 0\n\t"
    "v_mfma_f32_16x16x32_bf16 v[68:71], v[144:147], v[116:119], 0\n\t"
    "s_waitcnt vmcnt(4)\n\t"
    "s_barrier\n\t"
    "v_mfma_f32_16x16x32_bf16 v[52:55], v[140:143], v[116:119], 0\n\t"
    "ds_read_b128 v[172:175], v204\n\t"
    "ds_read_b128 v[156:159], v203 offset:0\n\t"
    "v_mfma_f32_16x16x32_bf16 v[36:39], v[136:139], v[116:119], 0\n\t"
    "ds_read_b128 v[160:163], v203 offset:1024\n\t"
    "ds_read_b128 v[164:167], v203 offset:2048\n\t"
    "v_mfma_f32_16x16x32_bf16 v[20:23], v[132:135], v[116:119], 0\n\t"
    "ds_read_b128 v[168:171], v203 offset:3072\n\t"
    "ds_read_b128 v[176:179], v204 offset:1024\n\t"
    "v_mfma_f32_16x16x32_bf16 v[4:7], v[128:131], v[116:119], 0\n\t"
    "ds_read_b128 v[180:183], v204 offset:2048\n\t"
    "ds_read_b128 v[184:187], v204 offset:3072\n\t"
    "v_mfma_f32_16x16x32_bf16 v[8:11], v[128:131], v[120:123], 0\n\t"
    "ds_read_b128 v[188:191], v204 offset:4096\n\t"
    "ds_read_b128 v[192:195], v204 offset:5120\n\t"
    "v_mfma_f32_16x16x32_bf16 v[24:27], v[132:135], v[120:123], 0\n\t"
    "ds_read_b128 v[196:199], v204 offset:6144\n\t"
    "v_mfma_f32_16x16x32_bf16 v[40:43], v[136:139], v[120:123], 0\n\t"
    "s_add_u32 m0, s51, s48\n\t"
    "v_mfma_f32_16x16x32_bf16 v[56:59], v[140:143], v[120:123], 0\n\t"
    "global_load_lds_dwordx4 v200, s[40:41] sc1\n\t"
    "s_add_u32 s40, s40, 0x400\n\t"
    "s_addc_u32 s41, s41, 0\n\t"
    "v_mfma_f32_16x16x32_bf16 v[72:75], v[144:147], v[120:123], 0\n\t"
    "s_add_u32 m0, m0, 0x400\n\t"
    "v_mfma_f32_16x16x32_bf16 v[88:91], v[148:151], v[120:123], 0\n\t"
    "global_load_lds_dwordx4 v200, s[42:43] sc1\n\t"
    "s_add_u32 s42, s42, 0x400\n\t"
    "s_addc_u32 s43, s43, 0\n\t"
    "v_mfma_f32_16x16x32_bf16 v[104:107], v[152:155], v[120:123], 0\n\t"
    "s_and_b32 s62, s51, s55\n\t"
    "s_add_u32 m0, s62, s49\n\t"
    "v_mfma_f32_16x16x32_bf16 v[108:111], v[152:155], v[124:127], 0\n\t"
    "global_load_lds_dwordx4 v205, s[44:45] sc1\n\t"
    "s_add_u32 s44, s44, s53\n\t"
    "s_addc_u32 s45, s45, 0\n\t"
    "v_mfma_f32_16x16x32_bf16 v[92:95], v[148:151], v[124:127], 0\n\t"
    "s_add_u32 m0, m0, s54\n\t"
    "v_mfma_f32_16x16x32_bf16 v[76:79], v[144:147], v[124:127], 0\n\t"
    "global_load_lds_dwordx4 v205, s[46:47] sc1\n\t"
    "s_add_u32 s46, s46, s53\n\t"
    "s_addc_u32 s47, s47, 0\n\t"
    "v_mfma_f32_16x16x32_bf16 v[60:63], v[140:143], v[124:127], 0\n\t"
    "s_add_u32 s50, s50, 30720\n\t"
    "s_cmp_eq_u32 s50, 153600\n\t"
    "s_cselect_b32 s50, 0, s50\n\t"
    "v_mfma_f32_16x16x32_bf16 v[44:47], v[136:139], v[124:127], 0\n\t"
    "s_add_u32 s51, s51, 30720\n\t"
    "s_cmp_eq_u32 s51, 153600\n\t"
    "s_cselect_b32 s51, 0, s51\n\t"
    "v_mfma_f32_16x16x32_bf16 v[28:31], v[132:135], v[124:127], 0\n\t"
    "v_mfma_f32_16x16x32_bf16 v[12:15], v[128:131], v[124:127], 0\n\t"
    "v_add_u32 v203, s50, v201\n\t"
    "v_add_u32 v204, s50, v202\n\t"
    "s_waitcnt vmcnt(4) lgkmcnt(0)\n\t"
    "s_barrier\n\t"
    "v_mfma_f32_16x16x32_bf16 v[0:3], v[172:175], v[156:159], v[0:3]\n\t"
    "ds_read_b128 v[128:131], v204\n\t"
    "v_mfma_f32_16x16x32_bf16 v[16:19], v[176:179], v[156:159], v[16:19]\n\t"
    "ds_read_b128 v[112:115], v203 offset:0\n\t"
    "v_mfma_f32_16x16x32_bf16 v[32:35], v[180:183], v[156:159], v[32:35]\n\t"
    "ds_read_b128 v[116:119], v203 offset:1024\n\t"
    "v_mfma_f32_16x16x32_bf16 v[48:51], v[184:187], v[156:159], v[48:51]\n\t"
    "ds_read_b128 v[120:123], v203 offset:2048\n\t"
    "v_mfma_f32_16x16x32_bf16 v[64:67], v[188:191], v[156:159], v[64:67]\n\t"
    "ds_read_b128 v[124:127], v203 offset:3072\n\t"
    "v_mfma_f32_16x16x32_bf16 v[80:83], v[192:195], v[156:159], v[80:83]\n\t"
    "ds_read_b128 v[132:135], v204 offset:1024\n\t"
    "v_mfma_f32_16x16x32_bf16 v[96:99], v[196:199], v[156:159], v[96:99]\n\t"
    "ds_read_b128 v[136:139], v204 offset:2048\n\t"
    "v_mfma_f32_16x16x32_bf16 v[100:103], v[196:199], v[160:163], v[100:103]\n\t"
    "ds_read_b128 v[140:143], v204 offset:3072\n\t"
    "v_mfma_f32_16x16x32_bf16 v[84:87], v[192:195], v[160:163], v[84:87]\n\t"
    "ds_read_b128 v[144:147], v204 offset:4096\n\t"
    "v_mfma_f32_16x16x32_bf16 v[68:71], v[188:191], v[160:163], v[68:71]\n\t"
    "ds_read_b128 v[148:151], v204 offset:5120\n\t"
    "v_mfma_f32_16x16x32_bf16 v[52:55], v[184:187], v[160:163], v[52:55]\n\t"
    "ds_read_b128 v[152:155], v204 offset:6144\n\t"
    "v_mfma_f32_16x16x32_bf16 v[36:39], v[180:183], v[160:163], v[36:39]\n\t"
    "s_add_u32 m0, s51, s48\n\t"
    "v_mfma_f32_16x16x32_bf16 v[20:23], v[176:179], v[160:163], v[20:23]\n\t"
    "global_load_lds_dwordx4 v200, s[40:41] sc1\n\t"
    "s_add_u32 s40, s40, 0x400\n\t"
    "s_addc_u32 s41, s41, 0\n\t"
    "v_mfma_f32_16x16x32_bf16 v[4:7], v[172:175], v[160:163], v[4:7]\n\t"
    "s_add_u32 m0, m0, 0x400\n\t"
    "v_mfma_f32_16x16x32_bf16 v[8:11], v[172:175], v[164:167], v[8:11]\n\t"
    "global_load_lds_dwordx4 v200, s[42:43] sc1\n\t"
    "s_add_u32 s42, s42, 0x400\n\t"
    "s_addc_u32 s43, s43, 0\n\t"
    "v_mfma_f32_16x16x32_bf16 v[24:27], v[176:179], v[164:167], v[24:27]\n\t"
    "s_and_b32 s62, s51, s55\n\t"
    "s_add_u32 m0, s62, s49\n\t"
    "v_mfma_f32_16x16x32_bf16 v[40:43], v[180:183], v[164:167], v[40:43]\n\t"
    "global_load_lds_dwordx4 v205, s[44:45] sc1\n\t"
    "s_add_u32 s44, s44, s53\n\t"
    "s_addc_u32 s45, s45, 0\n\t"
    "v_mfma_f32_16x16x32_bf16 v[56:59], v[184:187], v[164:167], v[56:59]\n\t"
    "s_add_u32 m0, m0, s54\n\t"
    "v_mfma_f32_16x16x32_bf16 v[72:75], v[188:191], v[164:167], v[72:75]\n\t"
    "global_load_lds_dwordx4 v205, s[46:47] sc1\n\t"
    "s_add_u32 s46, s46, s53\n\t"
    "s_addc_u32 s47, s47, 0\n\t"
    "v_mfma_f32_16x16x32_bf16 v[88:91], v[192:195], v[164:167], v[88:91]\n\t"
    "s_add_u32 s50, s50, 30720\n\t"
    "s_cmp_eq_u32 s50, 153600\n\t"
    "s_cselect_b32 s50, 0, s50\n\t"
    "v_mfma_f32_16x16x32_bf16 v[104:107], v[196:199], v[164:167], v[104:107]\n\t"
    "s_add_u32 s51, s51, 30720\n\t"
    "s_cmp_eq_u32 s51, 153600\n\t"
    "s_cselect_b32 s51, 0, s51\n\t"
    "v_mfma_f32_16x16x32_bf16 v[108:111], v[196:199], v[168:171], v[108:111]\n\t"
    "v_mfma_f32_16x16x32_bf16 v[92:95], v[192:195], v[168:171], v[92:95]\n\t"
    "v_mfma_f32_16x16x32_bf16 v[76:79], v[188:191], v[168:171], v[76:79]\n\t"
    "v_mfma_f32_16x16x32_bf16 v[60:63], v[184:187], v[168:171], v[60:63]\n\t"
    "v_mfma_f32_16x16x32_bf16 v[44:47], v[180:183], v[168:171], v[44:47]\n\t"
    "v_mfma_f32_16x16x32_bf16 v[28:31], v[176:179], v[168:171], v[28:31]\n\t"
    "v_mfma_f32_16x16x32_bf16 v[12:15], v[172:175], v[168:171], v[12:15]\n\t"
    "1:\n\t"
    "v_add_u32 v203, s50, v201\n\t"
    "v_add_u32 v204, s50, v202\n\t"
    "s_waitcnt vmcnt(4) lgkmcnt(0)\n\t"
    "s_barrier\n\t"
    "v_mfma_f32_16x16x32_bf16 v[0:3], v[128:131], v[112:115], v[0:3]\n\t"
    "ds_read_b128 v[172:175], v204\n\t"
    "v_mfma_f32_16x16x32_bf16 v[16:19], v[132:135], v[112:115], v[16:19]\n\t"
    "ds_read_b128 v[156:159], v203 offset:0\n\t"
    "v_mfma_f32_16x16x32_bf16 v[32:35], v[136:139], v[112:115], v[32:35]\n\t"
    "ds_read_b128 v[160:163], v203 offset:1024\n\t"
    "v_mfma_f32_16x16x32_bf16 v[48:51], v[140:143], v[112:115], v[48:51]\n\t"
    "ds_read_b128 v[164:167], v203 offset:2048\n\t"
    "v_mfma_f32_16x16x32_bf16 v[64:67], v[144:147], v[112:115], v[64:67]\n\t"
    "ds_read_b128 v[168:171], v203 offset:3072\n\t"
    "v_mfma_f32_16x16x32_bf16 v[80:83], v[148:151], v[112:115], v[80:83]\n\t"
    "ds_read_b128 v[176:179], v204 offset:1024\n\t"
    "v_mfma_f32_16x16x32_bf16 v[96:99], v[152:155], v[112:115], v[96:99]\n\t"
    "ds_read_b128 v[180:183], v204 offset:2048\n\t"
    "v_mfma_f32_16x16x32_bf16 v[100:103], v[152:155], v[116:119], v[100:103]\n\t"
    "ds_read_b128 v[184:187], v204 offset:3072\n\t"
    "v_mfma_f32_16x16x32_bf16 v[84:87], v[148:151], v[116:119], v[84:87]\n\t"
    "ds_read_b128 v[188:191], v204 offset:4096\n\t"
    "v_mfma_f32_16x16x32_bf16 v[68:71], v[144:147], v[116:119], v[68:71]\n\t"
    "ds_read_b128 v[192:195], v204 offset:5120\n\t"
    "v_mfma_f32_16x16x32_bf16 v[52:55], v[140:143], v[116:119], v[52:55]\n\t"
    "ds_read_b128 v[196:199], v204 offset:6144\n\t"
    "v_mfma_f32_16x16x32_bf16 v[36:39], v[136:139], v[116:119], v[36:39]\n\t"
    "s_add_u32 m0, s51, s48\n\t"
    "v_mfma_f32_16x16x32_bf16 v[20:23], v[132:135], v[116:119], v[20:23]\n\t"
    "global_load_lds_dwordx4 v200, s[40:41] sc1\n\t"
    "s_add_u32 s40, s40, 0x400\n\t"
    "s_addc_u32 s41, s41, 0\n\t"
    "v_mfma_f32_16x16x32_bf16 v[4:7], v[128:131], v[116:119], v[4:7]\n\t"
    "s_add_u32 m0, m0, 0x400\n\t"
    "v_mfma_f32_16x16x32_bf16 v[8:11], v[128:131], v[120:123], v[8:11]\n\t"
    "global_load_lds_dwordx4 v200, s[42:43] sc1\n\t"
    "s_add_u32 s42, s42, 0x400\n\t"
    "s_addc_u32 s43, s43, 0\n\t"
    "v_mfma_f32_16x16x32_bf16 v[24:27], v[132:135], v[120:123], v[24:27]\n\t"
    "s_and_b32 s62, s51, s55\n\t"
    "s_add_u32 m0, s62, s49\n\t"
    "v_mfma_f32_16x16x32_bf16 v[40:43], v[136:139], v[120:123], v[40:43]\n\t"
    "global_load_lds_dwordx4 v205, s[44:45] sc1\n\t"
    "s_add_u32 s44, s44, s53\n\t"
    "s_addc_u32 s45, s45, 0\n\t"
    "v_mfma_f32_16x16x32_bf16 v[56:59], v[140:143], v[120:123], v[56:59]\n\t"
    "s_add_u32 m0, m0, s54\n\t"
    "v_mfma_f32_16x16x32_bf16 v[72:75], v[144:147], v[120:123], v[72:75]\n\t"
    "global_load_lds_dwordx4 v205, s[46:47] sc1\n\t"
    "s_add_u32 s46, s46, s53\n\t"
    "s_addc_u32 s47, s47, 0\n\t"
    "v_mfma_f32_16x16x32_bf16 v[88:91], v[148:151], v[120:123], v[88:91]\n\t"
    "s_add_u32 s50, s50, 30720\n\t"
    "s_cmp_eq_u32 s50, 153600\n\t"
    "s_cselect_b32 s50, 0, s50\n\t"
    "v_mfma_f32_16x16x32_bf16 v[104:107], v[152:155], v[120:123], v[104:107]\n\t"
    "s_add_u32 s51, s51, 30720\n\t"
    "s_cmp_eq_u32 s51, 153600\n\t"
    "s_cselect_b32 s51, 0, s51\n\t"
    "v_mfma_f32_16x16x32_bf16 v[108:111], v[152:155], v[124:127], v[108:111]\n\t"
    "v_mfma_f32_16x16x32_bf16 v[92:95], v[148:151], v[124:127], v[92:95]\n\t"
    "v_mfma_f32_16x16x32_bf16 v[76:79], v[144:147], v[124:127], v[76:79]\n\t"
    "v_mfma_f32_16x16x32_bf16 v[60:63], v[140:143], v[124:127], v[60:63]\n\t"
    "v_mfma_f32_16x16x32_bf16 v[44:47], v[136:139], v[124:127], v[44:47]\n\t"
    "v_mfma_f32_16x16x32_bf16 v[28:31], v[132:135], v[124:127], v[28:31]\n\t"
    "v_mfma_f32_16x16x32_bf16 v[12:15], v[128:131], v[124:127], v[12:15]\n\t"
    "v_add_u32 v203, s50, v201\n\t"
    "v_add_u32 v204, s50, v202\n\t"
    "s_waitcnt vmcnt(4) lgkmcnt(0)\n\t"
    "s_barrier\n\t"
    "v_mfma_f32_16x16x32_bf16 v[0:3], v[172:175], v[156:159], v[0:3]\n\t"
    "ds_read_b128 v[128:131], v204\n\t"
    "v_mfma_f32_16x16x32_bf16 v[16:19], v[176:179], v[156:159], v[16:19]\n\t"
    "ds_read_b128 v[112:115], v203 offset:0\n\t"
    "v_mfma_f32_16x16x32_bf16 v[32:35], v[180:183], v[156:159], v[32:35]\n\t"
    "ds_read_b128 v[116:119], v203 offset:1024\n\t"
    "v_mfma_f32_16x16x32_bf16 v[48:51], v[184:187], v[156:159], v[48:51]\n\t"
    "ds_read_b128 v[120:123], v203 offset:2048\n\t"
    "v_mfma_f32_16x16x32_bf16 v[64:67], v[188:191], v[156:159], v[64:67]\n\t"
    "ds_read_b128 v[124:127], v203 offset:3072\n\t"
    "v_mfma_f32_16x16x32_bf16 v[80:83], v[192:195], v[156:159], v[80:83]\n\t"
    "ds_read_b128 v[132:135], v204 offset:1024\n\t"
    "v_mfma_f32_16x16x32_bf16 v[96:99], v[196:199], v[156:159], v[96:99]\n\t"
    "ds_read_b128 v[136:139], v204 offset:2048\n\t"
    "v_mfma_f32_16x16x32_bf16 v[100:103], v[196:199], v[160:163], v[100:103]\n\t"
    "ds_read_b128 v[140:143], v204 offset:3072\n\t"
    "v_mfma_f32_16x16x32_bf16 v[84:87], v[192:195], v[160:163], v[84:87]\n\t"
    "ds_read_b128 v[144:147], v204 offset:4096\n\t"
    "v_mfma_f32_16x16x32_bf16 v[68:71], v[188:191], v[160:163], v[68:71]\n\t"
    "ds_read_b128 v[148:151], v204 offset:5120\n\t"
    "v_mfma_f32_16x16x32_bf16 v[52:55], v[184:187], v[160:163], v[52:55]\n\t"
    "ds_read_b128 v[152:155], v204 offset:6144\n\t"
    "v_mfma_f32_16x16x32_bf16 v[36:39], v[180:183], v[160:163], v[36:39]\n\t"
    "s_add_u32 m0, s51, s48\n\t"
    "v_mfma_f32_16x16x32_bf16 v[20:23], v[176:179], v[160:163], v[20:23]\n\t"
    "global_load_lds_dwordx4 v200, s[40:41] sc1\n\t"
    "s_add_u32 s40, s40, 0x400\n\t"
    "s_addc_u32 s41, s41, 0\n\t"
    "v_mfma_f32_16x16x32_bf16 v[4:7], v[172:175], v[160:163], v[4:7]\n\t"
    "s_add_u32 m0, m0, 0x400\n\t"
    "v_mfma_f32_16x16x32_bf16 v[8:11], v[172:175], v[164:167], v[8:11]\n\t"
    "global_load_lds_dwordx4 v200, s[42:43] sc1\n\t"
    "s_add_u32 s42, s42, 0x400\n\t"
    "s_addc_u32 s43, s43, 0\n\t"
    "v_mfma_f32_16x16x32_bf16 v[24:27], v[176:179], v[164:167], v[24:27]\n\t"
    "s_and_b32 s62, s51, s55\n\t"
    "s_add_u32 m0, s62, s49\n\t"
    "v_mfma_f32_16x16x32_bf16 v[40:43], v[180:183], v[164:167], v[40:43]\n\t"
    "global_load_lds_dwordx4 v205, s[44:45] sc1\n\t"
    "s_add_u32 s44, s44, s53\n\t"
    "s_addc_u32 s45, s45, 0\n\t"
    "v_mfma_f32_16x16x32_bf16 v[56:59], v[184:187], v[164:167], v[56:59]\n\t"
    "s_add_u32 m0, m0, s54\n\t"
    "v_mfma_f32_16x16x32_bf16 v[72:75], v[188:191], v[164:167], v[72:75]\n\t"
    "global_load_lds_dwordx4 v205, s[46:47] sc1\n\t"
    "s_add_u32 s46, s46, s53\n\t"
    "s_addc_u32 s47, s47, 0\n\t"
    "v_mfma_f32_16x16x32_bf16 v[88:91], v[192:195], v[164:167], v[88:91]\n\t"
    "s_add_u32 s50, s50, 30720\n\t"
    "s_cmp_eq_u32 s50, 153600\n\t"
    "s_cselect_b32 s50, 0, s50\n\t"
    "v_mfma_f32_16x16x32_bf16 v[104:107], v[196:199], v[164:167], v[104:107]\n\t"
    "s_add_u32 s51, s51, 30720\n\t"
    "s_cmp_eq_u32 s51, 153600\n\t"
    "s_cselect_b32 s51, 0, s51\n\t"
    "v_mfma_f32_16x16x32_bf16 v[108:111], v[196:199], v[168:171], v[108:111]\n\t"
    "v_mfma_f32_16x16x32_bf16 v[92:95], v[192:195], v[168:171], v[92:95]\n\t"
    "v_mfma_f32_16x16x32_bf16 v[76:79], v[188:191], v[168:171], v[76:79]\n\t"
    "v_mfma_f32_16x16x32_bf16 v[60:63], v[184:187], v[168:171], v[60:63]\n\t"
    "v_mfma_f32_16x16x32_bf16 v[44:47], v[180:183], v[168:171], v[44:47]\n\t"
    "v_mfma_f32_16x16x32_bf16 v[28:31], v[176:179], v[168:171], v[28:31]\n\t"
    "v_mfma_f32_16x16x32_bf16 v[12:15], v[172:175], v[168:171], v[12:15]\n\t"
    "s_sub_u32 s52, s52, 1\n\t"
    "s_cmp_lg_u32 s52, 0\n\t"
    "s_cbranch_scc1 1b\n\t"
    "v_add_u32 v203, s50, v201\n\t"
    "v_add_u32 v204, s50, v202\n\t"
    "s_waitcnt vmcnt(4) lgkmcnt(0)\n\t"
    "s_barrier\n\t"
    "v_mfma_f32_16x16x32_bf16 v[0:3], v[128:131], v[112:115], v[0:3]\n\t"
    "ds_read_b128 v[172:175], v204\n\t"
    "v_mfma_f32_16x16x32_bf16 v[16:19], v[132:135], v[112:115], v[16:19]\n\t"
    "ds_read_b128 v[156:159], v203 offset:0\n\t"
    "v_mfma_f32_16x16x32_bf16 v[32:35], v[136:139], v[112:115], v[32:35]\n\t"
    "ds_read_b128 v[160:163], v203 offset:1024\n\t"
    "v_mfma_f32_16x16x32_bf16 v[48:51], v[140:143], v[112:115], v[48:51]\n\t"
    "ds_read_b128 v[164:167], v203 offset:2048\n\t"
    "v_mfma_f32_16x16x32_bf16 v[64:67], v[144:147], v[112:115], v[64:67]\n\t"
    "ds_read_b128 v[168:171], v203 offset:3072\n\t"
    "v_mfma_f32_16x16x32_bf16 v[80:83], v[148:151], v[112:115], v[80:83]\n\t"
    "ds_read_b128 v[176:179], v204 offset:1024\n\t"
    "v_mfma_f32_16x16x32_bf16 v[96:99], v[152:155], v[112:115], v[96:99]\n\t"
    "ds_read_b128 v[180:183], v204 offset:2048\n\t"
    "v_mfma_f32_16x16x32_bf16 v[100:103], v[152:155], v[116:119], v[100:103]\n\t"
    "ds_read_b128 v[184:187], v204 offset:3072\n\t"
    "v_mfma_f32_16x16x32_bf16 v[84:87], v[148:151], v[116:119], v[84:87]\n\t"
    "ds_read_b128 v[188:191], v204 offset:4096\n\t"
    "v_mfma_f32_16x16x32_bf16 v[68:71], v[144:147], v[116:119], v[68:71]\n\t"
    "ds_read_b128 v[192:195], v204 offset:5120\n\t"
    "v_mfma_f32_16x16x32_bf16 v[52:55], v[140:143], v[116:119], v[52:55]\n\t"
    "ds_read_b128 v[196:199], v204 offset:6144\n\t"
    "v_mfma_f32_16x16x32_bf16 v[36:39], v[136:139], v[116:119], v[36:39]\n\t"
    "s_add_u32 m0, s51, s48\n\t"
    "v_mfma_f32_16x16x32_bf16 v[20:23], v[132:135], v[116:119], v[20:23]\n\t"
    "global_load_lds_dwordx4 v200, s[40:41] sc1\n\t"
    "s_add_u32 s40, s40, 0x400\n\t"
    "s_addc_u32 s41, s41, 0\n\t"
    "v_mfma_f32_16x16x32_bf16 v[4:7], v[128:131], v[116:119], v[4:7]\n\t"
    "s_add_u32 m0, m0, 0x400\n\t"
    "v_mfma_f32_16x16x32_bf16 v[8:11], v[128:131], v[120:123], v[8:11]\n\t"
    "global_load_lds_dwordx4 v200, s[42:43] sc1\n\t"
    "s_add_u32 s42, s42, 0x400\n\t"
    "s_addc_u32 s43, s43, 0\n\t"
    "v_mfma_f32_16x16x32_bf16 v[24:27], v[132:135], v[120:123], v[24:27]\n\t"
    "s_and_b32 s62, s51, s55\n\t"
    "s_add_u32 m0, s62, s49\n\t"
    "v_mfma_f32_16x16x32_bf16 v[40:43], v[136:139], v[120:123], v[40:43]\n\t"
    "global_load_lds_dwordx4 v205, s[44:45] sc1\n\t"
    "s_add_u32 s44, s44, s53\n\t"
    "s_addc_u32 s45, s45, 0\n\t"
    "v_mfma_f32_16x16x32_bf16 v[56:59], v[140:143], v[120:123], v[56:59]\n\t"
    "s_add_u32 m0, m0, s54\n\t"
    "v_mfma_f32_16x16x32_bf16 v[72:75], v[144:147], v[120:123], v[72:75]\n\t"
    "global_load_lds_dwordx4 v205, s[46:47] sc1\n\t"
    "s_add_u32 s46, s46, s53\n\t"
    "s_addc_u32 s47, s47, 0\n\t"
    "v_mfma_f32_16x16x32_bf16 v[88:91], v[148:151], v[120:123], v[88:91]\n\t"
    "s_add_u32 s50, s50, 30720\n\t"
    "s_cmp_eq_u32 s50, 153600\n\t"
    "s_cselect_b32 s50, 0, s50\n\t"
    "v_mfma_f32_16x16x32_bf16 v[104:107], v[152:155], v[120:123], v[104:107]\n\t"
    "s_add_u32 s51, s51, 30720\n\t"
    "s_cmp_eq_u32 s51, 153600\n\t"
    "s_cselect_b32 s51, 0, s51\n\t"
    "v_mfma_f32_16x16x32_bf16 v[108:111], v[152:155], v[124:127], v[108:111]\n\t"
    "v_mfma_f32_16x16x32_bf16 v[92:95], v[148:151], v[124:127], v[92:95]\n\t"
    "v_mfma_f32_16x16x32_bf16 v[76:79], v[144:147], v[124:127], v[76:79]\n\t"
    "v_mfma_f32_16x16x32_bf16 v[60:63], v[140:143], v[124:127], v[60:63]\n\t"
    "v_mfma_f32_16x16x32_bf16 v[44:47], v[136:139], v[124:127], v[44:47]\n\t"
    "v_mfma_f32_16x16x32_bf16 v[28:31], v[132:135], v[124:127], v[28:31]\n\t"
    "v_mfma_f32_16x16x32_bf16 v[12:15], v[128:131], v[124:127], v[12:15]\n\t"
    "s_mov_b64 s[40:41], s[56:57]\n\t"
    "s_mov_b64 s[42:43], s[58:59]\n\t"
    "s_mov_b64 s[44:45], s[60:61]\n\t"
    "s_mov_b64 s[46:47], s[60:61]\n\t"
    "s_mov_b32 s54, 0\n\t"
    "s_mov_b32 s49, s63\n\t"
    "s_mov_b32 s55, -1\n\t"
    "v_mov_b32 v205, v206\n\t"
    "v_add_u32 v203, s50, v201\n\t"
    "v_add_u32 v204, s50, v202\n\t"
    "s_waitcnt vmcnt(4) lgkmcnt(0)\n\t"
    "s_barrier\n\t"
    "v_mfma_f32_16x16x32_bf16 v[0:3], v[172:175], v[156:159], v[0:3]\n\t"
    "ds_read_b128 v[128:131], v204\n\t"
    "v_mfma_f32_16x16x32_bf16 v[16:19], v[176:179], v[156:159], v[16:19]\n\t"
    "ds_read_b128 v[112:115], v203 offset:0\n\t"
    "v_mfma_f32_16x16x32_bf16 v[32:35], v[180:183], v[156:159], v[32:35]\n\t"
    "ds_read_b128 v[116:119], v203 offset:1024\n\t"
    "v_mfma_f32_16x16x32_bf16 v[48:51], v[184:187], v[156:159], v[48:51]\n\t"
    "ds_read_b128 v[120:123], v203 offset:2048\n\t"
    "v_mfma_f32_16x16x32_bf16 v[64:67], v[188:191], v[156:159], v[64:67]\n\t"
    "ds_read_b128 v[124:127], v203 offset:3072\n\t"
    "v_mfma_f32_16x16x32_bf16 v[80:83], v[192:195], v[156:159], v[80:83]\n\t"
    "ds_read_b128 v[132:135], v204 offset:1024\n\t"
    "v_mfma_f32_16x16x32_bf16 v[96:99], v[196:199], v[156:159], v[96:99]\n\t"
    "ds_read_b128 v[136:139], v204 offset:2048\n\t"
    "v_mfma_f32_16x16x32_bf16 v[100:103], v[196:199], v[160:163], v[100:103]\n\t"
    "ds_read_b128 v[140:143], v204 offset:3072\n\t"
    "v_mfma_f32_16x16x32_bf16 v[84:87], v[192:195], v[160:163], v[84:87]\n\t"
    "ds_read_b128 v[144:147], v204 offset:4096\n\t"
    "v_mfma_f32_16x16x32_bf16 v[68:71], v[188:191], v[160:163], v[68:71]\n\t"
    "ds_read_b128 v[148:151], v204 offset:5120\n\t"
    "v_mfma_f32_16x16x32_bf16 v[52:55], v[184:187], v[160:163], v[52:55]\n\t"
    "ds_read_b128 v[152:155], v204 offset:6144\n\t"
    "v_mfma_f32_16x16x32_bf16 v[36:39], v[180:183], v[160:163], v[36:39]\n\t"
    "s_add_u32 m0, s51, s48\n\t"
    "v_mfma_f32_16x16x32_bf16 v[20:23], v[176:179], v[160:163], v[20:23]\n\t"
    "global_load_lds_dwordx4 v200, s[40:41] sc1\n\t"
    "s_add_u32 s40, s40, 0x400\n\t"
    "s_addc_u32 s41, s41, 0\n\t"
    "v_mfma_f32_16x16x32_bf16 v[4:7], v[172:175], v[160:163], v[4:7]\n\t"
    "s_add_u32 m0, m0, 0x400\n\t"
    "v_mfma_f32_16x16x32_bf16 v[8:11], v[172:175], v[164:167], v[8:11]\n\t"
    "global_load_lds_dwordx4 v200, s[42:43] sc1\n\t"
    "s_add_u32 s42, s42, 0x400\n\t"
    "s_addc_u32 s43, s43, 0\n\t"
    "v_mfma_f32_16x16x32_bf16 v[24:27], v[176:179], v[164:167], v[24:27]\n\t"
    "s_and_b32 s62, s51, s55\n\t"
    "s_add_u32 m0, s62, s49\n\t"
    "v_mfma_f32_16x16x32_bf16 v[40:43], v[180:183], v[164:167], v[40:43]\n\t"
    "global_load_lds_dwordx4 v205, s[44:45] sc1\n\t"
    "s_add_u32 s44, s44, s53\n\t"
    "s_addc_u32 s45, s45, 0\n\t"
    "v_mfma_f32_16x16x32_bf16 v[56:59], v[184:187], v[164:167], v[56:59]\n\t"
    "s_add_u32 m0, m0, s54\n\t"
    "v_mfma_f32_16x16x32_bf16 v[72:75], v[188:191], v[164:167], v[72:75]\n\t"
    "global_load_lds_dwordx4 v205, s[46:47] sc1\n\t"
    "s_add_u32 s46, s46, s53\n\t"
    "s_addc_u32 s47, s47, 0\n\t"
    "v_mfma_f32_16x16x32_bf16 v[88:91], v[192:195], v[164:167], v[88:91]\n\t"
    "s_add_u32 s50, s50, 30720\n\t"
    "s_cmp_eq_u32 s50, 153600\n\t"
    "s_cselect_b32 s50, 0, s50\n\t"
    "v_mfma_f32_16x16x32_bf16 v[104:107], v[196:199], v[164:167], v[104:107]\n\t"
    "s_add_u32 s51, s51, 30720\n\t"
    "s_cmp_eq_u32 s51, 153600\n\t"
    "s_cselect_b32 s51, 0, s51\n\t"
    "v_mfma_f32_16x16x32_bf16 v[108:111], v[196:199], v[168:171], v[108:111]\n\t"
    "v_mfma_f32_16x16x32_bf16 v[92:95], v[192:195], v[168:171], v[92:95]\n\t"
    "v_mfma_f32_16x16x32_bf16 v[76:79], v[188:191], v[168:171], v[76:79]\n\t"
    "v_mfma_f32_16x16x32_bf16 v[60:63], v[184:187], v[168:171], v[60:63]\n\t"
    "v_mfma_f32_16x16x32_bf16 v[44:47], v[180:183], v[168:171], v[44:47]\n\t"
    "v_mfma_f32_16x16x32_bf16 v[28:31], v[176:179], v[168:171], v[28:31]\n\t"
    "v_mfma_f32_16x16x32_bf16 v[12:15], v[172:175], v[168:171], v[12:15]\n\t"
    "v_add_u32 v203, s50, v201\n\t"
    "v_add_u32 v204, s50, v202\n\t"
    "s_waitcnt vmcnt(4) lgkmcnt(0)\n\t"
    "s_barrier\n\t"
    "v_mfma_f32_16x16x32_bf16 v[0:3], v[128:131], v[112:115], v[0:3]\n\t"
    "ds_read_b128 v[172:175], v204\n\t"
    "v_mfma_f32_16x16x32_bf16 v[16:19], v[132:135], v[112:115], v[16:19]\n\t"
    "ds_read_b128 v[156:159], v203 offset:0\n\t"
    "v_mfma_f32_16x16x32_bf16 v[32:35], v[136:139], v[112:115], v[32:35]\n\t"
    "ds_read_b128 v[160:163], v203 offset:1024\n\t"
    "v_mfma_f32_16x16x32_bf16 v[48:51], v[140:143], v[112:115], v[48:51]\n\t"
    "ds_read_b128 v[164:167], v203 offset:2048\n\t"
    "v_mfma_f32_16x16x32_bf16 v[64:67], v[144:147], v[112:115], v[64:67]\n\t"
    "ds_read_b128 v[168:171], v203 offset:3072\n\t"
    "v_mfma_f32_16x16x32_bf16 v[80:83], v[148:151], v[112:115], v[80:83]\n\t"
    "ds_read_b128 v[176:179], v204 offset:1024\n\t"
    "v_mfma_f32_16x16x32_bf16 v[96:99], v[152:155], v[112:115], v[96:99]\n\t"
    "ds_read_b128 v[180:183], v204 offset:2048\n\t"
    "v_mfma_f32_16x16x32_bf16 v[100:103], v[152:155], v[116:119], v[100:103]\n\t"
    "ds_read_b128 v[184:187], v204 offset:3072\n\t"
    "v_mfma_f32_16x16x32_bf16 v[84:87], v[148:151], v[116:119], v[84:87]\n\t"
    "ds_read_b128 v[188:191], v204 offset:4096\n\t"
    "v_mfma_f32_16x16x32_bf16 v[68:71], v[144:147], v[116:119], v[68:71]\n\t"
    "ds_read_b128 v[192:195], v204 offset:5120\n\t"
    "v_mfma_f32_16x16x32_bf16 v[52:55], v[140:143], v[116:119], v[52:55]\n\t"
    "ds_read_b128 v[196:199], v204 offset:6144\n\t"
    "v_mfma_f32_16x16x32_bf16 v[36:39], v[136:139], v[116:119], v[36:39]\n\t"
    "s_add_u32 s50, s50, 30720\n\t"
    "s_cmp_eq_u32 s50, 153600\n\t"
    "s_cselect_b32 s50, 0, s50\n\t"
    "v_mfma_f32_16x16x32_bf16 v[20:23], v[132:135], v[116:119], v[20:23]\n\t"
    "s_add_u32 s51, s51, 30720\n\t"
    "s_cmp_eq_u32 s51, 153600\n\t"
    "s_cselect_b32 s51, 0, s51\n\t"
    "v_mfma_f32_16x16x32_bf16 v[4:7], v[128:131], v[116:119], v[4:7]\n\t"
    "v_mfma_f32_16x16x32_bf16 v[8:11], v[128:131], v[120:123], v[8:11]\n\t"
    "v_mfma_f32_16x16x32_bf16 v[24:27], v[132:135], v[120:123], v[24:27]\n\t"
    "v_mfma_f32_16x16x32_bf16 v[40:43], v[136:139], v[120:123], v[40:43]\n\t"
    "v_mfma_f32_16x16x32_bf16 v[56:59], v[140:143], v[120:123], v[56:59]\n\t"
    "v_mfma_f32_16x16x32_bf16 v[72:75], v[144:147], v[120:123], v[72:75]\n\t"
    "v_mfma_f32_16x16x32_bf16 v[88:91], v[148:151], v[120:123], v[88:91]\n\t"
    "v_mfma_f32_16x16x32_bf16 v[104:107], v[152:155], v[120:123], v[104:107]\n\t"
    "v_mfma_f32_16x16x32_bf16 v[108:111], v[152:155], v[124:127], v[108:111]\n\t"
    "v_mfma_f32_16x16x32_bf16 v[92:95], v[148:151], v[124:127], v[92:95]\n\t"
    "v_mfma_f32_16x16x32_bf16 v[76:79], v[144:147], v[124:127], v[76:79]\n\t"
    "v_mfma_f32_16x16x32_bf16 v[60:63], v[140:143], v[124:127], v[60:63]\n\t"
    "v_mfma_f32_16x16x32_bf16 v[44:47], v[136:139], v[124:127], v[44:47]\n\t"
    "v_mfma_f32_16x16x32_bf16 v[28:31], v[132:135], v[124:127], v[28:31]\n\t"
    "v_mfma_f32_16x16x32_bf16 v[12:15], v[128:131], v[124:127], v[12:15]\n\t"
    "s_waitcnt lgkmcnt(0)\n\t"
    "v_mfma_f32_16x16x32_bf16 v[0:3], v[172:175], v[156:159], v[0:3]\n\t"
    "v_mfma_f32_16x16x32_bf16 v[16:19], v[176:179], v[156:159], v[16:19]\n\t"
    "v_mfma_f32_16x16x32_bf16 v[32:35], v[180:183], v[156:159], v[32:35]\n\t"
    "v_mfma_f32_16x16x32_bf16 v[48:51], v[184:187], v[156:159], v[48:51]\n\t"
    "v_mfma_f32_16x16x32_bf16 v[64:67], v[188:191], v[156:159], v[64:67]\n\t"
    "v_mfma_f32_16x16x32_bf16 v[80:83], v[192:195], v[156:159], v[80:83]\n\t"
    "v_mfma_f32_16x16x32_bf16 v[96:99], v[196:199], v[156:159], v[96:99]\n\t"
    "v_mfma_f32_16x16x32_bf16 v[100:103], v[196:199], v[160:163], v[100:103]\n\t"
    "v_mfma_f32_16x16x32_bf16 v[84:87], v[192:195], v[160:163], v[84:87]\n\t"
    "v_mfma_f32_16x16x32_bf16 v[68:71], v[188:191], v[160:163], v[68:71]\n\t"
    "v_mfma_f32_16x16x32_bf16 v[52:55], v[184:187], v[160:163], v[52:55]\n\t"
    "v_mfma_f32_16x16x32_bf16 v[36:39], v[180:183], v[160:163], v[36:39]\n\t"
    "v_mfma_f32_16x16x32_bf16 v[20:23], v[176:179], v[160:163], v[20:23]\n\t"
    "v_mfma_f32_16x16x32_bf16 v[4:7], v[172:175], v[160:163], v[4:7]\n\t"
    "v_mfma_f32_16x16x32_bf16 v[8:11], v[172:175], v[164:167], v[8:11]\n\t"
    "v_mfma_f32_16x16x32_bf16 v[24:27], v[176:179], v[164:167], v[24:27]\n\t"
    "v_mfma_f32_16x16x32_bf16 v[40:43], v[180:183], v[164:167], v[40:43]\n\t"
    "v_mfma_f32_16x16x32_bf16 v[56:59], v[184:187], v[164:167], v[56:59]\n\t"
    "v_mfma_f32_16x16x32_bf16 v[72:75], v[188:191], v[164:167], v[72:75]\n\t"
    "v_mfma_f32_16x16x32_bf16 v[88:91], v[192:195], v[164:167], v[88:91]\n\t"
    "s_waitcnt vmcnt(0)\n\t"
    "s_barrier\n\t"
    "v_mfma_f32_16x16x32_bf16 v[104:107], v[196:199], v[164:167], v[104:107]\n\t"
    "s_add_u32 s50, s50, 30720\n\t"
    "s_cmp_eq_u32 s50, 153600\n\t"
    "s_cselect_b32 s50, 0, s50\n\t"
    "v_mfma_f32_16x16x32_bf16 v[108:111], v[196:199], v[168:171], v[108:111]\n\t"
    "s_add_u32 s51, s51, 30720\n\t"
    "s_cmp_eq_u32 s51, 153600\n\t"
    "s_cselect_b32 s51, 0, s51\n\t"
    "v_mfma_f32_16x16x32_bf16 v[92:95], v[192:195], v[168:171], v[92:95]\n\t"
    "v_mfma_f32_16x16x32_bf16 v[76:79], v[188:191], v[168:171], v[76:79]\n\t"
    "v_mfma_f32_16x16x32_bf16 v[60:63], v[184:187], v[168:171], v[60:63]\n\t"
    "v_mfma_f32_16x16x32_bf16 v[44:47], v[180:183], v[168:171], v[44:47]\n\t"
    "v_mfma_f32_16x16x32_bf16 v[28:31], v[176:179], v[168:171], v[28:31]\n\t"
    "v_mfma_f32_16x16x32_bf16 v[12:15], v[172:175], v[168:171], v[12:15]\n\t"
    "s_nop 7\n\t"
        ""
        : "=&{v[0:15]}"(acc[0]), "=&{v[16:31]}"(acc[1]), "=&{v[32:47]}"(acc[2]), "=&{v[48:63]}"(acc[3]), "=&{v[64:79]}"(acc[4]), "=&{v[80:95]}"(acc[5]), "=&{v[96:111]}"(acc[6]),
          "+{s[40:41]}"(xs0), "+{s[42:43]}"(xs1), "+{s[44:45]}"(ws0), "+{s[46:47]}"(ws1), "+{s54}"(w2v), "+{v205}"(wvov), "+{s49}"(dwv), "+{s55}"(wmv)
        : "{v200}"(xvo), "{v201}"(gxa), "{v202}"(gwa), "{v206}"(evo), "{s48}"(dx), "{s53}"(wstep), "{s63}"(de), "{s[56:57]}"(es0), "{s[58:59]}"(es1), "{s[60:61]}"(es2)
        : "memory", "scc", "s50", "s51", "s52", "s62", "v203", "v204",
          "v112", "v113", "v114", "v115", "v116", "v117", "v118", "v119", "v120", "v121", "v122", "v123", "v124", "v125", "v126", "v127", "v128", "v129", "v130", "v131", "v132", "v133",
          "v134", "v135", "v136", "v137", "v138", "v139", "v140", "v141", "v142", "v143", "v144", "v145", "v146", "v147", "v148", "v149", "v150", "v151", "v152", "v153", "v154", "v155",
          "v156", "v157", "v158", "v159", "v160", "v161", "v162", "v163", "v164", "v165", "v166", "v167", "v168", "v169", "v170", "v171", "v172", "v173", "v174", "v175", "v176", "v177",
          "v178", "v179", "v180", "v181", "v182", "v183", "v184", "v185", "v186", "v187", "v188", "v189", "v190", "v191", "v192", "v193", "v194", "v195", "v196", "v197", "v198", "v199");
    const unsigned eb = lds0 + K5_EXSLOT * K5_SLOT, fin = lds0 + 159744;
    if (tid < 256) {
        f32x2 p[8];
#pragma unroll
        for (int i = 0; i < 8; ++i) p[i] = *(const LAS f32x2*)(size_t)(eb + tid * 8 + i * 2048);
        const float mean = 0.125f * (((p[0][0] + p[1][0]) + (p[2][0] + p[3][0])) + ((p[4][0] + p[5][0]) + (p[6][0] + p[7][0])));
        float m2 = 0.f;
#pragma unroll
        for (int i = 0; i < 8; ++i) { const float d = p[i][0] - mean; m2 += p[i][1] + 128.f * d * d; }
        const float rstd = __builtin_amdgcn_rsqf(m2 * (1.f / 1024.f) + 1e-5f);
        *(LAS f32x2*)(size_t)(fin + tid * 8) = f32x2{rstd, -mean * rstd};
    } else if (tid < 256 + 112) {
        const int j = tid - 256, f = j >> 3, fw = f >= 7, n = f - 7 * fw, r4 = j & 3;
        const int line = n < 4 ? 4 * fw + n : n < 6 ? 4 + 2 * fw + n : 12 + 2 * fw + (r4 >> 1), rq = n < 6 ? r4 : 2 * par + (r4 & 1);
        const unsigned a = eb + 16384 + line * 128 + (j & 4) * 16 + rq * 16;
        *(LAS f32x4*)(size_t)(fin + 2048 + j * 16) = (ldsf4(a) + ldsf4(a + 2048)) + (ldsf4(a + 4096) + ldsf4(a + 6144));
    }
    WAITL0(); BAR();
    const int ks = xcd >> 2, dmt = (xcd & 3) * 4 + (idx & 3), dnt = idx >> 2;
    const int me = blockIdx.x, partner = blockIdx.x ^ 4;
    const int dwm = wid >> 2, dwn = wid & 3;
    const int dcb = dnt * 4 + ks * 2 + (dwn >> 1);
    const long xstep = ks ? -4096 : 4096, dwstep = ks ? -131072 : 131072;
    const char* dxs = ws + WS_ACT + (size_t)(dmt * 4 + (wid >> 2)) * 229376 + (wid & 3) * 1024 + (ks ? 55 * 4096 : 0);
    const char* wb0 = wd + dnt * 256 + (ks ? (size_t)55 * 131072 : 0);
    unsigned wvf, wvh;
    { const int r = wid * 4 + lg; wvf = (unsigned)r * 2048 + (unsigned)(((li >> 1) ^ ((r & 1) | (((r >> 3) & 3) << 1))) * 32 + (li & 1) * 16); }
    { const int r = wid * 8 + (lane >> 3); wvh = (unsigned)r * 2048 + (unsigned)(ks * 128 + ((((lane >> 1) & 3) ^ (wid & 3)) * 32) + (lane & 1) * 16); }
    auto stage = [&](int st, int slot, bool full, int part = 3) {
        const unsigned lb = lds0 + slot * 32768 + wid * 1024;
        const char* x0 = dxs + st * xstep; const char* wb = wb0 + st * dwstep;
        if (part & 1) { glds16c(x0, l16, lb); glds16c(x0 + 2 * 229376, l16, lb + 8192); }
        if (!(part & 2)) return;
        if (full) { glds16(wb, wvf, lb + 16384); glds16(wb + 65536, wvf, lb + 24576); } else glds16(wb, wvh, lb + 16384);
    };
    f32x4 gp;
    const int m16 = li;
    f32x2 rs[4];
    unsigned rsa = fin + (wm * 64 + m16) * 8, uta = fin + 2048 + wc * 896 + lg * 16;
    asm volatile("" : "+v"(rsa), "+v"(uta));
#pragma unroll
    for (int m = 0; m < 4; ++m) rs[m] = *(const LAS f32x2*)(size_t)(rsa + m * 128);
    f32x4 uu[7], tvv[7];
#pragma unroll
    for (int n = 4; n < 6; ++n) { uu[n] = ldsf4(uta + n * 128); tvv[n] = ldsf4(uta + n * 128 + 64); }
    auto act4 = [&](int n0, int dn, int m0, int dm) {
        f32x2 g[4], up[4], w[4]; u32x4 r;
#define ST4(x) _Pragma("unroll") for (int i = 0; i < 4; ++i) { const int n = n0 + dn * i, m = m0 + dm * i; (void)n; (void)m; x; } SB0()
        SB0();
        ST4(g[i] = (rs[m][1] * f32x2{uu[n][0], uu[n][1]} + f32x2{tvv[n][0], tvv[n][1]}));
        ST4(g[i] = (rs[m][0] * f32x2{acc[n][4 * m], acc[n][4 * m + 1]} + g[i]));
        ST4(w[i] = g[i] * g[i]);
        ST4(w[i] = -2.3022082f - 0.10294324f * w[i]);
        ST4(w[i] = w[i] * g[i]);
        ST4(w[i] = (f32x2{__builtin_amdgcn_exp2f(w[i][0]), __builtin_amdgcn_exp2f(w[i][1])}));
        ST4(up[i] = (rs[m][1] * f32x2{uu[n][2], uu[n][3]} + f32x2{tvv[n][2], tvv[n][3]}));
        ST4(w[i] = w[i] + 1.f);
        ST4(w[i] = (f32x2{__builtin_amdgcn_rcpf(w[i][0]), __builtin_amdgcn_rcpf(w[i][1])}));
        ST4(up[i] = (rs[m][0] * f32x2{acc[n][4 * m + 2], acc[n][4 * m + 3]} + up[i]));
        ST4(w[i] = w[i] * g[i]);
        ST4(w[i] = w[i] * up[i]);
        ST4(r[i] = cvtpk(w[i][0], w[i][1]));
        return r;
    };
    const unsigned lo = lds0 + 131072 + wm * 6144 + (m16 + 32 * (lg & 1)) * 16;
    unsigned lo8 = lo + (lg >> 1) * 1024 + wc * 8, lo4 = lo + 2048 + (2 * wc + (lg >> 1)) * 4;
    asm volatile("" : "+v"(lo8), "+v"(lo4));
    {
        const u32x4 a4 = act4(4, 0, 0, 1);
        stage(0, 0, true, 2);
        uu[6] = ldsf4(uta + 768); tvv[6] = ldsf4(uta + 832); uu[0] = ldsf4(uta); tvv[0] = ldsf4(uta + 64); uu[1] = ldsf4(uta + 128); tvv[1] = ldsf4(uta + 192);
        const u32x4 a5 = act4(5, 0, 0, 1);
#pragma unroll
        for (int m = 0; m < 4; ++m) *(LAS u32x2*)(size_t)(lo8 + (m >> 1) * 3072 + (m & 1) * 256) = u32x2{a4[m], a5[m]};
        stage(1, 1, true, 2);
        uu[2] = ldsf4(uta + 256); tvv[2] = ldsf4(uta + 320); uu[3] = ldsf4(uta + 384); tvv[3] = ldsf4(uta + 448);
        const u32x4 a6 = act4(6, 0, 0, 1);
#pragma unroll
        for (int m = 0; m < 4; ++m) *(LAS unsigned*)(size_t)(lo4 + (m >> 1) * 3072 + (m & 1) * 256) = a6[m];
        stage(2, 2, true, 2);
        asm volatile("global_load_dwordx4 %0, %1, %2 sc1" : "=&v"(gp) : "v"((lane >> 3) * 8192 + (lane & 7) * 16), "s"(ws + WS_MODG + 4096 + dcb * 128) : "memory");
    }
    const char* abase = ws + WS_ACT + (size_t)mt * 8 * 229376 + (size_t)G * 7 * 2048;
    const __amdgpu_buffer_rsrc_t ar0 = mkrs(abase + (size_t)wm * 2 * 229376 + (2 * par + wc) * 2048, 2048), ar1 = mkrs(abase + (size_t)(wm * 2 + 1) * 229376 + (2 * par + wc) * 2048, 2048);
    const __amdgpu_buffer_rsrc_t ar2 = mkrs(abase + (size_t)wid * 229376 + (4 + par) * 2048, 2048), ar3 = mkrs(abase + (size_t)wid * 229376 + 6 * 2048 + par * 1024, 1024);
    const unsigned avo = (lg >> 1) * 1024 + (m16 + 32 * (lg & 1)) * 16;
#pragma unroll
    for (int m = 0; m < 4; ++m) {
        SB0();
        st16wt(m < 2 ? ar0 : ar1, avo, (m & 1) * 256, act4(0, 1, m, 0));
        if (m == 1) stage(3, 3, true, 2);
        if (m == 0) {
            WAITL0(); BAR();
            const unsigned a = lds0 + 131072 + wid * 3072 + lane * 16;
            st16wt(ar2, xvo, 0, *(const LAS u32x4*)(size_t)a);
            st16wt(ar2, xvo, 1024, *(const LAS u32x4*)(size_t)(a + 1024));
            st16wt(ar3, xvo, 0, *(const LAS u32x4*)(size_t)(a + 2048));
        }
    }
    const int drl = 16 * lg + (qq >> 1) + 8 * (qq & 1);
    const unsigned dxa = dwm * 8192 + (lg >> 1) * 1024 + (li + 32 * (lg & 1)) * 16;
    const unsigned wak = 16384 + drl * 256 + (((ks * 4 + dwn) ^ ((qq >> 1) | ((qq & 1) << 1) | ((lg & 1) << 2))) * 32) + (li & 3) * 8;
    const unsigned wah = 16384 + drl * 128 + ((dwn ^ ((qq & 1) | ((lg & 1) << 1))) * 32) + (li & 3) * 8;
    asm volatile("s_waitcnt vmcnt(3)" : "+v"(gp) :: "memory");
#pragma unroll
    for (int j = 0; j < 4; ++j) gp[j] = bfr(sum_hi3(gp[j]));
    if (lane < 8) *(LAS f32x4*)(size_t)(lds0 + 155648 + wid * 128 + lane * 16) = gp;
    f32x4 gbv = ldsf4(lds0 + 155648 + wid * 128 + (dwn & 1) * 64 + lg * 16);
    asm volatile("s_waitcnt vmcnt(0) lgkmcnt(0)" : "+v"(gbv) :: "memory");
    BAR();
    if (tid >= 448 && tid < 456) __hip_atomic_store(cnt + (128 + mt * 8 + lane) * 32 + nt, 1u, __ATOMIC_RELAXED, __HIP_MEMORY_SCOPE_AGENT);
#pragma unroll
    for (int i = 0; i < 2; ++i) { const char* wb = wb0 + (4 + i) * dwstep; const unsigned lb = lds0 + 131072 + i * 16384 + wid * 1024; glds16(wb, wvf, lb); glds16(wb + 65536, wvf, lb + 8192); }
    if (wid == 7) {
#pragma nounroll
        for (unsigned spins = 0; spins < SPIN_MAX; ++spins) {
            if (__all(__hip_atomic_load(cnt + (128 + (dmt >> 1) * 8 + dnt) * 32 + (lane & 31), __ATOMIC_RELAXED, __HIP_MEMORY_SCOPE_AGENT) == 1u)) break;
            __builtin_amdgcn_s_sleep(POLL_SLEEP);
        }
    }
    BAR();
#pragma unroll
    for (int i = 0; i < 4; ++i) stage(i, i, true, 1);
    WAITV(6); BAR();
    f32x16 kp = {}, snd = {}, e0, e1, e2, e3, e4, e5;
    asm volatile("" : "=v"(e0), "=v"(e1), "=v"(e2), "=v"(e3), "=v"(e4), "=v"(e5));
    const char* ex0 = dxs + 4 * xstep; const char* ex1 = ex0 + 2 * 229376; const char* ew0 = wb0 + 6 * dwstep; const char* ew1 = ew0 + 65536;
    unsigned esr = 32768, esf = 0;
#define EOUT "+{v[0:15]}"(kp), "+{v[16:31]}"(snd), "+{v[32:47]}"(e0), "+{v[48:63]}"(e1), "+{v[64:79]}"(e2), "+{v[80:95]}"(e3), "+{v[96:111]}"(e4), "+{v[112:127]}"(e5), "+{s49}"(esr), "+{s50}"(esf)
#define EIN "{v128}"(lds0 + dxa), "{v129}"(lds0 + wak), "{v130}"(lds0 + wah)
#define ECLOB "memory", "scc", "v131", "v132", "v133"
    asm volatile(
    "v_mov_b32 v131, v128\n\t"
    "v_mov_b32 v132, v129\n\t"
    "v_xor_b32 v133, 0x80, v129\n\t"
    "ds_read_b64_tr_b16 v[64:65], v132 offset:0\n\t"
    "ds_read_b64_tr_b16 v[66:67], v132 offset:512\n\t"
    "ds_read_b128 v[32:35], v131 offset:0\n\t"
    "ds_read_b128 v[36:39], v131 offset:256\n\t"
    "ds_read_b128 v[40:43], v131 offset:4096\n\t"
    "ds_read_b128 v[44:47], v131 offset:4352\n\t"
    "ds_read_b64_tr_b16 v[72:73], v133 offset:0\n\t"
    "ds_read_b64_tr_b16 v[74:75], v133 offset:512\n\t"
    "ds_read_b64_tr_b16 v[68:69], v132 offset:1024\n\t"
    "ds_read_b64_tr_b16 v[70:71], v132 offset:1536\n\t"
    "ds_read_b128 v[48:51], v131 offset:2048\n\t"
    "ds_read_b128 v[52:55], v131 offset:2304\n\t"
    "ds_read_b128 v[56:59], v131 offset:6144\n\t"
    "ds_read_b128 v[60:63], v131 offset:6400\n\t"
    "ds_read_b64_tr_b16 v[76:77], v133 offset:1024\n\t"
    "ds_read_b64_tr_b16 v[78:79], v133 offset:1536\n\t"
    "v_add_u32 v131, s49, v128\n\t"
    "v_add_u32 v132, s49, v129\n\t"
    "v_xor_b32 v133, 0x80, v132\n\t"
    "s_waitcnt vmcnt(4) lgkmcnt(0)\n\t"
    "s_barrier\n\t"
    "s_add_u32 m0, s50, s48\n\t"
    "v_mfma_f32_16x16x32_bf16 v[0:3], v[64:67], v[32:35], v[0:3]\n\t"
    "ds_read_b64_tr_b16 v[112:113], v132 offset:0\n\t"
    "ds_read_b64_tr_b16 v[114:115], v132 offset:512\n\t"
    "v_mfma_f32_16x16x32_bf16 v[4:7], v[64:67], v[36:39], v[4:7]\n\t"
    "ds_read_b128 v[80:83], v131 offset:0\n\t"
    "v_mfma_f32_16x16x32_bf16 v[8:11], v[64:67], v[40:43], v[8:11]\n\t"
    "ds_read_b128 v[84:87], v131 offset:256\n\t"
    "ds_read_b128 v[88:91], v131 offset:4096\n\t"
    "v_mfma_f32_16x16x32_bf16 v[12:15], v[64:67], v[44:47], v[12:15]\n\t"
    "ds_read_b128 v[92:95], v131 offset:4352\n\t"
    "v_mfma_f32_16x16x32_bf16 v[28:31], v[72:75], v[44:47], v[28:31]\n\t"
    "ds_read_b64_tr_b16 v[120:121], v133 offset:0\n\t"
    "ds_read_b64_tr_b16 v[122:123], v133 offset:512\n\t"
    "v_mfma_f32_16x16x32_bf16 v[24:27], v[72:75], v[40:43], v[24:27]\n\t"
    "ds_read_b64_tr_b16 v[116:117], v132 offset:1024\n\t"
    "v_mfma_f32_16x16x32_bf16 v[20:23], v[72:75], v[36:39], v[20:23]\n\t"
    "ds_read_b64_tr_b16 v[118:119], v132 offset:1536\n\t"
    "ds_read_b128 v[96:99], v131 offset:2048\n\t"
    "v_mfma_f32_16x16x32_bf16 v[16:19], v[72:75], v[32:35], v[16:19]\n\t"
    "ds_read_b128 v[100:103], v131 offset:2304\n\t"
    "v_mfma_f32_16x16x32_bf16 v[0:3], v[68:71], v[48:51], v[0:3]\n\t"
    "ds_read_b128 v[104:107], v131 offset:6144\n\t"
    "ds_read_b128 v[108:111], v131 offset:6400\n\t"
    "v_mfma_f32_16x16x32_bf16 v[4:7], v[68:71], v[52:55], v[4:7]\n\t"
    "ds_read_b64_tr_b16 v[124:125], v133 offset:1024\n\t"
    "v_mfma_f32_16x16x32_bf16 v[8:11], v[68:71], v[56:59], v[8:11]\n\t"
    "ds_read_b64_tr_b16 v[126:127], v133 offset:1536\n\t"
    "v_mfma_f32_16x16x32_bf16 v[12:15], v[68:71], v[60:63], v[12:15]\n\t"
    "global_load_lds_dwordx4 v134, s[40:41] sc1\n\t"
    "s_add_u32 s40, s40, s52\n\t"
    "s_addc_u32 s41, s41, s53\n\t"
    "s_add_u32 m0, m0, 0x2000\n\t"
    "v_mfma_f32_16x16x32_bf16 v[28:31], v[76:79], v[60:63], v[28:31]\n\t"
    "global_load_lds_dwordx4 v134, s[42:43] sc1\n\t"
    "s_add_u32 s42, s42, s52\n\t"
    "s_addc_u32 s43, s43, s53\n\t"
    "s_add_u32 m0, m0, 0x2000\n\t"
    "v_mfma_f32_16x16x32_bf16 v[24:27], v[76:79], v[56:59], v[24:27]\n\t"
    "v_mfma_f32_16x16x32_bf16 v[20:23], v[76:79], v[52:55], v[20:23]\n\t"
    "v_mfma_f32_16x16x32_bf16 v[16:19], v[76:79], v[48:51], v[16:19]\n\t"
    "s_mov_b32 s50, s49\n\t"
    "s_add_u32 s49, s49, 0x8000\n\t"
    "s_and_b32 s49, s49, 0x1ffff\n\t"
    "v_add_u32 v131, s49, v128\n\t"
    "v_add_u32 v132, s49, v129\n\t"
    "v_xor_b32 v133, 0x80, v132\n\t"
    "s_waitcnt vmcnt(4) lgkmcnt(0)\n\t"
    "s_barrier\n\t"
    "s_add_u32 m0, s50, s48\n\t"
    "v_mfma_f32_16x16x32_bf16 v[0:3], v[112:115], v[80:83], v[0:3]\n\t"
    "ds_read_b64_tr_b16 v[64:65], v132 offset:0\n\t"
    "ds_read_b64_tr_b16 v[66:67], v132 offset:512\n\t"
    "v_mfma_f32_16x16x32_bf16 v[4:7], v[112:115], v[84:87], v[4:7]\n\t"
    "ds_read_b128 v[32:35], v131 offset:0\n\t"
    "v_mfma_f32_16x16x32_bf16 v[8:11], v[112:115], v[88:91], v[8:11]\n\t"
    "ds_read_b128 v[36:39], v131 offset:256\n\t"
    "ds_read_b128 v[40:43], v131 offset:4096\n\t"
    "v_mfma_f32_16x16x32_bf16 v[12:15], v[112:115], v[92:95], v[12:15]\n\t"
    "ds_read_b128 v[44:47], v131 offset:4352\n\t"
    "v_mfma_f32_16x16x32_bf16 v[28:31], v[120:123], v[92:95], v[28:31]\n\t"
    "ds_read_b64_tr_b16 v[72:73], v133 offset:0\n\t"
    "ds_read_b64_tr_b16 v[74:75], v133 offset:512\n\t"
    "v_mfma_f32_16x16x32_bf16 v[24:27], v[120:123], v[88:91], v[24:27]\n\t"
    "ds_read_b64_tr_b16 v[68:69], v132 offset:1024\n\t"
    "v_mfma_f32_16x16x32_bf16 v[20:23], v[120:123], v[84:87], v[20:23]\n\t"
    "ds_read_b64_tr_b16 v[70:71], v132 offset:1536\n\t"
    "ds_read_b128 v[48:51], v131 offset:2048\n\t"
    "v_mfma_f32_16x16x32_bf16 v[16:19], v[120:123], v[80:83], v[16:19]\n\t"
    "ds_read_b128 v[52:55], v131 offset:2304\n\t"
    "v_mfma_f32_16x16x32_bf16 v[0:3], v[116:119], v[96:99], v[0:3]\n\t"
    "ds_read_b128 v[56:59], v131 offset:6144\n\t"
    "ds_read_b128 v[60:63], v131 offset:6400\n\t"
    "v_mfma_f32_16x16x32_bf16 v[4:7], v[116:119], v[100:103], v[4:7]\n\t"
    "ds_read_b64_tr_b16 v[76:77], v133 offset:1024\n\t"
    "v_mfma_f32_16x16x32_bf16 v[8:11], v[116:119], v[104:107], v[8:11]\n\t"
    "ds_read_b64_tr_b16 v[78:79], v133 offset:1536\n\t"
    "v_mfma_f32_16x16x32_bf16 v[12:15], v[116:119], v[108:111], v[12:15]\n\t"
    "global_load_lds_dwordx4 v134, s[40:41] sc1\n\t"
    "s_add_u32 s40, s40, s52\n\t"
    "s_addc_u32 s41, s41, s53\n\t"
    "s_add_u32 m0, m0, 0x2000\n\t"
    "v_mfma_f32_16x16x32_bf16 v[28:31], v[124:127], v[108:111], v[28:31]\n\t"
    "global_load_lds_dwordx4 v134, s[42:43] sc1\n\t"
    "s_add_u32 s42, s42, s52\n\t"
    "s_addc_u32 s43, s43, s53\n\t"
    "s_add_u32 m0, m0, 0x2000\n\t"
    "v_mfma_f32_16x16x32_bf16 v[24:27], v[124:127], v[104:107], v[24:27]\n\t"
    "v_mfma_f32_16x16x32_bf16 v[20:23], v[124:127], v[100:103], v[20:23]\n\t"
    "v_mfma_f32_16x16x32_bf16 v[16:19], v[124:127], v[96:99], v[16:19]\n\t"
    "s_mov_b32 s50, s49\n\t"
    "s_add_u32 s49, s49, 0x8000\n\t"
    "s_and_b32 s49, s49, 0x1ffff\n\t"
    "v_add_u32 v131, s49, v128\n\t"
    "v_add_u32 v132, s49, v129\n\t"
    "v_xor_b32 v133, 0x80, v132\n\t"
    "s_waitcnt vmcnt(4) lgkmcnt(0)\n\t"
    "s_barrier\n\t"
    "s_add_u32 m0, s50, s48\n\t"
    "v_mfma_f32_16x16x32_bf16 v[0:3], v[64:67], v[32:35], v[0:3]\n\t"
    "ds_read_b64_tr_b16 v[112:113], v132 offset:0\n\t"
    "ds_read_b64_tr_b16 v[114:115], v132 offset:512\n\t"
    "v_mfma_f32_16x16x32_bf16 v[4:7], v[64:67], v[36:39], v[4:7]\n\t"
    "ds_read_b128 v[80:83], v131 offset:0\n\t"
    "v_mfma_f32_16x16x32_bf16 v[8:11], v[64:67], v[40:43], v[8:11]\n\t"
    "ds_read_b128 v[84:87], v131 offset:256\n\t"
    "ds_read_b128 v[88:91], v131 offset:4096\n\t"
    "v_mfma_f32_16x16x32_bf16 v[12:15], v[64:67], v[44:47], v[12:15]\n\t"
    "ds_read_b128 v[92:95], v131 offset:4352\n\t"
    "v_mfma_f32_16x16x32_bf16 v[28:31], v[72:75], v[44:47], v[28:31]\n\t"
    "ds_read_b64_tr_b16 v[120:121], v133 offset:0\n\t"
    "ds_read_b64_tr_b16 v[122:123], v133 offset:512\n\t"
    "v_mfma_f32_16x16x32_bf16 v[24:27], v[72:75], v[40:43], v[24:27]\n\t"
    "ds_read_b64_tr_b16 v[116:117], v132 offset:1024\n\t"
    "v_mfma_f32_16x16x32_bf16 v[20:23], v[72:75], v[36:39], v[20:23]\n\t"
    "ds_read_b64_tr_b16 v[118:119], v132 offset:1536\n\t"
    "ds_read_b128 v[96:99], v131 offset:2048\n\t"
    "v_mfma_f32_16x16x32_bf16 v[16:19], v[72:75], v[32:35], v[16:19]\n\t"
    "ds_read_b128 v[100:103], v131 offset:2304\n\t"
    "v_mfma_f32_16x16x32_bf16 v[0:3], v[68:71], v[48:51], v[0:3]\n\t"
    "ds_read_b128 v[104:107], v131 offset:6144\n\t"
    "ds_read_b128 v[108:111], v131 offset:6400\n\t"
    "v_mfma_f32_16x16x32_bf16 v[4:7], v[68:71], v[52:55], v[4:7]\n\t"
    "ds_read_b64_tr_b16 v[124:125], v133 offset:1024\n\t"
    "v_mfma_f32_16x16x32_bf16 v[8:11], v[68:71], v[56:59], v[8:11]\n\t"
    "ds_read_b64_tr_b16 v[126:127], v133 offset:1536\n\t"
    "v_mfma_f32_16x16x32_bf16 v[12:15], v[68:71], v[60:63], v[12:15]\n\t"
    "global_load_lds_dwordx4 v134, s[40:41] sc1\n\t"
    "s_add_u32 s40, s40, s52\n\t"
    "s_addc_u32 s41, s41, s53\n\t"
    "s_add_u32 m0, m0, 0x2000\n\t"
    "v_mfma_f32_16x16x32_bf16 v[28:31], v[76:79], v[60:63], v[28:31]\n\t"
    "global_load_lds_dwordx4 v134, s[42:43] sc1\n\t"
    "s_add_u32 s42, s42, s52\n\t"
    "s_addc_u32 s43, s43, s53\n\t"
    "s_add_u32 m0, m0, 0x2000\n\t"
    "v_mfma_f32_16x16x32_bf16 v[24:27], v[76:79], v[56:59], v[24:27]\n\t"
    "global_load_lds_dwordx4 v135, s[44:45]\n\t"
    "s_add_u32 s44, s44, s54\n\t"
    "s_addc_u32 s45, s45, s55\n\t"
    "s_add_u32 m0, m0, 0x2000\n\t"
    "v_mfma_f32_16x16x32_bf16 v[20:23], v[76:79], v[52:55], v[20:23]\n\t"
    "global_load_lds_dwordx4 v135, s[46:47]\n\t"
    "s_add_u32 s46, s46, s54\n\t"
    "s_addc_u32 s47, s47, s55\n\t"
    "v_mfma_f32_16x16x32_bf16 v[16:19], v[76:79], v[48:51], v[16:19]\n\t"
    "s_mov_b32 s50, s49\n\t"
    "s_add_u32 s49, s49, 0x8000\n\t"
    "s_and_b32 s49, s49, 0x1ffff\n\t"
    "v_add_u32 v131, s49, v128\n\t"
    "v_add_u32 v132, 114688, v129\n\t"
    "v_xor_b32 v133, 0x80, v132\n\t"
    "s_waitcnt vmcnt(6) lgkmcnt(0)\n\t"
    "s_barrier\n\t"
    "s_add_u32 m0, s50, s48\n\t"
    "v_mfma_f32_16x16x32_bf16 v[0:3], v[112:115], v[80:83], v[0:3]\n\t"
    "ds_read_b64_tr_b16 v[64:65], v132 offset:0\n\t"
    "ds_read_b64_tr_b16 v[66:67], v132 offset:512\n\t"
    "v_mfma_f32_16x16x32_bf16 v[4:7], v[112:115], v[84:87], v[4:7]\n\t"
    "ds_read_b128 v[32:35], v131 offset:0\n\t"
    "v_mfma_f32_16x16x32_bf16 v[8:11], v[112:115], v[88:91], v[8:11]\n\t"
    "ds_read_b128 v[36:39], v131 offset:256\n\t"
    "ds_read_b128 v[40:43], v131 offset:4096\n\t"
    "v_mfma_f32_16x16x32_bf16 v[12:15], v[112:115], v[92:95], v[12:15]\n\t"
    "ds_read_b128 v[44:47], v131 offset:4352\n\t"
    "v_mfma_f32_16x16x32_bf16 v[28:31], v[120:123], v[92:95], v[28:31]\n\t"
    "ds_read_b64_tr_b16 v[72:73], v133 offset:0\n\t"
    "ds_read_b64_tr_b16 v[74:75], v133 offset:512\n\t"
    "v_mfma_f32_16x16x32_bf16 v[24:27], v[120:123], v[88:91], v[24:27]\n\t"
    "ds_read_b64_tr_b16 v[68:69], v132 offset:1024\n\t"
    "v_mfma_f32_16x16x32_bf16 v[20:23], v[120:123], v[84:87], v[20:23]\n\t"
    "ds_read_b64_tr_b16 v[70:71], v132 offset:1536\n\t"
    "ds_read_b128 v[48:51], v131 offset:2048\n\t"
    "v_mfma_f32_16x16x32_bf16 v[16:19], v[120:123], v[80:83], v[16:19]\n\t"
    "ds_read_b128 v[52:55], v131 offset:2304\n\t"
    "v_mfma_f32_16x16x32_bf16 v[0:3], v[116:119], v[96:99], v[0:3]\n\t"
    "ds_read_b128 v[56:59], v131 offset:6144\n\t"
    "ds_read_b128 v[60:63], v131 offset:6400\n\t"
    "v_mfma_f32_16x16x32_bf16 v[4:7], v[116:119], v[100:103], v[4:7]\n\t"
    "ds_read_b64_tr_b16 v[76:77], v133 offset:1024\n\t"
    "v_mfma_f32_16x16x32_bf16 v[8:11], v[116:119], v[104:107], v[8:11]\n\t"
    "ds_read_b64_tr_b16 v[78:79], v133 offset:1536\n\t"
    "v_mfma_f32_16x16x32_bf16 v[12:15], v[116:119], v[108:111], v[12:15]\n\t"
    "global_load_lds_dwordx4 v134, s[40:41] sc1\n\t"
    "s_add_u32 s40, s40, s52\n\t"
    "s_addc_u32 s41, s41, s53\n\t"
    "s_add_u32 m0, m0, 0x2000\n\t"
    "v_mfma_f32_16x16x32_bf16 v[28:31], v[124:127], v[108:111], v[28:31]\n\t"
    "global_load_lds_dwordx4 v134, s[42:43] sc1\n\t"
    "s_add_u32 s42, s42, s52\n\t"
    "s_addc_u32 s43, s43, s53\n\t"
    "s_add_u32 m0, m0, 0x2000\n\t"
    "v_mfma_f32_16x16x32_bf16 v[24:27], v[124:127], v[104:107], v[24:27]\n\t"
    "global_load_lds_dwordx4 v135, s[44:45]\n\t"
    "s_add_u32 s44, s44, s54\n\t"
    "s_addc_u32 s45, s45, s55\n\t"
    "s_add_u32 m0, m0, 0x2000\n\t"
    "v_mfma_f32_16x16x32_bf16 v[20:23], v[124:127], v[100:103], v[20:23]\n\t"
    "global_load_lds_dwordx4 v135, s[46:47]\n\t"
    "s_add_u32 s46, s46, s54\n\t"
    "s_addc_u32 s47, s47, s55\n\t"
    "v_mfma_f32_16x16x32_bf16 v[16:19], v[124:127], v[96:99], v[16:19]\n\t"
    "s_mov_b32 s50, s49\n\t"
    "s_add_u32 s49, s49, 0x8000\n\t"
    "s_and_b32 s49, s49, 0x1ffff\n\t"
    "v_add_u32 v131, s49, v128\n\t"
    "v_add_u32 v132, 131072, v129\n\t"
    "v_xor_b32 v133, 0x80, v132\n\t"
    "s_waitcnt vmcnt(8) lgkmcnt(0)\n\t"
    "s_barrier\n\t"
    "s_add_u32 m0, s50, s48\n\t"
    "v_mfma_f32_16x16x32_bf16 v[0:3], v[64:67], v[32:35], v[0:3]\n\t"
    "ds_read_b64_tr_b16 v[112:113], v132 offset:0\n\t"
    "ds_read_b64_tr_b16 v[114:115], v132 offset:512\n\t"
    "v_mfma_f32_16x16x32_bf16 v[4:7], v[64:67], v[36:39], v[4:7]\n\t"
    "ds_read_b128 v[80:83], v131 offset:0\n\t"
    "v_mfma_f32_16x16x32_bf16 v[8:11], v[64:67], v[40:43], v[8:11]\n\t"
    "ds_read_b128 v[84:87], v131 offset:256\n\t"
    "ds_read_b128 v[88:91], v131 offset:4096\n\t"
    "v_mfma_f32_16x16x32_bf16 v[12:15], v[64:67], v[44:47], v[12:15]\n\t"
    "ds_read_b128 v[92:95], v131 offset:4352\n\t"
    "v_mfma_f32_16x16x32_bf16 v[28:31], v[72:75], v[44:47], v[28:31]\n\t"
    "ds_read_b64_tr_b16 v[120:121], v133 offset:0\n\t"
    "ds_read_b64_tr_b16 v[122:123], v133 offset:512\n\t"
    "v_mfma_f32_16x16x32_bf16 v[24:27], v[72:75], v[40:43], v[24:27]\n\t"
    "ds_read_b64_tr_b16 v[116:117], v132 offset:1024\n\t"
    "v_mfma_f32_16x16x32_bf16 v[20:23], v[72:75], v[36:39], v[20:23]\n\t"
    "ds_read_b64_tr_b16 v[118:119], v132 offset:1536\n\t"
    "ds_read_b128 v[96:99], v131 offset:2048\n\t"
    "v_mfma_f32_16x16x32_bf16 v[16:19], v[72:75], v[32:35], v[16:19]\n\t"
    "ds_read_b128 v[100:103], v131 offset:2304\n\t"
    "v_mfma_f32_16x16x32_bf16 v[0:3], v[68:71], v[48:51], v[0:3]\n\t"
    "ds_read_b128 v[104:107], v131 offset:6144\n\t"
    "ds_read_b128 v[108:111], v131 offset:6400\n\t"
    "v_mfma_f32_16x16x32_bf16 v[4:7], v[68:71], v[52:55], v[4:7]\n\t"
    "ds_read_b64_tr_b16 v[124:125], v133 offset:1024\n\t"
    "v_mfma_f32_16x16x32_bf16 v[8:11], v[68:71], v[56:59], v[8:11]\n\t"
    "ds_read_b64_tr_b16 v[126:127], v133 offset:1536\n\t"
    "v_mfma_f32_16x16x32_bf16 v[12:15], v[68:71], v[60:63], v[12:15]\n\t"
    "global_load_lds_dwordx4 v134, s[40:41] sc1\n\t"
    "s_add_u32 s40, s40, s52\n\t"
    "s_addc_u32 s41, s41, s53\n\t"
    "s_add_u32 m0, m0, 0x2000\n\t"
    "v_mfma_f32_16x16x32_bf16 v[28:31], v[76:79], v[60:63], v[28:31]\n\t"
    "global_load_lds_dwordx4 v134, s[42:43] sc1\n\t"
    "s_add_u32 s42, s42, s52\n\t"
    "s_addc_u32 s43, s43, s53\n\t"
    "s_add_u32 m0, m0, 0x2000\n\t"
    "v_mfma_f32_16x16x32_bf16 v[24:27], v[76:79], v[56:59], v[24:27]\n\t"
    "global_load_lds_dwordx4 v135, s[44:45]\n\t"
    "s_add_u32 s44, s44, s54\n\t"
    "s_addc_u32 s45, s45, s55\n\t"
    "s_add_u32 m0, m0, 0x2000\n\t"
    "v_mfma_f32_16x16x32_bf16 v[20:23], v[76:79], v[52:55], v[20:23]\n\t"
    "global_load_lds_dwordx4 v135, s[46:47]\n\t"
    "s_add_u32 s46, s46, s54\n\t"
    "s_addc_u32 s47, s47, s55\n\t"
    "v_mfma_f32_16x16x32_bf16 v[16:19], v[76:79], v[48:51], v[16:19]\n\t"
    "s_mov_b32 s50, s49\n\t"
    "s_add_u32 s49, s49, 0x8000\n\t"
    "s_and_b32 s49, s49, 0x1ffff\n\t"
    "v_add_u32 v131, s49, v128\n\t"
    "v_add_u32 v132, s49, v129\n\t"
    "v_xor_b32 v133, 0x80, v132\n\t"
    "s_waitcnt vmcnt(8) lgkmcnt(0)\n\t"
    "s_barrier\n\t"
    "s_add_u32 m0, s50, s48\n\t"
    "v_mfma_f32_16x16x32_bf16 v[0:3], v[112:115], v[80:83], v[0:3]\n\t"
    "ds_read_b64_tr_b16 v[64:65], v132 offset:0\n\t"
    "ds_read_b64_tr_b16 v[66:67], v132 offset:512\n\t"
    "v_mfma_f32_16x16x32_bf16 v[4:7], v[112:115], v[84:87], v[4:7]\n\t"
    "ds_read_b128 v[32:35], v131 offset:0\n\t"
    "v_mfma_f32_16x16x32_bf16 v[8:11], v[112:115], v[88:91], v[8:11]\n\t"
    "ds_read_b128 v[36:39], v131 offset:256\n\t"
    "ds_read_b128 v[40:43], v131 offset:4096\n\t"
    "v_mfma_f32_16x16x32_bf16 v[12:15], v[112:115], v[92:95], v[12:15]\n\t"
    "ds_read_b128 v[44:47], v131 offset:4352\n\t"
    "v_mfma_f32_16x16x32_bf16 v[28:31], v[120:123], v[92:95], v[28:31]\n\t"
    "ds_read_b64_tr_b16 v[72:73], v133 offset:0\n\t"
    "ds_read_b64_tr_b16 v[74:75], v133 offset:512\n\t"
    "v_mfma_f32_16x16x32_bf16 v[24:27], v[120:123], v[88:91], v[24:27]\n\t"
    "ds_read_b64_tr_b16 v[68:69], v132 offset:1024\n\t"
    "v_mfma_f32_16x16x32_bf16 v[20:23], v[120:123], v[84:87], v[20:23]\n\t"
    "ds_read_b64_tr_b16 v[70:71], v132 offset:1536\n\t"
    "ds_read_b128 v[48:51], v131 offset:2048\n\t"
    "v_mfma_f32_16x16x32_bf16 v[16:19], v[120:123], v[80:83], v[16:19]\n\t"
    "ds_read_b128 v[52:55], v131 offset:2304\n\t"
    "v_mfma_f32_16x16x32_bf16 v[0:3], v[116:119], v[96:99], v[0:3]\n\t"
    "ds_read_b128 v[56:59], v131 offset:6144\n\t"
    "ds_read_b128 v[60:63], v131 offset:6400\n\t"
    "v_mfma_f32_16x16x32_bf16 v[4:7], v[116:119], v[100:103], v[4:7]\n\t"
    "ds_read_b64_tr_b16 v[76:77], v133 offset:1024\n\t"
    "v_mfma_f32_16x16x32_bf16 v[8:11], v[116:119], v[104:107], v[8:11]\n\t"
    "ds_read_b64_tr_b16 v[78:79], v133 offset:1536\n\t"
    "v_mfma_f32_16x16x32_bf16 v[12:15], v[116:119], v[108:111], v[12:15]\n\t"
    "global_load_lds_dwordx4 v134, s[40:41] sc1\n\t"
    "s_add_u32 s40, s40, s52\n\t"
    "s_addc_u32 s41, s41, s53\n\t"
    "s_add_u32 m0, m0, 0x2000\n\t"
    "v_mfma_f32_16x16x32_bf16 v[28:31], v[124:127], v[108:111], v[28:31]\n\t"
    "global_load_lds_dwordx4 v134, s[42:43] sc1\n\t"
    "s_add_u32 s42, s42, s52\n\t"
    "s_addc_u32 s43, s43, s53\n\t"
    "s_add_u32 m0, m0, 0x2000\n\t"
    "v_mfma_f32_16x16x32_bf16 v[24:27], v[124:127], v[104:107], v[24:27]\n\t"
    "global_load_lds_dwordx4 v135, s[44:45]\n\t"
    "s_add_u32 s44, s44, s54\n\t"
    "s_addc_u32 s45, s45, s55\n\t"
    "s_add_u32 m0, m0, 0x2000\n\t"
    "v_mfma_f32_16x16x32_bf16 v[20:23], v[124:127], v[100:103], v[20:23]\n\t"
    "global_load_lds_dwordx4 v135, s[46:47]\n\t"
    "s_add_u32 s46, s46, s54\n\t"
    "s_addc_u32 s47, s47, s55\n\t"
    "v_mfma_f32_16x16x32_bf16 v[16:19], v[124:127], v[96:99], v[16:19]\n\t"
    "s_mov_b32 s50, s49\n\t"
    "s_add_u32 s49, s49, 0x8000\n\t"
    "s_and_b32 s49, s49, 0x1ffff\n\t"
    "s_mov_b32 s51, 8\n\t"
    "2:\n\t"
    "v_add_u32 v131, s49, v128\n\t"
    "v_add_u32 v132, s49, v129\n\t"
    "v_xor_b32 v133, 0x80, v132\n\t"
    "s_waitcnt vmcnt(8) lgkmcnt(0)\n\t"
    "s_barrier\n\t"
    "s_add_u32 m0, s50, s48\n\t"
    "v_mfma_f32_16x16x32_bf16 v[0:3], v[64:67], v[32:35], v[0:3]\n\t"
    "ds_read_b64_tr_b16 v[112:113], v132 offset:0\n\t"
    "ds_read_b64_tr_b16 v[114:115], v132 offset:512\n\t"
    "v_mfma_f32_16x16x32_bf16 v[4:7], v[64:67], v[36:39], v[4:7]\n\t"
    "ds_read_b128 v[80:83], v131 offset:0\n\t"
    "v_mfma_f32_16x16x32_bf16 v[8:11], v[64:67], v[40:43], v[8:11]\n\t"
    "ds_read_b128 v[84:87], v131 offset:256\n\t"
    "ds_read_b128 v[88:91], v131 offset:4096\n\t"
    "v_mfma_f32_16x16x32_bf16 v[12:15], v[64:67], v[44:47], v[12:15]\n\t"
    "ds_read_b128 v[92:95], v131 offset:4352\n\t"
    "v_mfma_f32_16x16x32_bf16 v[28:31], v[72:75], v[44:47], v[28:31]\n\t"
    "ds_read_b64_tr_b16 v[120:121], v133 offset:0\n\t"
    "ds_read_b64_tr_b16 v[122:123], v133 offset:512\n\t"
    "v_mfma_f32_16x16x32_bf16 v[24:27], v[72:75], v[40:43], v[24:27]\n\t"
    "ds_read_b64_tr_b16 v[116:117], v132 offset:1024\n\t"
    "v_mfma_f32_16x16x32_bf16 v[20:23], v[72:75], v[36:39], v[20:23]\n\t"
    "ds_read_b64_tr_b16 v[118:119], v132 offset:1536\n\t"
    "ds_read_b128 v[96:99], v131 offset:2048\n\t"
    "v_mfma_f32_16x16x32_bf16 v[16:19], v[72:75], v[32:35], v[16:19]\n\t"
    "ds_read_b128 v[100:103], v131 offset:2304\n\t"
    "v_mfma_f32_16x16x32_bf16 v[0:3], v[68:71], v[48:51], v[0:3]\n\t"
    "ds_read_b128 v[104:107], v131 offset:6144\n\t"
    "ds_read_b128 v[108:111], v131 offset:6400\n\t"
    "v_mfma_f32_16x16x32_bf16 v[4:7], v[68:71], v[52:55], v[4:7]\n\t"
    "ds_read_b64_tr_b16 v[124:125], v133 offset:1024\n\t"
    "v_mfma_f32_16x16x32_bf16 v[8:11], v[68:71], v[56:59], v[8:11]\n\t"
    "ds_read_b64_tr_b16 v[126:127], v133 offset:1536\n\t"
    "v_mfma_f32_16x16x32_bf16 v[12:15], v[68:71], v[60:63], v[12:15]\n\t"
    "global_load_lds_dwordx4 v134, s[40:41] sc1\n\t"
    "s_add_u32 s40, s40, s52\n\t"
    "s_addc_u32 s41, s41, s53\n\t"
    "s_add_u32 m0, m0, 0x2000\n\t"
    "v_mfma_f32_16x16x32_bf16 v[28:31], v[76:79], v[60:63], v[28:31]\n\t"
    "global_load_lds_dwordx4 v134, s[42:43] sc1\n\t"
    "s_add_u32 s42, s42, s52\n\t"
    "s_addc_u32 s43, s43, s53\n\t"
    "s_add_u32 m0, m0, 0x2000\n\t"
    "v_mfma_f32_16x16x32_bf16 v[24:27], v[76:79], v[56:59], v[24:27]\n\t"
    "global_load_lds_dwordx4 v135, s[44:45]\n\t"
    "s_add_u32 s44, s44, s54\n\t"
    "s_addc_u32 s45, s45, s55\n\t"
    "s_add_u32 m0, m0, 0x2000\n\t"
    "v_mfma_f32_16x16x32_bf16 v[20:23], v[76:79], v[52:55], v[20:23]\n\t"
    "global_load_lds_dwordx4 v135, s[46:47]\n\t"
    "s_add_u32 s46, s46, s54\n\t"
    "s_addc_u32 s47, s47, s55\n\t"
    "v_mfma_f32_16x16x32_bf16 v[16:19], v[76:79], v[48:51], v[16:19]\n\t"
    "s_mov_b32 s50, s49\n\t"
    "s_add_u32 s49, s49, 0x8000\n\t"
    "s_and_b32 s49, s49, 0x1ffff\n\t"
    "v_add_u32 v131, s49, v128\n\t"
    "v_add_u32 v132, s49, v129\n\t"
    "v_xor_b32 v133, 0x80, v132\n\t"
    "s_waitcnt vmcnt(8) lgkmcnt(0)\n\t"
    "s_barrier\n\t"
    "s_add_u32 m0, s50, s48\n\t"
    "v_mfma_f32_16x16x32_bf16 v[0:3], v[112:115], v[80:83], v[0:3]\n\t"
    "ds_read_b64_tr_b16 v[64:65], v132 offset:0\n\t"
    "ds_read_b64_tr_b16 v[66:67], v132 offset:512\n\t"
    "v_mfma_f32_16x16x32_bf16 v[4:7], v[112:115], v[84:87], v[4:7]\n\t"
    "ds_read_b128 v[32:35], v131 offset:0\n\t"
    "v_mfma_f32_16x16x32_bf16 v[8:11], v[112:115], v[88:91], v[8:11]\n\t"
    "ds_read_b128 v[36:39], v131 offset:256\n\t"
    "ds_read_b128 v[40:43], v131 offset:4096\n\t"
    "v_mfma_f32_16x16x32_bf16 v[12:15], v[112:115], v[92:95], v[12:15]\n\t"
    "ds_read_b128 v[44:47], v131 offset:4352\n\t"
    "v_mfma_f32_16x16x32_bf16 v[28:31], v[120:123], v[92:95], v[28:31]\n\t"
    "ds_read_b64_tr_b16 v[72:73], v133 offset:0\n\t"
    "ds_read_b64_tr_b16 v[74:75], v133 offset:512\n\t"
    "v_mfma_f32_16x16x32_bf16 v[24:27], v[120:123], v[88:91], v[24:27]\n\t"
    "ds_read_b64_tr_b16 v[68:69], v132 offset:1024\n\t"
    "v_mfma_f32_16x16x32_bf16 v[20:23], v[120:123], v[84:87], v[20:23]\n\t"
    "ds_read_b64_tr_b16 v[70:71], v132 offset:1536\n\t"
    "ds_read_b128 v[48:51], v131 offset:2048\n\t"
    "v_mfma_f32_16x16x32_bf16 v[16:19], v[120:123], v[80:83], v[16:19]\n\t"
    "ds_read_b128 v[52:55], v131 offset:2304\n\t"
    "v_mfma_f32_16x16x32_bf16 v[0:3], v[116:119], v[96:99], v[0:3]\n\t"
    "ds_read_b128 v[56:59], v131 offset:6144\n\t"
    "ds_read_b128 v[60:63], v131 offset:6400\n\t"
    "v_mfma_f32_16x16x32_bf16 v[4:7], v[116:119], v[100:103], v[4:7]\n\t"
    "ds_read_b64_tr_b16 v[76:77], v133 offset:1024\n\t"
    "v_mfma_f32_16x16x32_bf16 v[8:11], v[116:119], v[104:107], v[8:11]\n\t"
    "ds_read_b64_tr_b16 v[78:79], v133 offset:1536\n\t"
    "v_mfma_f32_16x16x32_bf16 v[12:15], v[116:119], v[108:111], v[12:15]\n\t"
    "global_load_lds_dwordx4 v134, s[40:41] sc1\n\t"
    "s_add_u32 s40, s40, s52\n\t"
    "s_addc_u32 s41, s41, s53\n\t"
    "s_add_u32 m0, m0, 0x2000\n\t"
    "v_mfma_f32_16x16x32_bf16 v[28:31], v[124:127], v[108:111], v[28:31]\n\t"
    "global_load_lds_dwordx4 v134, s[42:43] sc1\n\t"
    "s_add_u32 s42, s42, s52\n\t"
    "s_addc_u32 s43, s43, s53\n\t"
    "s_add_u32 m0, m0, 0x2000\n\t"
    "v_mfma_f32_16x16x32_bf16 v[24:27], v[124:127], v[104:107], v[24:27]\n\t"
    "global_load_lds_dwordx4 v135, s[44:45]\n\t"
    "s_add_u32 s44, s44, s54\n\t"
    "s_addc_u32 s45, s45, s55\n\t"
    "s_add_u32 m0, m0, 0x2000\n\t"
    "v_mfma_f32_16x16x32_bf16 v[20:23], v[124:127], v[100:103], v[20:23]\n\t"
    "global_load_lds_dwordx4 v135, s[46:47]\n\t"
    "s_add_u32 s46, s46, s54\n\t"
    "s_addc_u32 s47, s47, s55\n\t"
    "v_mfma_f32_16x16x32_bf16 v[16:19], v[124:127], v[96:99], v[16:19]\n\t"
    "s_mov_b32 s50, s49\n\t"
    "s_add_u32 s49, s49, 0x8000\n\t"
    "s_and_b32 s49, s49, 0x1ffff\n\t"
    "s_sub_u32 s51, s51, 1\n\t"
    "s_cmp_lg_u32 s51, 0\n\t"
    "s_cbranch_scc1 2b\n\t"
    "v_add_u32 v131, s49, v128\n\t"
    "v_add_u32 v132, s49, v129\n\t"
    "v_xor_b32 v133, 0x80, v132\n\t"
    "s_waitcnt vmcnt(8) lgkmcnt(0)\n\t"
    "s_barrier\n\t"
    "s_add_u32 m0, s50, s48\n\t"
    "v_mfma_f32_16x16x32_bf16 v[0:3], v[64:67], v[32:35], v[0:3]\n\t"
    "ds_read_b64_tr_b16 v[112:113], v132 offset:0\n\t"
    "ds_read_b64_tr_b16 v[114:115], v132 offset:512\n\t"
    "v_mfma_f32_16x16x32_bf16 v[4:7], v[64:67], v[36:39], v[4:7]\n\t"
    "ds_read_b128 v[80:83], v131 offset:0\n\t"
    "v_mfma_f32_16x16x32_bf16 v[8:11], v[64:67], v[40:43], v[8:11]\n\t"
    "ds_read_b128 v[84:87], v131 offset:256\n\t"
    "ds_read_b128 v[88:91], v131 offset:4096\n\t"
    "v_mfma_f32_16x16x32_bf16 v[12:15], v[64:67], v[44:47], v[12:15]\n\t"
    "ds_read_b128 v[92:95], v131 offset:4352\n\t"
    "v_mfma_f32_16x16x32_bf16 v[28:31], v[72:75], v[44:47], v[28:31]\n\t"
    "ds_read_b64_tr_b16 v[120:121], v133 offset:0\n\t"
    "ds_read_b64_tr_b16 v[122:123], v133 offset:512\n\t"
    "v_mfma_f32_16x16x32_bf16 v[24:27], v[72:75], v[40:43], v[24:27]\n\t"
    "ds_read_b64_tr_b16 v[116:117], v132 offset:1024\n\t"
    "v_mfma_f32_16x16x32_bf16 v[20:23], v[72:75], v[36:39], v[20:23]\n\t"
    "ds_read_b64_tr_b16 v[118:119], v132 offset:1536\n\t"
    "ds_read_b128 v[96:99], v131 offset:2048\n\t"
    "v_mfma_f32_16x16x32_bf16 v[16:19], v[72:75], v[32:35], v[16:19]\n\t"
    "ds_read_b128 v[100:103], v131 offset:2304\n\t"
    "v_mfma_f32_16x16x32_bf16 v[0:3], v[68:71], v[48:51], v[0:3]\n\t"
    "ds_read_b128 v[104:107], v131 offset:6144\n\t"
    "ds_read_b128 v[108:111], v131 offset:6400\n\t"
    "v_mfma_f32_16x16x32_bf16 v[4:7], v[68:71], v[52:55], v[4:7]\n\t"
    "ds_read_b64_tr_b16 v[124:125], v133 offset:1024\n\t"
    "v_mfma_f32_16x16x32_bf16 v[8:11], v[68:71], v[56:59], v[8:11]\n\t"
    "ds_read_b64_tr_b16 v[126:127], v133 offset:1536\n\t"
    "v_mfma_f32_16x16x32_bf16 v[12:15], v[68:71], v[60:63], v[12:15]\n\t"
    "global_load_lds_dwordx4 v134, s[40:41] sc1\n\t"
    "s_add_u32 s40, s40, s52\n\t"
    "s_addc_u32 s41, s41, s53\n\t"
    "s_add_u32 m0, m0, 0x2000\n\t"
    "v_mfma_f32_16x16x32_bf16 v[28:31], v[76:79], v[60:63], v[28:31]\n\t"
    "global_load_lds_dwordx4 v134, s[42:43] sc1\n\t"
    "s_add_u32 s42, s42, s52\n\t"
    "s_addc_u32 s43, s43, s53\n\t"
    "s_add_u32 m0, m0, 0x2000\n\t"
    "v_mfma_f32_16x16x32_bf16 v[24:27], v[76:79], v[56:59], v[24:27]\n\t"
    "global_load_lds_dwordx4 v136, s[44:45]\n\t"
    "s_add_u32 s44, s44, s54\n\t"
    "s_addc_u32 s45, s45, s55\n\t"
    "s_add_u32 m0, m0, 0x2000\n\t"
    "v_mfma_f32_16x16x32_bf16 v[20:23], v[76:79], v[52:55], v[20:23]\n\t"
    "v_mfma_f32_16x16x32_bf16 v[16:19], v[76:79], v[48:51], v[16:19]\n\t"
    "s_mov_b32 s50, s49\n\t"
    "s_add_u32 s49, s49, 0x8000\n\t"
    "s_and_b32 s49, s49, 0x1ffff\n\t"
    "v_add_u32 v131, s49, v128\n\t"
    "v_add_u32 v132, s49, v129\n\t"
    "v_xor_b32 v133, 0x80, v132\n\t"
    "s_waitcnt vmcnt(7) lgkmcnt(0)\n\t"
    "s_barrier\n\t"
    "s_add_u32 m0, s50, s48\n\t"
    "v_mfma_f32_16x16x32_bf16 v[0:3], v[112:115], v[80:83], v[0:3]\n\t"
    "ds_read_b64_tr_b16 v[64:65], v132 offset:0\n\t"
    "ds_read_b64_tr_b16 v[66:67], v132 offset:512\n\t"
    "v_mfma_f32_16x16x32_bf16 v[4:7], v[112:115], v[84:87], v[4:7]\n\t"
    "ds_read_b128 v[32:35], v131 offset:0\n\t"
    "v_mfma_f32_16x16x32_bf16 v[8:11], v[112:115], v[88:91], v[8:11]\n\t"
    "ds_read_b128 v[36:39], v131 offset:256\n\t"
    "ds_read_b128 v[40:43], v131 offset:4096\n\t"
    "v_mfma_f32_16x16x32_bf16 v[12:15], v[112:115], v[92:95], v[12:15]\n\t"
    "ds_read_b128 v[44:47], v131 offset:4352\n\t"
    "v_mfma_f32_16x16x32_bf16 v[28:31], v[120:123], v[92:95], v[28:31]\n\t"
    "ds_read_b64_tr_b16 v[72:73], v133 offset:0\n\t"
    "ds_read_b64_tr_b16 v[74:75], v133 offset:512\n\t"
    "v_mfma_f32_16x16x32_bf16 v[24:27], v[120:123], v[88:91], v[24:27]\n\t"
    "ds_read_b64_tr_b16 v[68:69], v132 offset:1024\n\t"
    "v_mfma_f32_16x16x32_bf16 v[20:23], v[120:123], v[84:87], v[20:23]\n\t"
    "ds_read_b64_tr_b16 v[70:71], v132 offset:1536\n\t"
    "ds_read_b128 v[48:51], v131 offset:2048\n\t"
    "v_mfma_f32_16x16x32_bf16 v[16:19], v[120:123], v[80:83], v[16:19]\n\t"
    "ds_read_b128 v[52:55], v131 offset:2304\n\t"
    "v_mfma_f32_16x16x32_bf16 v[0:3], v[116:119], v[96:99], v[0:3]\n\t"
    "ds_read_b128 v[56:59], v131 offset:6144\n\t"
    "ds_read_b128 v[60:63], v131 offset:6400\n\t"
    "v_mfma_f32_16x16x32_bf16 v[4:7], v[116:119], v[100:103], v[4:7]\n\t"
    "ds_read_b64_tr_b16 v[76:77], v133 offset:1024\n\t"
    "v_mfma_f32_16x16x32_bf16 v[8:11], v[116:119], v[104:107], v[8:11]\n\t"
    "ds_read_b64_tr_b16 v[78:79], v133 offset:1536\n\t"
    "v_mfma_f32_16x16x32_bf16 v[12:15], v[116:119], v[108:111], v[12:15]\n\t"
    "global_load_lds_dwordx4 v134, s[40:41] sc1\n\t"
    "s_add_u32 s40, s40, s52\n\t"
    "s_addc_u32 s41, s41, s53\n\t"
    "s_add_u32 m0, m0, 0x2000\n\t"
    "v_mfma_f32_16x16x32_bf16 v[28:31], v[124:127], v[108:111], v[28:31]\n\t"
    "global_load_lds_dwordx4 v134, s[42:43] sc1\n\t"
    "s_add_u32 s42, s42, s52\n\t"
    "s_addc_u32 s43, s43, s53\n\t"
    "s_add_u32 m0, m0, 0x2000\n\t"
    "v_mfma_f32_16x16x32_bf16 v[24:27], v[124:127], v[104:107], v[24:27]\n\t"
    "global_load_lds_dwordx4 v136, s[44:45]\n\t"
    "s_add_u32 s44, s44, s54\n\t"
    "s_addc_u32 s45, s45, s55\n\t"
    "s_add_u32 m0, m0, 0x2000\n\t"
    "v_mfma_f32_16x16x32_bf16 v[20:23], v[124:127], v[100:103], v[20:23]\n\t"
    "v_mfma_f32_16x16x32_bf16 v[16:19], v[124:127], v[96:99], v[16:19]\n\t"
    "s_mov_b32 s50, s49\n\t"
    "s_add_u32 s49, s49, 0x8000\n\t"
    "s_and_b32 s49, s49, 0x1ffff\n\t"
    "v_add_u32 v131, s49, v128\n\t"
    "v_add_u32 v132, s49, v129\n\t"
    "v_xor_b32 v133, 0x80, v132\n\t"
    "s_waitcnt vmcnt(6) lgkmcnt(0)\n\t"
    "s_barrier\n\t"
    "s_add_u32 m0, s50, s48\n\t"
    "v_mfma_f32_16x16x32_bf16 v[0:3], v[64:67], v[32:35], v[0:3]\n\t"
    "ds_read_b64_tr_b16 v[112:113], v132 offset:0\n\t"
    "ds_read_b64_tr_b16 v[114:115], v132 offset:512\n\t"
    "v_mfma_f32_16x16x32_bf16 v[4:7], v[64:67], v[36:39], v[4:7]\n\t"
    "ds_read_b128 v[80:83], v131 offset:0\n\t"
    "v_mfma_f32_16x16x32_bf16 v[8:11], v[64:67], v[40:43], v[8:11]\n\t"
    "ds_read_b128 v[84:87], v131 offset:256\n\t"
    "ds_read_b128 v[88:91], v131 offset:4096\n\t"
    "v_mfma_f32_16x16x32_bf16 v[12:15], v[64:67], v[44:47], v[12:15]\n\t"
    "ds_read_b128 v[92:95], v131 offset:4352\n\t"
    "v_mfma_f32_16x16x32_bf16 v[28:31], v[72:75], v[44:47], v[28:31]\n\t"
    "ds_read_b64_tr_b16 v[120:121], v133 offset:0\n\t"
    "ds_read_b64_tr_b16 v[122:123], v133 offset:512\n\t"
    "v_mfma_f32_16x16x32_bf16 v[24:27], v[72:75], v[40:43], v[24:27]\n\t"
    "ds_read_b64_tr_b16 v[116:117], v132 offset:1024\n\t"
    "v_mfma_f32_16x16x32_bf16 v[20:23], v[72:75], v[36:39], v[20:23]\n\t"
    "ds_read_b64_tr_b16 v[118:119], v132 offset:1536\n\t"
    "ds_read_b128 v[96:99], v131 offset:2048\n\t"
    "v_mfma_f32_16x16x32_bf16 v[16:19], v[72:75], v[32:35], v[16:19]\n\t"
    "ds_read_b128 v[100:103], v131 offset:2304\n\t"
    "v_mfma_f32_16x16x32_bf16 v[0:3], v[68:71], v[48:51], v[0:3]\n\t"
    "ds_read_b128 v[104:107], v131 offset:6144\n\t"
    "ds_read_b128 v[108:111], v131 offset:6400\n\t"
    "v_mfma_f32_16x16x32_bf16 v[4:7], v[68:71], v[52:55], v[4:7]\n\t"
    "ds_read_b64_tr_b16 v[124:125], v133 offset:1024\n\t"
    "v_mfma_f32_16x16x32_bf16 v[8:11], v[68:71], v[56:59], v[8:11]\n\t"
    "ds_read_b64_tr_b16 v[126:127], v133 offset:1536\n\t"
    "v_mfma_f32_16x16x32_bf16 v[12:15], v[68:71], v[60:63], v[12:15]\n\t"
    "global_load_lds_dwordx4 v134, s[40:41] sc1\n\t"
    "s_add_u32 s40, s40, s52\n\t"
    "s_addc_u32 s41, s41, s53\n\t"
    "s_add_u32 m0, m0, 0x2000\n\t"
    "v_mfma_f32_16x16x32_bf16 v[28:31], v[76:79], v[60:63], v[28:31]\n\t"
    "global_load_lds_dwordx4 v134, s[42:43] sc1\n\t"
    "s_add_u32 s42, s42, s52\n\t"
    "s_addc_u32 s43, s43, s53\n\t"
    "s_add_u32 m0, m0, 0x2000\n\t"
    "v_mfma_f32_16x16x32_bf16 v[24:27], v[76:79], v[56:59], v[24:27]\n\t"
    "global_load_lds_dwordx4 v136, s[44:45]\n\t"
    "s_add_u32 s44, s44, s54\n\t"
    "s_addc_u32 s45, s45, s55\n\t"
    "s_add_u32 m0, m0, 0x2000\n\t"
    "v_mfma_f32_16x16x32_bf16 v[20:23], v[76:79], v[52:55], v[20:23]\n\t"
    "v_mfma_f32_16x16x32_bf16 v[16:19], v[76:79], v[48:51], v[16:19]\n\t"
    "s_mov_b32 s50, s49\n\t"
    "s_add_u32 s49, s49, 0x8000\n\t"
    "s_and_b32 s49, s49, 0x1ffff\n\t"
    "v_add_u32 v131, s49, v128\n\t"
    "v_add_u32 v132, s49, v130\n\t"
    "s_waitcnt vmcnt(6) lgkmcnt(0)\n\t"
    "s_barrier\n\t"
    "s_add_u32 m0, s50, s48\n\t"
    "v_mfma_f32_16x16x32_bf16 v[0:3], v[112:115], v[80:83], v[0:3]\n\t"
    "ds_read_b64_tr_b16 v[64:65], v132 offset:0\n\t"
    "ds_read_b64_tr_b16 v[66:67], v132 offset:256\n\t"
    "v_mfma_f32_16x16x32_bf16 v[4:7], v[112:115], v[84:87], v[4:7]\n\t"
    "ds_read_b128 v[32:35], v131 offset:0\n\t"
    "v_mfma_f32_16x16x32_bf16 v[8:11], v[112:115], v[88:91], v[8:11]\n\t"
    "ds_read_b128 v[36:39], v131 offset:256\n\t"
    "ds_read_b128 v[40:43], v131 offset:4096\n\t"
    "v_mfma_f32_16x16x32_bf16 v[12:15], v[112:115], v[92:95], v[12:15]\n\t"
    "ds_read_b128 v[44:47], v131 offset:4352\n\t"
    "v_mfma_f32_16x16x32_bf16 v[28:31], v[120:123], v[92:95], v[28:31]\n\t"
    "ds_read_b64_tr_b16 v[68:69], v132 offset:512\n\t"
    "ds_read_b64_tr_b16 v[70:71], v132 offset:768\n\t"
    "v_mfma_f32_16x16x32_bf16 v[24:27], v[120:123], v[88:91], v[24:27]\n\t"
    "ds_read_b128 v[48:51], v131 offset:2048\n\t"
    "v_mfma_f32_16x16x32_bf16 v[20:23], v[120:123], v[84:87], v[20:23]\n\t"
    "ds_read_b128 v[52:55], v131 offset:2304\n\t"
    "ds_read_b128 v[56:59], v131 offset:6144\n\t"
    "v_mfma_f32_16x16x32_bf16 v[16:19], v[120:123], v[80:83], v[16:19]\n\t"
    "ds_read_b128 v[60:63], v131 offset:6400\n\t"
    "v_mfma_f32_16x16x32_bf16 v[0:3], v[116:119], v[96:99], v[0:3]\n\t"
    "v_mfma_f32_16x16x32_bf16 v[4:7], v[116:119], v[100:103], v[4:7]\n\t"
    "v_mfma_f32_16x16x32_bf16 v[8:11], v[116:119], v[104:107], v[8:11]\n\t"
    "v_mfma_f32_16x16x32_bf16 v[12:15], v[116:119], v[108:111], v[12:15]\n\t"
    "global_load_lds_dwordx4 v134, s[40:41] sc1\n\t"
    "s_add_u32 s40, s40, s52\n\t"
    "s_addc_u32 s41, s41, s53\n\t"
    "s_add_u32 m0, m0, 0x2000\n\t"
    "v_mfma_f32_16x16x32_bf16 v[28:31], v[124:127], v[108:111], v[28:31]\n\t"
    "global_load_lds_dwordx4 v134, s[42:43] sc1\n\t"
    "s_add_u32 s42, s42, s52\n\t"
    "s_addc_u32 s43, s43, s53\n\t"
    "s_add_u32 m0, m0, 0x2000\n\t"
    "v_mfma_f32_16x16x32_bf16 v[24:27], v[124:127], v[104:107], v[24:27]\n\t"
    "global_load_lds_dwordx4 v136, s[44:45]\n\t"
    "s_add_u32 s44, s44, s54\n\t"
    "s_addc_u32 s45, s45, s55\n\t"
    "s_add_u32 m0, m0, 0x2000\n\t"
    "v_mfma_f32_16x16x32_bf16 v[20:23], v[124:127], v[100:103], v[20:23]\n\t"
    "v_mfma_f32_16x16x32_bf16 v[16:19], v[124:127], v[96:99], v[16:19]\n\t"
    "s_mov_b32 s50, s49\n\t"
    "s_add_u32 s49, s49, 0x8000\n\t"
    "s_and_b32 s49, s49, 0x1ffff\n\t"
    "s_waitcnt lgkmcnt(0)\n\t"
    "s_nop 7\n\t"
        "" : EOUT, "+{s[40:41]}"(ex0), "+{s[42:43]}"(ex1), "+{s[44:45]}"(ew0), "+{s[46:47]}"(ew1)
        : EIN, "{v134}"(l16), "{v135}"(wvf), "{v136}"(wvh), "{s48}"(lds0 + wid * 1024), "{s[52:53]}"(xstep), "{s[54:55]}"(dwstep) : ECLOB, "s51");
    {
        const __amdgpu_buffer_rsrc_t rs = mkrs(ws + WS_XB + (size_t)me * 16384 + wid * 2048, 2048);
#pragma unroll
        for (int i = 0; i < 2; ++i) {
            u32x4 v;
#pragma unroll
            for (int j = 0; j < 4; ++j) v[j] = min(cvtpk(snd[8 * i + 2 * j], snd[8 * i + 2 * j + 1]), 0xfffffffeu);
            st16wt(rs, l16, i * 1024, v);
        }
    }
    asm volatile(
    "v_add_u32 v131, s49, v128\n\t"
    "v_add_u32 v132, s49, v130\n\t"
    "s_waitcnt vmcnt(8) lgkmcnt(0)\n\t"
    "s_barrier\n\t"
    "v_mfma_f32_16x16x32_bf16 v[0:3], v[64:67], v[32:35], v[0:3]\n\t"
    "ds_read_b64_tr_b16 v[112:113], v132 offset:0\n\t"
    "ds_read_b64_tr_b16 v[114:115], v132 offset:256\n\t"
    "v_mfma_f32_16x16x32_bf16 v[4:7], v[64:67], v[36:39], v[4:7]\n\t"
    "ds_read_b128 v[80:83], v131 offset:0\n\t"
    "ds_read_b128 v[84:87], v131 offset:256\n\t"
    "v_mfma_f32_16x16x32_bf16 v[8:11], v[64:67], v[40:43], v[8:11]\n\t"
    "ds_read_b128 v[88:91], v131 offset:4096\n\t"
    "ds_read_b128 v[92:95], v131 offset:4352\n\t"
    "v_mfma_f32_16x16x32_bf16 v[12:15], v[64:67], v[44:47], v[12:15]\n\t"
    "ds_read_b64_tr_b16 v[116:117], v132 offset:512\n\t"
    "ds_read_b64_tr_b16 v[118:119], v132 offset:768\n\t"
    "v_mfma_f32_16x16x32_bf16 v[0:3], v[68:71], v[48:51], v[0:3]\n\t"
    "ds_read_b128 v[96:99], v131 offset:2048\n\t"
    "ds_read_b128 v[100:103], v131 offset:2304\n\t"
    "v_mfma_f32_16x16x32_bf16 v[4:7], v[68:71], v[52:55], v[4:7]\n\t"
    "ds_read_b128 v[104:107], v131 offset:6144\n\t"
    "ds_read_b128 v[108:111], v131 offset:6400\n\t"
    "v_mfma_f32_16x16x32_bf16 v[8:11], v[68:71], v[56:59], v[8:11]\n\t"
    "v_mfma_f32_16x16x32_bf16 v[12:15], v[68:71], v[60:63], v[12:15]\n\t"
    "s_mov_b32 s50, s49\n\t"
    "s_add_u32 s49, s49, 0x8000\n\t"
    "s_and_b32 s49, s49, 0x1ffff\n\t"
    "s_waitcnt lgkmcnt(0)\n\t"
        "" : EOUT : EIN : ECLOB);
    const unsigned exb = lds0 + 2 * 32768 + wid * 2048;
    { const char* xp = ws + WS_X1 + ((size_t)(dmt * 4 + dwm * 2) * 64 + dcb * 2 + (dwn & 1)) * 1024; glds16c(xp, l16, exb); glds16c(xp + 65536, l16, exb + 1024); }
    asm volatile(
    "v_add_u32 v131, s49, v128\n\t"
    "v_add_u32 v132, s49, v130\n\t"
    "s_waitcnt vmcnt(7) lgkmcnt(0)\n\t"
    "s_barrier\n\t"
    "v_mfma_f32_16x16x32_bf16 v[0:3], v[112:115], v[80:83], v[0:3]\n\t"
    "ds_read_b64_tr_b16 v[64:65], v132 offset:0\n\t"
    "ds_read_b64_tr_b16 v[66:67], v132 offset:256\n\t"
    "v_mfma_f32_16x16x32_bf16 v[4:7], v[112:115], v[84:87], v[4:7]\n\t"
    "ds_read_b128 v[32:35], v131 offset:0\n\t"
    "ds_read_b128 v[36:39], v131 offset:256\n\t"
    "v_mfma_f32_16x16x32_bf16 v[8:11], v[112:115], v[88:91], v[8:11]\n\t"
    "ds_read_b128 v[40:43], v131 offset:4096\n\t"
    "ds_read_b128 v[44:47], v131 offset:4352\n\t"
    "v_mfma_f32_16x16x32_bf16 v[12:15], v[112:115], v[92:95], v[12:15]\n\t"
    "ds_read_b64_tr_b16 v[68:69], v132 offset:512\n\t"
    "ds_read_b64_tr_b16 v[70:71], v132 offset:768\n\t"
    "v_mfma_f32_16x16x32_bf16 v[0:3], v[116:119], v[96:99], v[0:3]\n\t"
    "ds_read_b128 v[48:51], v131 offset:2048\n\t"
    "ds_read_b128 v[52:55], v131 offset:2304\n\t"
    "v_mfma_f32_16x16x32_bf16 v[4:7], v[116:119], v[100:103], v[4:7]\n\t"
    "ds_read_b128 v[56:59], v131 offset:6144\n\t"
    "ds_read_b128 v[60:63], v131 offset:6400\n\t"
    "v_mfma_f32_16x16x32_bf16 v[8:11], v[116:119], v[104:107], v[8:11]\n\t"
    "v_mfma_f32_16x16x32_bf16 v[12:15], v[116:119], v[108:111], v[12:15]\n\t"
    "s_mov_b32 s50, s49\n\t"
    "s_add_u32 s49, s49, 0x8000\n\t"
    "s_and_b32 s49, s49, 0x1ffff\n\t"
    "v_add_u32 v131, s49, v128\n\t"
    "v_add_u32 v132, s49, v130\n\t"
    "s_waitcnt vmcnt(4) lgkmcnt(0)\n\t"
    "s_barrier\n\t"
    "v_mfma_f32_16x16x32_bf16 v[0:3], v[64:67], v[32:35], v[0:3]\n\t"
    "ds_read_b64_tr_b16 v[112:113], v132 offset:0\n\t"
    "ds_read_b64_tr_b16 v[114:115], v132 offset:256\n\t"
    "v_mfma_f32_16x16x32_bf16 v[4:7], v[64:67], v[36:39], v[4:7]\n\t"
    "ds_read_b128 v[80:83], v131 offset:0\n\t"
    "ds_read_b128 v[84:87], v131 offset:256\n\t"
    "v_mfma_f32_16x16x32_bf16 v[8:11], v[64:67], v[40:43], v[8:11]\n\t"
    "ds_read_b128 v[88:91], v131 offset:4096\n\t"
    "ds_read_b128 v[92:95], v131 offset:4352\n\t"
    "v_mfma_f32_16x16x32_bf16 v[12:15], v[64:67], v[44:47], v[12:15]\n\t"
    "ds_read_b64_tr_b16 v[116:117], v132 offset:512\n\t"
    "ds_read_b64_tr_b16 v[118:119], v132 offset:768\n\t"
    "v_mfma_f32_16x16x32_bf16 v[0:3], v[68:71], v[48:51], v[0:3]\n\t"
    "ds_read_b128 v[96:99], v131 offset:2048\n\t"
    "ds_read_b128 v[100:103], v131 offset:2304\n\t"
    "v_mfma_f32_16x16x32_bf16 v[4:7], v[68:71], v[52:55], v[4:7]\n\t"
    "ds_read_b128 v[104:107], v131 offset:6144\n\t"
    "ds_read_b128 v[108:111], v131 offset:6400\n\t"
    "v_mfma_f32_16x16x32_bf16 v[8:11], v[68:71], v[56:59], v[8:11]\n\t"
    "v_mfma_f32_16x16x32_bf16 v[12:15], v[68:71], v[60:63], v[12:15]\n\t"
    "s_mov_b32 s50, s49\n\t"
    "s_add_u32 s49, s49, 0x8000\n\t"
    "s_and_b32 s49, s49, 0x1ffff\n\t"
    "s_waitcnt lgkmcnt(0)\n\t"
        "" : EOUT : EIN : ECLOB);
    const __amdgpu_buffer_rsrc_t prs = mkrs(ws + WS_XB + (size_t)partner * 16384 + wid * 2048, 2048);
    u32x4 pv[2];
#pragma unroll
    for (int i = 0; i < 2; ++i) pv[i] = __builtin_amdgcn_raw_buffer_load_b128(prs, l16 + i * 1024, 0, 16);
    asm volatile(
    "s_waitcnt lgkmcnt(0)\n\t"
    "v_mfma_f32_16x16x32_bf16 v[0:3], v[112:115], v[80:83], v[0:3]\n\t"
    "v_mfma_f32_16x16x32_bf16 v[4:7], v[112:115], v[84:87], v[4:7]\n\t"
    "v_mfma_f32_16x16x32_bf16 v[8:11], v[112:115], v[88:91], v[8:11]\n\t"
    "v_mfma_f32_16x16x32_bf16 v[12:15], v[112:115], v[92:95], v[12:15]\n\t"
    "v_mfma_f32_16x16x32_bf16 v[0:3], v[116:119], v[96:99], v[0:3]\n\t"
    "v_mfma_f32_16x16x32_bf16 v[4:7], v[116:119], v[100:103], v[4:7]\n\t"
    "v_mfma_f32_16x16x32_bf16 v[8:11], v[116:119], v[104:107], v[8:11]\n\t"
    "v_mfma_f32_16x16x32_bf16 v[12:15], v[116:119], v[108:111], v[12:15]\n\t"
    "s_mov_b32 s50, s49\n\t"
    "s_add_u32 s49, s49, 0x8000\n\t"
    "s_and_b32 s49, s49, 0x1ffff\n\t"
    "s_nop 7\n\t"
        "" : EOUT : EIN : ECLOB);
    WAITV(2);
    f32x4 kq4[4];
#pragma unroll
    for (int i = 0; i < 4; ++i) {
        const u32x2 xv = *(const LAS u32x2*)(size_t)(exb + (i >> 1) * 1024 + l16 + (i & 1) * 8);
        kq4[i] = f32x4{bflo(xv[0]), bfhi(xv[0]), bflo(xv[1]), bfhi(xv[1])} + gbv * f32x4{kp[4 * i], kp[4 * i + 1], kp[4 * i + 2], kp[4 * i + 3]};
    }
    asm volatile("" : "+v"(kq4[0]), "+v"(kq4[1]), "+v"(kq4[2]), "+v"(kq4[3]));
#pragma nounroll
    for (unsigned spins = 0; spins < SPIN_MAX; ++spins) {
        unsigned mx = 0;
#pragma unroll
        for (int i = 0; i < 2; ++i) mx = max(max(max(pv[i][0], pv[i][1]), max(pv[i][2], pv[i][3])), mx);
        if (!__any(mx == 0xffffffffu)) break;
        asm volatile("" ::: "memory");
#pragma unroll
        for (int i = 0; i < 2; ++i) pv[i] = __builtin_amdgcn_raw_buffer_load_b128(prs, l16 + i * 1024, 0, 16);
    }
    const unsigned ob = lds0 + 3 * 32768;
#pragma unroll
    for (int i = 0; i < 4; ++i) {
        const unsigned p0 = pv[i >> 1][2 * (i & 1)], p1 = pv[i >> 1][2 * (i & 1) + 1];
        const f32x4 r = kq4[i] + gbv * f32x4{bflo(p0), bfhi(p0), bflo(p1), bfhi(p1)};
        *(LAS u32x2*)(size_t)(ob + (dwm * 64 + i * 16 + li) * 144 + dwn * 32 + lg * 8) = u32x2{cvtpk(r[0], r[1]), cvtpk(r[2], r[3])};
    }
    WAITL0(); BAR();
#pragma unroll
    for (int i = 0; i < 2; ++i) {
        const int r = i * 64 + (tid >> 3);
        __builtin_amdgcn_raw_buffer_store_b128(*(const LAS u32x4*)(size_t)(ob + r * 144 + (tid & 7) * 16), mkrs(out + (size_t)dmt * 262144 + dnt * 256 + ks * 128, 262144 - 1920),
                                               r * 2048 + (tid & 7) * 16, 0, OUT_AUX);
    }
}


extern "C" void kernel_launch(void* const* d_in, const int* in_sizes, int n_in, void* d_out, int out_size, void* d_ws, size_t ws_size, hipStream_t stream) {
    const char* x = (const char*)d_in[0]; const char* wq = (const char*)d_in[1]; const char* wk = (const char*)d_in[2]; const char* wv = (const char*)d_in[3];
    const char* wo = (const char*)d_in[4]; const char* wg = (const char*)d_in[6]; const char* wu = (const char*)d_in[7]; const char* wd = (const char*)d_in[8];
    const u16* c = (const u16*)d_in[9]; const u16* wmod = (const u16*)d_in[10];
    char* ws = (char*)d_ws;
    (void)hipFuncSetAttribute((const void*)k_main, hipFuncAttributeMaxDynamicSharedMemorySize, K3_LDS);
    k_main<<<256, 512, K3_LDS, stream>>>(wq, wk, wv, wg, wu, wo, wd, (const char*)wmod, (const char*)c, x, ws, (char*)d_out);
}
```
